```python
import torch

def _softmax(x, axis=-1):
    return torch.softmax(x, dim=axis)

def _gelu(x):
    return torch.nn.functional.gelu(x, approximate='tanh')

def _silu(x):
    return torch.nn.functional.silu(x)

def _sigmoid(x):
    return torch.sigmoid(x)

def _layernorm(x, axis=-1, eps=1e-05):
    m = x.mean(dim=axis, keepdim=True)
    v = x.var(dim=axis, keepdim=True, unbiased=False)
    return (x - m) / torch.sqrt(v + eps)

def _scatter_add(tgt, idx, src, axis=0):
    return tgt.clone().index_add_(axis, idx, src)

def _segment_sum(src, seg, n):
    out = torch.zeros((n,) + src.shape[1:], dtype=src.dtype)
    return out.index_add_(0, seg, src)

def _pad(x, pw):
    flat = tuple((v for pair in reversed(pw) for v in pair))
    return torch.nn.functional.pad(x, flat)
kernel_name = 'standalone__molecular_angular_painn__graph'

def setup_inputs():

    _w = lambda a: (lambda t: t.to(torch.bfloat16) if t.is_floating_point() else t)(torch.as_tensor(a))
    N, deg, D = (50000, 16, 64)
    n_rbf = max(16, 4)
    E = N * deg
    cutoff = 5.0
    freqs = (torch.arange(1, n_rbf + 1) * torch.pi / cutoff).to(float)
    return {'sv': _w((lambda *s: torch.randn(*s, dtype=torch.bfloat16) * 0.4)(N, 4, D) * 0.3), 'pos': _w((lambda *s: torch.randn(*s, dtype=torch.bfloat16) * 3.2)(N, 3) * 1.2), 'src': _w((lambda lo, hi, *s: torch.randint(lo, hi, s))(0, N, E).to(torch.int64)), 'dst': _w((lambda lo, hi, *s: torch.randint(lo, hi, s))(0, N, E).to(torch.int64)), 'freqs': _w(freqs), 'w_phi1': _w((lambda *s: torch.randn(*s, dtype=torch.bfloat16) * 3.2)(D, D) / D ** 0.5), 'w_phi2': _w((lambda *s: torch.randn(*s, dtype=torch.bfloat16) * 3.2)(D, 3 * D) / D ** 0.5), 'w_rbf': _w((lambda *s: torch.randn(*s, dtype=torch.bfloat16) * 3.2)(n_rbf, 3 * D) / n_rbf ** 0.5)}

def reference(sv, pos, src, dst, freqs, w_phi1, w_phi2, w_rbf):
    N, _, D = sv.shape
    E = src.shape[0]
    PI, cutoff = (3.141592653589793, 5.0)
    s, v = (sv[:, 0, :].clone(), sv[:, 1:, :].clone())
    r_vec = pos[src] - pos[dst]
    d = torch.sqrt((r_vec * r_vec).sum(-1) + 1e-08)
    r_hat = r_vec / d[:, None]
    rbf = torch.sin(d[:, None] * freqs[None, :]) / d[:, None]
    fcut = 0.5 * (torch.cos(PI * d / cutoff) + 1.0)
    fcut = torch.where(d < cutoff, fcut, 0.0 * d)
    Wf = rbf @ w_rbf * fcut[:, None]
    phi = (_silu(s @ w_phi1) @ w_phi2)[src]
    m = phi * Wf
    m_s, m_vv, m_vr = (m[:, :D].clone(), m[:, D:2 * D].clone(), m[:, 2 * D:].clone())
    ds = _segment_sum(m_s, dst, N)
    dv_e = m_vv[:, None, :] * v[src] + m_vr[:, None, :] * r_hat[:, :, None]
    dv = _segment_sum(dv_e.reshape(E, 3 * D), dst, N).reshape(N, 3, D)
    return sv + torch.concatenate([ds[:, None, :], dv], axis=1)
```

```cpp
#include <hip/hip_runtime.h>
#include <hip/hip_bf16.h>
#include <stdint.h>
#define EMU 0
typedef unsigned short u16;
__device__ __forceinline__ float bf2f(u16 x){ return __uint_as_float(((unsigned)x)<<16); }
__device__ __forceinline__ u16 f2bf(float f){ unsigned u=__float_as_uint(f); u += 0x7fffu + ((u>>16)&1u); return (u16)(u>>16); }
__device__ __forceinline__ float rb(float f){ return bf2f(f2bf(f)); }
#define R1(x) (x)
#define R2(x) (x)

__global__ void k_phi(const u16* sv, const u16* w1, const u16* w2, float* phi, int N){
  __shared__ float s[64], h[64];
  int n = blockIdx.x; int t = threadIdx.x;
  if (t<64) s[t]=bf2f(sv[(size_t)n*256+t]);
  __syncthreads();
  if (t<64){ float a=0; for(int k=0;k<64;k++) a+=s[k]*bf2f(w1[k*64+t]); a=R2(a); float y=a/(1.f+expf(-a)); h[t]=rb(y);}
  __syncthreads();
  float a=0; for(int k=0;k<64;k++) a+=h[k]*bf2f(w2[k*192+t]);
  phi[(size_t)n*192+t]=R2(a);
}
__global__ void k_edge(const u16* sv, const u16* pos, const int* src, const int* dst, const u16* freqs, const u16* wr, const float* phi, float* acc, int E){
  int e = blockIdx.x*4 + (threadIdx.x>>6); int c = threadIdx.x&63;
  if (e>=E) return;
  int s=src[e], d_=dst[e];
  float rx=R1(bf2f(pos[s*3])-bf2f(pos[d_*3])), ry=R1(bf2f(pos[s*3+1])-bf2f(pos[d_*3+1])), rz=R1(bf2f(pos[s*3+2])-bf2f(pos[d_*3+2]));
  float q=R1(R1(rx*rx)+R1(ry*ry)+R1(rz*rz)); q=R1(q+1e-8f);
  float d=R1(sqrtf(q));
  if (!(d<5.0f)) return;
  float hx=R1(rx/d), hy=R1(ry/d), hz=R1(rz/d);
  float fc=R1(0.5f*R1(R1(cosf(R1(R1(3.141592653589793f*d)/5.0f)))+1.0f));
  float w0=0,w1=0,w2=0;
  for(int k=0;k<16;k++){ float r=R1(R1(sinf(R1(d*bf2f(freqs[k]))))/d); w0+=r*bf2f(wr[k*192+c]); w1+=r*bf2f(wr[k*192+64+c]); w2+=r*bf2f(wr[k*192+128+c]); }
  w0=R2(R2(w0)*fc); w1=R2(R2(w1)*fc); w2=R2(R2(w2)*fc);
  float ms=R2(phi[(size_t)s*192+c]*w0), mvv=R2(phi[(size_t)s*192+64+c]*w1), mvr=R2(phi[(size_t)s*192+128+c]*w2);
  float* a=acc+(size_t)d_*256;
  atomicAdd(a+c, ms);
  atomicAdd(a+64+c,  R2(R2(mvv*bf2f(sv[(size_t)s*256+64+c]))+R2(mvr*hx)));
  atomicAdd(a+128+c, R2(R2(mvv*bf2f(sv[(size_t)s*256+128+c]))+R2(mvr*hy)));
  atomicAdd(a+192+c, R2(R2(mvv*bf2f(sv[(size_t)s*256+192+c]))+R2(mvr*hz)));
}
__global__ void k_fin(const u16* sv, const float* acc, u16* out, size_t n){
  size_t i=(size_t)blockIdx.x*256+threadIdx.x; if(i<n) out[i]=f2bf(bf2f(sv[i])+R2(acc[i]));
}
extern "C" void kernel_launch(void* const* d_in, const int* in_sizes, int n_in, void* d_out, int out_size, void* d_ws, size_t ws_size, hipStream_t stream){
  const u16* sv=(const u16*)d_in[0]; const u16* pos=(const u16*)d_in[1]; const int* src=(const int*)d_in[2]; const int* dst=(const int*)d_in[3];
  const u16* fr=(const u16*)d_in[4]; const u16* w1=(const u16*)d_in[5]; const u16* w2=(const u16*)d_in[6]; const u16* wr=(const u16*)d_in[7];
  int N=in_sizes[1]/3, E=in_sizes[2];
  float* acc=(float*)d_ws; float* phi=acc+(size_t)N*256;
  hipMemsetAsync(acc,0,(size_t)N*256*4,stream);
  k_phi<<<N,192,0,stream>>>(sv,w1,w2,phi,N);
  k_edge<<<(E+3)/4,256,0,stream>>>(sv,pos,src,dst,fr,wr,phi,acc,E);
  size_t n=(size_t)N*256;
  k_fin<<<(n+255)/256,256,0,stream>>>(sv,acc,(u16*)d_out,n);
}
```

```cpp
#include <hip/hip_runtime.h>
#include <stdint.h>

typedef unsigned short u16;
typedef __attribute__((ext_vector_type(8))) __bf16 bf16x8;
typedef __attribute__((ext_vector_type(2))) __bf16 bf16x2;
typedef __attribute__((ext_vector_type(4))) short s16x4;
typedef __attribute__((ext_vector_type(2))) unsigned short u16x2;
typedef __attribute__((ext_vector_type(2))) float f32x2;
typedef __attribute__((ext_vector_type(4))) float f32x4;
typedef __attribute__((ext_vector_type(16))) float f32x16;
typedef __attribute__((ext_vector_type(4))) unsigned u32x4;
typedef __attribute__((ext_vector_type(2))) unsigned u32x2;
typedef __attribute__((ext_vector_type(4))) int i32x4;
#define LDSP(T, off) ((__attribute__((address_space(3))) T*)(uintptr_t)(off))
#define DI __device__ __forceinline__

constexpr int CH = 4096;
constexpr int SEGQ = CH / 4 + 192;
constexpr int GN = 25;
constexpr int BNMAX = 8 * GN;
constexpr int NFRAG = 38;
constexpr int CAP = 128;
constexpr int NFW = 6;
constexpr int BQ = 192;
constexpr unsigned SENT = 0xFFFFFFFFu;
constexpr int L_W = 0;
constexpr int L_TB = NFRAG * 1024;
constexpr int TBS = GN * 512;
constexpr int L_LIST = L_TB + 8 * TBS;
constexpr int L_CNT = L_LIST + 8 * 2048;
constexpr int L_NB = L_CNT + 256;
constexpr int L_FR = L_NB + 64;
constexpr int L_DPOS = L_FR + 64;
constexpr int L_END = L_DPOS + 256 * 8;
constexpr size_t WS_T = 65536;
constexpr size_t WS_SEG = WS_T + 256 * 256 * 4;

DI float lo16(unsigned x) { return __uint_as_float(x << 16); }
DI float hi16(unsigned x) { return __uint_as_float(x & 0xffff0000u); }
DI unsigned pk2(float a, float b) { f32x2 v = {a, b}; return __builtin_bit_cast(unsigned, __builtin_convertvector(v, bf16x2)); }
DI float rb(float a) { return lo16(pk2(a, a)); }
DI f32x16 mfma(u32x4 a, u32x4 b, f32x16 c) {
  return __builtin_amdgcn_mfma_f32_32x32x16_bf16(__builtin_bit_cast(bf16x8, a), __builtin_bit_cast(bf16x8, b), c, 0, 0, 0);
}
DI unsigned wave_incl_scan(unsigned v, int lane) {
#pragma unroll
  for (int o = 1; o < 64; o <<= 1) { unsigned t = __shfl_up(v, o, 64); v += lane >= o ? t : 0u; }
  return v;
}
DI int chan_of(int t2, int i32) { return 32 * ((i32 >> 2) & 1) + 16 * t2 + 4 * (i32 >> 3) + (i32 & 3); }

__global__ __launch_bounds__(1024) void k_bin(const int* __restrict__ src, const int* __restrict__ dst, unsigned* __restrict__ T,
                                              u32x4* __restrict__ seg, const u16* __restrict__ w1, const u16* __restrict__ w2,
                                              const u16* __restrict__ wr, u32x4* __restrict__ wf, int E, int W, unsigned magicBN, int BN) {
  __shared__ __attribute__((aligned(16))) unsigned cnt[16 * 256];
  __shared__ u32x4 stage[SEGQ];
  __shared__ unsigned s_total;
  const int tid = threadIdx.x, lane = tid & 63, wave = tid >> 6;
  if ((int)blockIdx.x >= W) {
    const int idx = ((int)blockIdx.x - W) * 1024 + tid;
    if (idx >= NFRAG * 64) return;
    const int f = idx >> 6, i32 = lane & 31, h = lane >> 5;
    const u16* p; int s1, s2;
    if (f < 8) { p = w1 + (32 * h + 8 * (f & 3)) * 64 + 32 * (f >> 2) + i32; s1 = 64; s2 = 256; }
    else if (f < 32) { const int ct = (f - 8) >> 2; p = w2 + (16 * (f & 3) + 4 * h) * 192 + 64 * (ct >> 1) + chan_of(ct & 1, i32); s1 = 192; s2 = 8 * 192; }
    else { const int ct = f - 32; p = wr + 8 * h * 192 + 64 * (ct >> 1) + chan_of(ct & 1, i32); s1 = 192; s2 = 4 * 192; }
    unsigned v[8];
#pragma unroll
    for (int j = 0; j < 8; ++j) v[j] = p[(j & 3) * s1 + (j >> 2) * s2];
    wf[idx] = u32x4{v[0] | v[1] << 16, v[2] | v[3] << 16, v[4] | v[5] << 16, v[6] | v[7] << 16};
    return;
  }
  const int e0 = blockIdx.x * CH + tid * 4;
  i32x4 s4 = {0, 0, 0, 0}, d4 = {0, 0, 0, 0};
  if (e0 + 3 < E) { s4 = *(const i32x4*)(src + e0); d4 = *(const i32x4*)(dst + e0); }
  else {
#pragma unroll
    for (int k = 0; k < 4; ++k) if (e0 + k < E) { s4[k] = src[e0 + k]; d4[k] = dst[e0 + k]; }
  }
  *(u32x4*)&cnt[tid * 4] = u32x4{0, 0, 0, 0};
  stage[tid] = u32x4{SENT, SENT, SENT, SENT};
  if (tid < SEGQ - 1024) stage[1024 + tid] = u32x4{SENT, SENT, SENT, SENT};
  __syncthreads();
  unsigned bk[4], rec[4], rank[4];
#pragma unroll
  for (int k = 0; k < 4; ++k) {
    const unsigned b = __umulhi((unsigned)d4[k], magicBN);
    const unsigned dl = (unsigned)d4[k] - b * (unsigned)BN;
    bk[k] = wave * 256 + b;
    rec[k] = (unsigned)s4[k] | dl << 24;
    rank[k] = 0;
    if (e0 + k < E) rank[k] = atomicAdd(&cnt[bk[k]], 1u);
  }
  __syncthreads();
  if (wave == 0) {
    u32x4 c[16], tot = {0, 0, 0, 0};
#pragma unroll
    for (int w = 0; w < 16; ++w) { c[w] = *(const u32x4*)&cnt[w * 256 + 4 * lane]; tot += c[w]; }
    const u32x4 q = (tot + 3u) >> 2;
    const unsigned ls = q[0] + q[1] + q[2] + q[3];
    const unsigned incl = wave_incl_scan(ls, lane);
    u32x4 st; st[0] = incl - ls; st[1] = st[0] + q[0]; st[2] = st[1] + q[1]; st[3] = st[2] + q[2];
    *(u32x4*)&T[blockIdx.x * 256 + 4 * lane] = st | (q << 16);
    u32x4 run = st * 4u;
#pragma unroll
    for (int w = 0; w < 16; ++w) { *(u32x4*)&cnt[w * 256 + 4 * lane] = run; run += c[w]; }
    if (lane == 63) s_total = incl;
  }
  __syncthreads();
#pragma unroll
  for (int k = 0; k < 4; ++k) if (e0 + k < E) ((unsigned*)stage)[cnt[bk[k]] + rank[k]] = rec[k];
  __syncthreads();
  const int total = s_total;
  for (int i = tid; i < total; i += 1024) seg[(size_t)blockIdx.x * SEGQ + i] = stage[i];
}

DI void dma16(u32x4 rsrc, unsigned voff, unsigned ldsaddr) {
  asm volatile("s_mov_b32 m0, %2\n\ts_nop 0\n\tbuffer_load_dwordx4 %0, %1, 0 offen lds" ::"v"(voff), "s"(rsrc), "s"(ldsaddr) : "memory");
}
DI u32x2 ldpos(const char* pos, unsigned node, unsigned tot8) {
  const unsigned off = node * 6u;
  const unsigned a = min(off & ~3u, tot8);
  typedef __attribute__((aligned(2))) u32x2 u32x2u;
  const u32x2 v = *(const u32x2u*)(pos + a);
  const unsigned sh = (off - a) * 8u;
  return u32x2{__builtin_amdgcn_alignbit(v[1], v[0], sh), v[1] >> sh};
}
DI unsigned eqmask(unsigned a, unsigned b) {
  const u16x2 x = __builtin_bit_cast(u16x2, a ^ b);
  const u16x2 one = {1, 1};
  return __builtin_bit_cast(unsigned, (u16x2)(__builtin_elementwise_min(x, one) - one));
}
DI u32x4 pack8(const f32x16& x, int s) {
  return u32x4{pk2(x[8 * s], x[8 * s + 1]), pk2(x[8 * s + 2], x[8 * s + 3]), pk2(x[8 * s + 4], x[8 * s + 5]), pk2(x[8 * s + 6], x[8 * s + 7])};
}

__global__ __launch_bounds__(512, 2) void k_agg(const u16* __restrict__ sv, const char* __restrict__ pos, const u16* __restrict__ freqs,
                                                const unsigned* __restrict__ T, const u32x4* __restrict__ seg, const u32x4* __restrict__ wf,
                                                u16* __restrict__ out, int N, int W, int BN, int B, int bpx) {
  extern __shared__ __attribute__((aligned(16))) char lds_raw[];
  const int tid = threadIdx.x, lane0 = tid & 63;
  const int wave = __builtin_amdgcn_readfirstlane(tid >> 6);
  const int b = ((int)blockIdx.x & 7) * bpx + ((int)blockIdx.x >> 3);
  if (b >= B) return;
  const unsigned L0 = (unsigned)(uintptr_t)(__attribute__((address_space(3))) char*)lds_raw;
  const int node0 = b * BN;
  const int nb = min(BN, N - node0);
  const unsigned tot8 = (unsigned)N * 6u - 8u;
  const uint64_t svp = (uint64_t)sv, wfp = (uint64_t)wf;
  const u32x4 rs_sv = {(unsigned)svp, (unsigned)(svp >> 32), (unsigned)N * 512u, 0x00020000u};
  const __amdgpu_buffer_rsrc_t rb_sv = __builtin_amdgcn_make_buffer_rsrc((void*)sv, 0, N * 512, 0x00020000);
#define FRESH_LANE int lane = lane0; asm volatile("" : "+v"(lane)); const int l31 = lane & 31, h = lane >> 5; (void)l31; (void)h
  const unsigned TB = L0 + L_TB + wave * TBS, LIST = L0 + L_LIST + wave * 2048;

  if (wave < 2) {
    FRESH_LANE;
    const int ni = (nb + 1) >> 1;
    for (int i = wave; i < ni; i += 2) {
      const int r = 2 * i + h;
      dma16(rs_sv, (unsigned)(node0 + r) * 512u + 16u * (l31 ^ (r & 15)), L0 + L_TB + i * 1024);
    }
    const u32x4 rs_wf = {(unsigned)wfp, (unsigned)(wfp >> 32), NFRAG * 1024u, 0x00020000u};
    for (int i = wave; i < NFRAG; i += 2) dma16(rs_wf, i * 1024u + 16u * lane, L0 + L_W + i * 1024);
    if (lane < 8) *LDSP(unsigned, L0 + L_CNT + wave * 32 + lane * 4) = 0;
    if (wave == 0 && lane < 16) *LDSP(float, L0 + L_FR + lane * 4) = lo16(freqs[lane]);
  }

  f32x16 acc[4][2];
#pragma unroll
  for (int j = 0; j < 4; ++j)
#pragma unroll
    for (int t = 0; t < 2; ++t)
#pragma unroll
      for (int r = 0; r < 16; ++r) acc[j][t][r] = 0.f;

  int batch = 0, round = 0;
  bool first = true;
  while (true) {
    u32x4 cand[12];
#pragma unroll
    for (int c = 0; c < 12; ++c) cand[c] = u32x4{SENT, 0u, 0u, SENT};
    if (wave >= 2) {
      FRESH_LANE;
      const int fw = wave - 2;
      const int cpw = (W + NFW - 1) / NFW;
      const int cell = fw * cpw + lane;
      const unsigned t = (lane < cpw && cell < W) ? T[cell * 256 + b] : 0u;
      u32x2 dp[4];
#pragma unroll
      for (int i = 0; i < 4; ++i) dp[i] = ldpos(pos, (unsigned)min(node0 + 64 * i + lane, N - 1), tot8);
      if (lane < 8) *LDSP(unsigned, L0 + L_CNT + wave * 32 + lane * 4) = 0;
      const int q = t >> 16;
      const int incl = wave_incl_scan(q, lane), excl = incl - q;
      const int total = __builtin_amdgcn_readlane(incl, 63);
      const int win0 = batch * BQ;
      const int gq = cell * SEGQ + (int)(t & 0xffffu) - excl;
      const int hi = min(incl, win0 + BQ);
      for (int i = max(excl, win0); i < hi; ++i) *LDSP(int, LIST + (i - win0) * 4) = gq + i;
      if (lane == 0) *LDSP(int, L0 + L_NB + wave * 4) = (total + BQ - 1) / BQ;
      u32x4 r4[3];
#pragma unroll
      for (int s = 0; s < 3; ++s) {
        const int f = 64 * s + lane;
        r4[s] = u32x4{SENT, SENT, SENT, SENT};
        if (win0 + f < total) r4[s] = seg[*LDSP(int, LIST + f * 4)];
      }
#pragma unroll
      for (int i = 0; i < 4; ++i) *LDSP(u32x2, L0 + L_DPOS + (64 * i + lane) * 8) = dp[i];
      u32x2 ps[12];
#pragma unroll
      for (int c = 0; c < 12; ++c) {
        const unsigned rec = r4[c >> 2][c & 3];
        ps[c] = ldpos(pos, rec == SENT ? 0u : (rec & 0xffffffu), tot8);
      }
#pragma unroll
      for (int c = 0; c < 12; ++c) {
        const unsigned rec = r4[c >> 2][c & 3], dl = rec >> 24;
        const u32x2 pd = *LDSP(u32x2, L0 + L_DPOS + dl * 8);
        const unsigned xy = pk2(lo16(ps[c][0]) - lo16(pd[0]), hi16(ps[c][0]) - hi16(pd[0]));
        const float z = rb(lo16(ps[c][1]) - lo16(pd[1]));
        const float x = lo16(xy), y = hi16(xy);
        const unsigned sq = pk2(x * x, y * y);
        const float q2 = rb(rb(lo16(sq) + hi16(sq) + rb(z * z)) + 1e-8f);
        const unsigned zd = pk2(z, __builtin_amdgcn_sqrtf(q2));
        unsigned rank = SENT;
        if ((rec != SENT) & (hi16(zd) < 5.0f))
          rank = __hip_atomic_fetch_add(LDSP(unsigned, L0 + L_CNT + wave * 32 + (dl / GN) * 4), 1u, __ATOMIC_RELAXED, __HIP_MEMORY_SCOPE_WORKGROUP);
        cand[c] = u32x4{rec, xy, zd, rank};
      }
    } else {
      asm volatile("s_waitcnt vmcnt(0)" ::: "memory");
    }
    __syncthreads();
    int maxtot = 0, nbatch = 0, cnt;
    {
    FRESH_LANE;
    const int cm = *LDSP(int, L0 + L_CNT + lane * 4);
    int ci = cm;
#pragma unroll
    for (int o = 8; o < 64; o <<= 1) { const int t = __shfl_up(ci, o, 64); ci += lane >= o ? t : 0; }
#pragma unroll
    for (int g = 0; g < 8; ++g) maxtot = max(maxtot, __builtin_amdgcn_readlane(ci, 56 + g));
    {
      const int v = *LDSP(int, L0 + L_NB + (lane & 7) * 4);
#pragma unroll
      for (int g = 2; g < 8; ++g) nbatch = max(nbatch, __builtin_amdgcn_readlane(v, g));
    }
    cnt = min(max(__builtin_amdgcn_readlane(ci, 56 + wave) - CAP * round, 0), CAP);
    if (wave >= 2) {
      const int ex = ci - cm - CAP * round;
#pragma unroll
      for (int c = 0; c < 12; ++c) {
        const unsigned g = (cand[c][0] >> 24) / GN;
        const unsigned p = __shfl(ex, 8 * wave + (g & 7), 64) + cand[c][3];
        if ((cand[c][3] != SENT) & (p < (unsigned)CAP)) *LDSP(u32x4, L0 + L_LIST + g * 2048 + p * 16) = cand[c];
      }
    }
    const unsigned key = (unsigned)(wave * GN + l31) & 15u;
    if (first) {
#pragma unroll
      for (int j = 0; j < 4; ++j)
#pragma unroll
        for (int t = 0; t < 2; ++t)
#pragma unroll
          for (int u = 0; u < 2; ++u) {
            const u32x4 v = *LDSP(u32x4, TB + l31 * 512 + 16 * ((unsigned)(8 * j + 4 * h + 2 * t + u) ^ key));
#pragma unroll
            for (int i = 0; i < 4; ++i) { acc[j][t][8 * u + 2 * i] = lo16(v[i]); acc[j][t][8 * u + 2 * i + 1] = hi16(v[i]); }
          }
      first = false;
    }
    }
    __syncthreads();
    const int ntile = (cnt + 31) >> 5;
    {
      FRESH_LANE;
      u32x4 raw[2];
#pragma unroll
      for (int i = 0; i < 2; ++i) raw[i] = *LDSP(u32x4, LIST + (64 * i + lane) * 16);
      asm volatile("s_waitcnt lgkmcnt(0)" : "+v"(raw[0]), "+v"(raw[1])::"memory");
#pragma unroll
      for (int i = 0; i < 2; ++i) {
        const int p = 64 * i + lane;
        const bool ok = p < cnt;
        const float d = ok ? hi16(raw[i][2]) : 1.0f;
        const float inv = __builtin_amdgcn_rcpf(d);
        const float a = rb(rb(3.141592653589793f * d) * 0.2f);
        const float f = ok ? 0.5f * rb(rb(__builtin_amdgcn_cosf(a * 0.15915494309189535f)) + 1.0f) : 0.f;
        const unsigned hxy = pk2(lo16(raw[i][1]) * inv, hi16(raw[i][1]) * inv);
        const float hz = rb(lo16(raw[i][2]) * inv);
        const unsigned fxy = ok ? pk2(f * lo16(hxy), f * hi16(hxy)) : 0u;
        const unsigned fzf = ok ? pk2(f * hz, f) : 0u;
        *LDSP(unsigned, LIST + p * 4) = ok ? (raw[i][0] & 0xffffffu) : (unsigned)N;
        *LDSP(u16, LIST + 512 + p * 2) = (u16)(__float_as_uint(d) >> 16);
        *LDSP(u16, LIST + 768 + p * 2) = ok ? (u16)(raw[i][0] >> 24) : (u16)0xffff;
        *LDSP(u16, LIST + 1024 + p * 2) = (u16)(fzf >> 16);
        *LDSP(u16, LIST + 1280 + p * 2) = (u16)fxy;
        *LDSP(u16, LIST + 1536 + p * 2) = (u16)(fxy >> 16);
        *LDSP(u16, LIST + 1792 + p * 2) = (u16)fzf;
      }
    }
    FRESH_LANE;
    const int pe = (l31 & 19) | ((l31 & 4) << 1) | ((l31 & 8) >> 1);
    const int drow = lane >> 3;
    const unsigned dchunk = 128u + 16u * ((lane & 7) ^ (drow & 2));
    const int q4 = (lane >> 2) & 3, pp = lane & 3, chh = (lane >> 4) & 1;
    const unsigned trb = TB + (8 * h + q4) * 128 + 64 * (pp & 1) + 16 * chh + 8 * (pp >> 1);
    const unsigned trb0 = trb + 32 * (q4 >> 1), trb1 = trb + 32 * ((q4 >> 1) ^ 1);
    const unsigned mypair = (unsigned)(wave * GN + l31) * 0x10001u;
    const unsigned WL = L0 + L_W + lane * 16;
    u32x4 sf[4];
    auto issue = [&](int t) {
      const unsigned se = *LDSP(unsigned, LIST + (32 * t + pe) * 4);
      unsigned sr[4];
#pragma unroll
      for (int i = 0; i < 4; ++i) sr[i] = *LDSP(unsigned, LIST + (32 * t + 8 * i + drow) * 4);
#pragma unroll
      for (int ks = 0; ks < 4; ++ks) sf[ks] = __builtin_amdgcn_raw_buffer_load_b128(rb_sv, se * 512u + 64u * h + 16u * ks, 0, 0);
      asm volatile("s_waitcnt lgkmcnt(0)" ::: "memory");
#pragma unroll
      for (int i = 0; i < 4; ++i)
#pragma unroll
        for (int k = 0; k < 3; ++k) dma16(rs_sv, sr[i] * 512u + dchunk + 128u * k, TB + k * 4096 + i * 1024);
    };
    if (ntile > 0) issue(0);
    for (int t = 0; t < ntile; ++t) {
      const int p0 = 32 * t;
      u32x4 rbf;
      {
        const float d = lo16(*LDSP(u16, LIST + 512 + (p0 + pe) * 2));
        const float inv = __builtin_amdgcn_rcpf(d);
        const f32x4 f0 = *LDSP(f32x4, L0 + L_FR + 32 * h), f1 = *LDSP(f32x4, L0 + L_FR + 32 * h + 16);
#pragma unroll
        for (int i = 0; i < 4; ++i) {
          const float fa = i < 2 ? f0[2 * i] : f1[2 * i - 4], fb = i < 2 ? f0[2 * i + 1] : f1[2 * i - 3];
          const unsigned x = pk2(d * fa, d * fb);
          float ra = lo16(x) * 0.15915494309189535f, rc = hi16(x) * 0.15915494309189535f;
          ra -= __builtin_rintf(ra); rc -= __builtin_rintf(rc);
          const unsigned sn = pk2(__builtin_amdgcn_sinf(ra), __builtin_amdgcn_sinf(rc));
          rbf[i] = pk2(lo16(sn) * inv, hi16(sn) * inv);
        }
      }
      u32x4 msk[2], S1[2];
#pragma unroll
      for (int s = 0; s < 2; ++s) {
        const u32x4 dl = *LDSP(u32x4, LIST + 768 + (p0 + 16 * s + 8 * h) * 2);
        const u32x4 f = *LDSP(u32x4, LIST + 1024 + (p0 + 16 * s + 8 * h) * 2);
#pragma unroll
        for (int i = 0; i < 4; ++i) { msk[s][i] = eqmask(dl[i], mypair); S1[s][i] = msk[s][i] & f[i]; }
      }
      asm volatile("s_waitcnt vmcnt(0)" ::: "memory");
      u32x4 hA[4];
#pragma unroll
      for (int ht = 0; ht < 2; ++ht) {
        f32x16 d1;
#pragma unroll
        for (int r = 0; r < 16; ++r) d1[r] = 0.f;
#pragma unroll
        for (int ks = 0; ks < 4; ++ks) d1 = mfma(*LDSP(u32x4, WL + (4 * ht + ks) * 1024), sf[ks], d1);
#pragma unroll
        for (int r = 0; r < 16; ++r) d1[r] = d1[r] * __builtin_amdgcn_rcpf(1.0f + __builtin_amdgcn_exp2f(d1[r] * -1.4426950408889634f));
        hA[2 * ht] = pack8(d1, 0); hA[2 * ht + 1] = pack8(d1, 1);
      }
      auto msg = [&](int ct) {
        f32x16 ph, w;
#pragma unroll
        for (int r = 0; r < 16; ++r) { ph[r] = 0.f; w[r] = 0.f; }
        w = mfma(rbf, *LDSP(u32x4, WL + (32 + ct) * 1024), w);
#pragma unroll
        for (int ks = 0; ks < 4; ++ks) ph = mfma(hA[ks], *LDSP(u32x4, WL + (8 + 4 * ct + ks) * 1024), ph);
        return ph * w;
      };
#pragma unroll
      for (int t2 = 0; t2 < 2; ++t2) {
        const f32x16 m = msg(2 + t2);
#pragma unroll
        for (int k = 0; k < 3; ++k) {
          u32x4 a[2];
#pragma unroll
          for (int bb = 0; bb < 4; ++bb) {
            const u32x2 v = __builtin_bit_cast(u32x2, __builtin_amdgcn_ds_read_tr16_b64_v4i16(
                LDSP(s16x4, (t2 ? trb1 : trb0) + k * 4096 + (16 * (bb >> 1) + 4 * (bb & 1)) * 128)));
            a[bb >> 1][2 * (bb & 1)] = pk2(m[4 * bb] * lo16(v[0]), m[4 * bb + 1] * hi16(v[0]));
            a[bb >> 1][2 * (bb & 1) + 1] = pk2(m[4 * bb + 2] * lo16(v[1]), m[4 * bb + 3] * hi16(v[1]));
          }
          acc[1 + k][t2] = mfma(a[0], S1[0], acc[1 + k][t2]);
          acc[1 + k][t2] = mfma(a[1], S1[1], acc[1 + k][t2]);
        }
      }
      if (t + 1 < ntile) issue(t + 1);
#pragma unroll
      for (int t2 = 0; t2 < 2; ++t2) {
        const f32x16 m = msg(t2);
        acc[0][t2] = mfma(pack8(m, 0), S1[0], acc[0][t2]);
        acc[0][t2] = mfma(pack8(m, 1), S1[1], acc[0][t2]);
      }
      u32x4 a2[2][2];
#pragma unroll
      for (int t2 = 0; t2 < 2; ++t2) { const f32x16 m = msg(4 + t2); a2[t2][0] = pack8(m, 0); a2[t2][1] = pack8(m, 1); }
#pragma unroll
      for (int k = 0; k < 3; ++k)
#pragma unroll
        for (int s = 0; s < 2; ++s) {
          const u32x4 sr = *LDSP(u32x4, LIST + 1280 + 256 * k + (p0 + 16 * s + 8 * h) * 2) & msk[s];
          acc[1 + k][0] = mfma(a2[0][s], sr, acc[1 + k][0]);
          acc[1 + k][1] = mfma(a2[1][s], sr, acc[1 + k][1]);
        }
    }
    if (maxtot > CAP * (round + 1)) ++round;
    else if (batch + 1 < nbatch) { ++batch; round = 0; }
    else break;
    __syncthreads();
  }
  {
    FRESH_LANE;
    const unsigned key = (unsigned)(wave * GN + l31) & 15u;
    if (l31 < GN) {
#pragma unroll
      for (int j = 0; j < 4; ++j)
#pragma unroll
        for (int t = 0; t < 2; ++t)
#pragma unroll
          for (int u = 0; u < 2; ++u) *LDSP(u32x4, TB + l31 * 512 + 16 * ((unsigned)(8 * j + 4 * h + 2 * t + u) ^ key)) = pack8(acc[j][t], u);
    }
    const int gw = min(nb - wave * GN, GN);
#pragma unroll
    for (int i = 0; i < (GN + 1) / 2; ++i) {
      const int r = 2 * i + h;
      if (r < gw) {
        const u32x4 v = *LDSP(u32x4, TB + i * 1024 + lane * 16);
        const int dl = wave * GN + r;
        *(u32x4*)((char*)out + (size_t)(node0 + dl) * 512 + 16 * (l31 ^ (dl & 15))) = v;
      }
    }
  }
}

extern "C" void kernel_launch(void* const* d_in, const int* in_sizes, int n_in, void* d_out, int out_size, void* d_ws, size_t ws_size,
                              hipStream_t stream) {
  const u16* sv = (const u16*)d_in[0];
  const char* pos = (const char*)d_in[1];
  const int* src = (const int*)d_in[2];
  const int* dst = (const int*)d_in[3];
  const u16* fr = (const u16*)d_in[4];
  const u16* w1 = (const u16*)d_in[5];
  const u16* w2 = (const u16*)d_in[6];
  const u16* wr = (const u16*)d_in[7];
  const int N = in_sizes[1] / 3, E = in_sizes[2];
  const int BN = (N + 255) / 256 < 2 ? 2 : (N + 255) / 256;
  const int B = (N + BN - 1) / BN, bpx = (B + 7) / 8;
  const int W = (E + CH - 1) / CH;
  const unsigned magicBN = (unsigned)(0x100000000ull / (unsigned)BN) + 1u;
  char* ws = (char*)d_ws;
  (void)hipFuncSetAttribute((const void*)k_agg, hipFuncAttributeMaxDynamicSharedMemorySize, L_END);
  k_bin<<<W + 3, 1024, 0, stream>>>(src, dst, (unsigned*)(ws + WS_T), (u32x4*)(ws + WS_SEG), w1, w2, wr, (u32x4*)ws, E, W, magicBN, BN);
  k_agg<<<8 * bpx, 512, L_END, stream>>>(sv, pos, fr, (const unsigned*)(ws + WS_T), (const u32x4*)(ws + WS_SEG), (const u32x4*)ws,
                                         (u16*)d_out, N, W, BN, B, bpx);
}
```

```cpp
#include <hip/hip_runtime.h>
#include <stdint.h>

typedef unsigned short u16;
typedef __attribute__((ext_vector_type(8))) __bf16 bf16x8;
typedef __attribute__((ext_vector_type(2))) __bf16 bf16x2;
typedef __attribute__((ext_vector_type(4))) short s16x4;
typedef __attribute__((ext_vector_type(2))) float f32x2;
typedef __attribute__((ext_vector_type(4))) float f32x4;
typedef __attribute__((ext_vector_type(16))) float f32x16;
typedef __attribute__((ext_vector_type(4))) unsigned u32x4;
typedef __attribute__((ext_vector_type(2))) unsigned u32x2;
typedef __attribute__((ext_vector_type(4))) int i32x4;
#define LDSP(T, off) ((__attribute__((address_space(3))) T*)(uintptr_t)(off))
#define DI __device__ __forceinline__
#define SB() __builtin_amdgcn_sched_barrier(0)

constexpr int CH = 4096;
constexpr int SEGQ = CH / 4 + 192;
constexpr int GN = 25;
constexpr int NFRAG = 30;
constexpr int NFW = 6;
constexpr int SUBC = 32;
constexpr int BQ = 192;
constexpr unsigned SENT = 0xFFFFFFFFu;
constexpr int L_W = 0;
constexpr int L_TB = NFRAG * 1024;
constexpr int TBS = GN * 512;
constexpr int L_LIST = L_TB + 8 * TBS;
constexpr int LISTS = NFW * SUBC * 16;
constexpr int L_CNT = L_LIST + 8 * LISTS;
constexpr int L_NB = L_CNT + 256;
constexpr int L_FR = L_NB + 64;
constexpr int L_DPOS = L_FR + 64;
constexpr int L_END = L_DPOS + 256 * 8;
constexpr int O_D = 768, O_DL = 1152, O_F = 1536, O_FX = 1920;
constexpr size_t WS_T = 65536;
constexpr size_t WS_SEG = WS_T + 256 * 256 * 4;
constexpr size_t WS_H = WS_SEG + (size_t)256 * SEGQ * 16;

#define STAMP(i)
DI float lo16(unsigned x) { return __uint_as_float(x << 16); }
DI float hi16(unsigned x) { return __uint_as_float(x & 0xffff0000u); }
DI unsigned pk2(float a, float b) { f32x2 v = {a, b}; return __builtin_bit_cast(unsigned, __builtin_convertvector(v, bf16x2)); }
DI float rb(float a) { return hi16(pk2(a, a)); }
DI f32x16 mfma(u32x4 a, u32x4 b, f32x16 c) {
  return __builtin_amdgcn_mfma_f32_32x32x16_bf16(__builtin_bit_cast(bf16x8, a), __builtin_bit_cast(bf16x8, b), c, 0, 0, 0);
}
DI f32x16 zero16() { f32x16 z;
#pragma unroll
  for (int r = 0; r < 16; ++r) z[r] = 0.f;
  return z; }
DI int wave_incl_scan(int v) {
  v += __builtin_amdgcn_update_dpp(0, v, 0x111, 0xf, 0xf, false);
  v += __builtin_amdgcn_update_dpp(0, v, 0x112, 0xf, 0xf, false);
  v += __builtin_amdgcn_update_dpp(0, v, 0x114, 0xf, 0xf, false);
  v += __builtin_amdgcn_update_dpp(0, v, 0x118, 0xf, 0xf, false);
  v += __builtin_amdgcn_update_dpp(0, v, 0x142, 0xa, 0xf, false);
  v += __builtin_amdgcn_update_dpp(0, v, 0x143, 0xc, 0xf, false);
  return v;
}
DI int chan_of(int t2, int i32) { return 32 * ((i32 >> 2) & 1) + 16 * t2 + 4 * (i32 >> 3) + (i32 & 3); }
DI u32x4 pack8(const f32x16& x, int s) {
  return u32x4{pk2(x[8 * s], x[8 * s + 1]), pk2(x[8 * s + 2], x[8 * s + 3]), pk2(x[8 * s + 4], x[8 * s + 5]), pk2(x[8 * s + 6], x[8 * s + 7])};
}
DI u32x2 trread(unsigned a) { return __builtin_bit_cast(u32x2, __builtin_amdgcn_ds_read_tr16_b64_v4i16(LDSP(s16x4, a))); }

constexpr int K1_CNT = 0, K1_STAGE = 8 * 256 * 4, K1_W1 = K1_STAGE + SEGQ * 16, K1_HT = K1_W1 + 8192, K1_TOT = K1_HT + 8 * 4096, K1_END = K1_TOT + 16;
__global__ __launch_bounds__(1024) void k_bin(const int* __restrict__ src, const int* __restrict__ dst, unsigned* __restrict__ T,
                                              u32x4* __restrict__ seg, const u16* __restrict__ sv, const u16* __restrict__ w1,
                                              const u16* __restrict__ w2, const u16* __restrict__ wr, u32x4* __restrict__ wf,
                                              char* __restrict__ htab, int N, int E, int W, int G1, unsigned magicBN, int BN) {
  __shared__ __attribute__((aligned(16))) char lds_raw[K1_END];
  const unsigned L0 = (unsigned)(uintptr_t)(__attribute__((address_space(3))) char*)lds_raw;
  const int tid = threadIdx.x, lane = tid & 63;
  const int wave = __builtin_amdgcn_readfirstlane(tid >> 6);
  if ((int)blockIdx.x >= G1) {
    const int idx = ((int)blockIdx.x - G1) * 1024 + tid;
    if (idx >= NFRAG * 64) return;
    const int f = idx >> 6, i32 = lane & 31, h = lane >> 5;
    const u16* p; int s2;
    if (f < 24) { const int ct = f >> 2; p = w2 + (16 * (f & 3) + 4 * h) * 192 + 64 * (ct >> 1) + chan_of(ct & 1, i32); s2 = 8 * 192; }
    else { const int ct = f - 24; p = wr + 8 * h * 192 + 64 * (ct >> 1) + chan_of(ct & 1, i32); s2 = 4 * 192; }
    unsigned v[8];
#pragma unroll
    for (int j = 0; j < 8; ++j) v[j] = p[(j & 3) * 192 + (j >> 2) * s2];
    wf[idx] = u32x4{v[0] | v[1] << 16, v[2] | v[3] << 16, v[4] | v[5] << 16, v[6] | v[7] << 16};
    return;
  }
  if (wave >= 8) {
    const int l31 = lane & 31, h = lane >> 5;
    const int n0 = ((int)blockIdx.x * 8 + wave - 8) * 32;
    const u32x4 wv = ((const u32x4*)w1)[tid - 512];
    const u32x4* sp = (const u32x4*)(sv + (size_t)min(n0 + l31, N - 1) * 256 + 32 * h);
    u32x4 sf[4];
#pragma unroll
    for (int ks = 0; ks < 4; ++ks) sf[ks] = sp[ks];
    *LDSP(u32x4, L0 + K1_W1 + (tid - 512) * 16) = wv;
    __syncthreads();
    if (n0 >= N) return;
    const unsigned ta = L0 + K1_W1 + (32 * h + ((lane >> 2) & 3)) * 128 + (16 * ((lane >> 4) & 1) + 4 * (lane & 3)) * 2;
    const unsigned HT = L0 + K1_HT + (wave - 8) * 4096;
#pragma unroll
    for (int ht = 0; ht < 2; ++ht) {
      f32x16 d1 = zero16();
#pragma unroll
      for (int ks = 0; ks < 4; ++ks) {
        const u32x2 a0 = trread(ta + ks * 1024 + ht * 64), a1 = trread(ta + ks * 1024 + 512 + ht * 64);
        d1 = mfma(u32x4{a0[0], a0[1], a1[0], a1[1]}, sf[ks], d1);
      }
#pragma unroll
      for (int r = 0; r < 16; ++r) { const float a = rb(d1[r]); d1[r] = a * __builtin_amdgcn_rcpf(1.0f + __builtin_amdgcn_exp2f(a * -1.4426950408889634f)); }
#pragma unroll
      for (int s = 0; s < 2; ++s) *LDSP(u32x4, HT + l31 * 128 + 16 * ((4 * h + 2 * ht + s) ^ (l31 & 7))) = pack8(d1, s);
    }
#pragma unroll
    for (int i = 0; i < 4; ++i) {
      const int n = 8 * i + (lane >> 3);
      const u32x4 v = *LDSP(u32x4, HT + i * 1024 + lane * 16);
      if (n0 + n < N) *(u32x4*)(htab + (size_t)(n0 + n) * 128 + 16 * ((lane & 7) ^ (n & 7))) = v;
    }
    return;
  }
  i32x4 s4[2], d4[2];
#pragma unroll
  for (int u = 0; u < 2; ++u) {
    const int e0 = blockIdx.x * CH + u * 2048 + tid * 4;
    s4[u] = i32x4{0, 0, 0, 0}; d4[u] = i32x4{-1, -1, -1, -1};
    if (e0 + 3 < E) { s4[u] = *(const i32x4*)(src + e0); d4[u] = *(const i32x4*)(dst + e0); }
    else {
#pragma unroll
      for (int k = 0; k < 4; ++k) if (e0 + k < E) { s4[u][k] = src[e0 + k]; d4[u][k] = dst[e0 + k]; }
    }
  }
  *LDSP(u32x4, L0 + K1_CNT + wave * 1024 + lane * 16) = u32x4{0, 0, 0, 0};
#pragma unroll
  for (int i = 0; i < 3; ++i) if (i * 512 + tid < SEGQ) *LDSP(u32x4, L0 + K1_STAGE + (i * 512 + tid) * 16) = u32x4{SENT, SENT, SENT, SENT};
  unsigned ca[8], rec[8], rank[8];
#pragma unroll
  for (int k = 0; k < 8; ++k) {
    const unsigned d = d4[k >> 2][k & 3];
    const unsigned b = __umulhi(d, magicBN);
    ca[k] = L0 + K1_CNT + wave * 1024 + b * 4;
    rec[k] = (unsigned)s4[k >> 2][k & 3] | (d - b * (unsigned)BN) << 24;
    rank[k] = SENT;
    if (d != SENT) rank[k] = __hip_atomic_fetch_add(LDSP(unsigned, ca[k]), 1u, __ATOMIC_RELAXED, __HIP_MEMORY_SCOPE_WORKGROUP);
  }
  __syncthreads();
  if ((int)blockIdx.x >= W) return;
  if (wave == 0) {
    u32x4 c[8], tot = {0, 0, 0, 0};
#pragma unroll
    for (int w = 0; w < 8; ++w) { c[w] = *LDSP(u32x4, L0 + K1_CNT + w * 1024 + lane * 16); tot += c[w]; }
    const u32x4 q = (tot + 3u) >> 2;
    const unsigned ls = q[0] + q[1] + q[2] + q[3];
    const unsigned incl = wave_incl_scan(ls);
    u32x4 st; st[0] = incl - ls; st[1] = st[0] + q[0]; st[2] = st[1] + q[1]; st[3] = st[2] + q[2];
    u32x4 run = st * 4u;
#pragma unroll
    for (int w = 0; w < 8; ++w) { *LDSP(u32x4, L0 + K1_CNT + w * 1024 + lane * 16) = run; run += c[w]; }
    if (lane == 63) *LDSP(unsigned, L0 + K1_TOT) = incl;
    *(u32x4*)&T[blockIdx.x * 256 + 4 * lane] = st | (q << 16);
  }
  __syncthreads();
  unsigned base[8];
#pragma unroll
  for (int k = 0; k < 8; ++k) base[k] = *LDSP(unsigned, rank[k] != SENT ? ca[k] : L0);
#pragma unroll
  for (int k = 0; k < 8; ++k) if (rank[k] != SENT) *LDSP(unsigned, L0 + K1_STAGE + (base[k] + rank[k]) * 4) = rec[k];
  const int total = *LDSP(int, L0 + K1_TOT);
  __syncthreads();
#pragma unroll
  for (int i = 0; i < 3; ++i) {
    const int qd = i * 512 + tid;
    if (qd < total) seg[(size_t)blockIdx.x * SEGQ + qd] = *LDSP(u32x4, L0 + K1_STAGE + qd * 16);
  }
}

DI void dma16(u32x4 rsrc, unsigned voff, unsigned ldsaddr) {
  asm volatile("s_mov_b32 m0, %2\n\ts_nop 0\n\tbuffer_load_dwordx4 %0, %1, 0 offen lds" ::"v"(voff), "s"(rsrc), "s"(ldsaddr) : "memory");
}
DI void dma16x3(u32x4 rsrc, unsigned voff, unsigned m0, unsigned m1, unsigned m2) {
  asm volatile("s_mov_b32 m0, %2\n\ts_nop 0\n\tbuffer_load_dwordx4 %0, %1, 0 offen lds\n\t"
               "s_mov_b32 m0, %3\n\ts_nop 0\n\tbuffer_load_dwordx4 %0, %1, 0 offen offset:128 lds\n\t"
               "s_mov_b32 m0, %4\n\ts_nop 0\n\tbuffer_load_dwordx4 %0, %1, 0 offen offset:256 lds"
               ::"v"(voff), "s"(rsrc), "s"(m0), "s"(m1), "s"(m2) : "memory");
}
DI u32x2 ldpos_raw(const char* pos, unsigned off, unsigned tot8) {
  typedef __attribute__((aligned(2))) u32x2 u32x2u;
  return *(const u32x2u*)(pos + min(off & ~3u, tot8));
}
DI u32x2 fixpos(u32x2 v, unsigned off, unsigned tot8) {
  const unsigned sh = (off - min(off & ~3u, tot8)) * 8u;
  return u32x2{__builtin_amdgcn_alignbit(v[1], v[0], sh), v[1] >> sh};
}
DI unsigned eqmask(unsigned a, unsigned b, unsigned ones) {
  unsigned x = a ^ b;
  asm("v_pk_min_u16 %0, %0, %1\n\tv_pk_sub_u16 %0, %0, %1" : "+v"(x) : "s"(ones));
  return x;
}

__global__ __launch_bounds__(512, 2) void k_agg(const u16* __restrict__ sv, const char* __restrict__ pos, const u16* __restrict__ freqs,
                                                const unsigned* __restrict__ T, const u32x4* __restrict__ seg, const u32x4* __restrict__ wf,
                                                const char* __restrict__ htab, u16* __restrict__ out, int N, int W, int BN, int B, int bpx) {
  extern __shared__ __attribute__((aligned(16))) char lds_raw[];
  const int tid = threadIdx.x, lane0 = tid & 63;
  const int wave = __builtin_amdgcn_readfirstlane(tid >> 6);
  const int b = ((int)blockIdx.x & 7) * bpx + ((int)blockIdx.x >> 3);
  if (b >= B) return;
  unsigned long long* diag = (unsigned long long*)((char*)wf + (64 << 20)); (void)diag;
  STAMP(0);
  const unsigned L0 = (unsigned)(uintptr_t)(__attribute__((address_space(3))) char*)lds_raw;
  const int node0 = b * BN;
  const int nb = min(BN, N - node0);
  const unsigned tot8 = (unsigned)N * 6u - 8u;
  const uint64_t svp = (uint64_t)sv, wfp = (uint64_t)wf;
  const u32x4 rs_sv = {(unsigned)svp, (unsigned)(svp >> 32), (unsigned)N * 512u, 0x00020000u};
  const __amdgpu_buffer_rsrc_t rb_h = __builtin_amdgcn_make_buffer_rsrc((void*)htab, 0, N * 128, 0x00020000);
#define FRESH_LANE int lane = lane0; asm volatile("" : "+v"(lane)); const int l31 = lane & 31, h = lane >> 5; (void)l31; (void)h
  const unsigned TB = L0 + L_TB + wave * TBS, LIST = L0 + L_LIST + wave * LISTS;

  f32x16 acc[4][2];

  int batch = 0, round = 0;
  bool first = true;
  while (true) {
    if (wave >= 2) {
      FRESH_LANE;
      const int fw = wave - 2;
      const int cpw = (W + NFW - 1) / NFW;
      const int cell = fw * cpw + lane;
      const unsigned t = (lane < cpw && cell < W) ? T[cell * 256 + b] : 0u;
      u32x2 dp[4];
#pragma unroll
      for (int i = 0; i < 4; ++i) dp[i] = ldpos_raw(pos, (unsigned)min(node0 + 64 * i + lane, N - 1) * 6u, tot8);
      if (lane < 8) *LDSP(unsigned, L0 + L_CNT + wave * 32 + lane * 4) = 0;
      const int q = t >> 16;
      const int incl = wave_incl_scan(q), excl = incl - q;
      const int total = __builtin_amdgcn_readlane(incl, 63);
      STAMP(1);
      const int win0 = batch * BQ;
      const int gq = cell * SEGQ + (int)(t & 0xffffu) - excl;
      const int hi = min(incl, win0 + BQ);
#pragma nounroll
      for (int i = max(excl, win0); i < hi; ++i) *LDSP(int, TB + (i - win0) * 4) = gq + i;
      if (lane == 0) *LDSP(int, L0 + L_NB + wave * 4) = (total + BQ - 1) / BQ;
      int mq[3];
#pragma unroll
      for (int s = 0; s < 3; ++s) mq[s] = *LDSP(int, TB + (64 * s + lane) * 4);
      u32x4 r4[3];
#pragma unroll
      for (int s = 0; s < 3; ++s) {
        const bool ok = win0 + 64 * s + lane < total;
        r4[s] = seg[ok ? mq[s] : 0];
        if (!ok) r4[s] = u32x4{SENT, SENT, SENT, SENT};
      }
#pragma unroll
      for (int i = 0; i < 4; ++i)
        *LDSP(u32x2, L0 + L_DPOS + (64 * i + lane) * 8) = fixpos(dp[i], (unsigned)min(node0 + 64 * i + lane, N - 1) * 6u, tot8);
      STAMP(2);
      u32x2 ps[12], pd[12];
#pragma unroll
      for (int c = 0; c < 12; ++c) {
        const unsigned rec = r4[c >> 2][c & 3];
        ps[c] = ldpos_raw(pos, rec == SENT ? 0u : (rec & 0xffffffu) * 6u, tot8);
      }
      SB();
      __builtin_amdgcn_s_barrier();
      SB();
#pragma unroll
      for (int c = 0; c < 12; ++c) pd[c] = *LDSP(u32x2, L0 + L_DPOS + (r4[c >> 2][c & 3] >> 24) * 8);
      u32x4 cand[12];
#pragma unroll
      for (int c = 0; c < 12; ++c) {
        const unsigned rec = r4[c >> 2][c & 3];
        const u32x2 p = fixpos(ps[c], rec == SENT ? 0u : (rec & 0xffffffu) * 6u, tot8);
        const unsigned xy = pk2(lo16(p[0]) - lo16(pd[c][0]), hi16(p[0]) - hi16(pd[c][0]));
        const float z = rb(lo16(p[1]) - lo16(pd[c][1]));
        const float x = lo16(xy), y = hi16(xy);
        const unsigned sq = pk2(x * x, y * y);
        const float q2 = rb(rb(lo16(sq) + hi16(sq) + rb(z * z)) + 1e-8f);
        const unsigned zd = pk2(z, __builtin_amdgcn_sqrtf(q2));
        unsigned rank = SENT;
        if ((rec != SENT) & (hi16(zd) < 5.0f))
          rank = __hip_atomic_fetch_add(LDSP(unsigned, L0 + L_CNT + wave * 32 + ((rec >> 24) / GN) * 4), 1u, __ATOMIC_RELAXED, __HIP_MEMORY_SCOPE_WORKGROUP);
        cand[c] = u32x4{rec, xy, zd, rank};
      }
#pragma unroll
      for (int c = 0; c < 12; ++c) {
        const unsigned p = cand[c][3] - SUBC * round;
        if ((cand[c][3] != SENT) & (p < (unsigned)SUBC))
          *LDSP(u32x4, L0 + L_LIST + ((cand[c][0] >> 24) / GN) * LISTS + (fw * SUBC + p) * 16) = cand[c];
      }
    } else {
      FRESH_LANE;
      if (lane < 8) *LDSP(unsigned, L0 + L_CNT + wave * 32 + lane * 4) = 0;
      if (wave == 0 && lane < 16) *LDSP(float, L0 + L_FR + lane * 4) = lo16(freqs[lane]);
      __builtin_amdgcn_s_barrier();
      if (first) {
        const int ni = (nb + 1) >> 1;
        for (int i = wave; i < ni; i += 2) {
          const int r = 2 * i + h;
          dma16(rs_sv, (unsigned)(node0 + r) * 512u + 16u * (l31 ^ (r & 15)), L0 + L_TB + i * 1024);
        }
        const u32x4 rs_wf = {(unsigned)wfp, (unsigned)(wfp >> 32), NFRAG * 1024u, 0x00020000u};
        for (int i = wave; i < NFRAG; i += 2) dma16(rs_wf, i * 1024u + 16u * lane, L0 + L_W + i * 1024);
        asm volatile("s_waitcnt vmcnt(0)" ::: "memory");
      }
    }
    STAMP(3);
    __syncthreads();
    STAMP(4);
    FRESH_LANE;
    u32x4 own[16];
    {
      const unsigned key = (unsigned)(wave * GN + l31) & 15u;
#pragma unroll
      for (int i = 0; i < 16; ++i) own[i] = *LDSP(u32x4, TB + l31 * 512 + 16 * ((unsigned)(8 * (i >> 2) + 4 * h + (i & 3)) ^ key));
    }
    const int cm = *LDSP(int, L0 + L_CNT + lane * 4);
    const int nbv = *LDSP(int, L0 + L_NB + (lane & 7) * 4);
    const bool more = __ballot(cm > SUBC * (round + 1)) != 0;
    int nbatch = 0;
#pragma unroll
    for (int g = 2; g < 8; ++g) nbatch = max(nbatch, __builtin_amdgcn_readlane(nbv, g));
    int cnt = 0, off[NFW], gap[NFW];
#pragma unroll
    for (int i = 0; i < NFW; ++i) {
      const int c = min(max(__builtin_amdgcn_readlane(cm, 16 + 8 * i + wave) - SUBC * round, 0), SUBC);
      cnt += c; off[i] = cnt; gap[i] = SUBC - c;
    }
    const int ntile = (cnt + 31) >> 5;
    u32x4 raw[3];
#pragma unroll
    for (int i = 0; i < 3; ++i) {
      const int p = 64 * i + lane;
      int idx = p;
#pragma unroll
      for (int k = 0; k < NFW - 1; ++k) idx += p >= off[k] ? gap[k] : 0;
      raw[i] = *LDSP(u32x4, LIST + min(idx, NFW * SUBC - 1) * 16);
    }
    asm volatile("s_waitcnt lgkmcnt(0)" : "+v"(raw[0]), "+v"(raw[1]), "+v"(raw[2])::"memory");
#pragma unroll
    for (int i = 0; i < 3; ++i) *LDSP(unsigned, LIST + (64 * i + lane) * 4) = 64 * i + lane < cnt ? (raw[i][0] & 0xffffffu) : (unsigned)N;
    const int pe = (l31 & 19) | ((l31 & 4) << 1) | ((l31 & 8) >> 1);
    const int drow = lane >> 3;
    const unsigned dchunk = 128u + 16u * ((lane & 7) ^ (drow & 2));
    u32x4 hN[4];
    auto issue = [&](int t) {
      const unsigned se = *LDSP(unsigned, LIST + (32 * t + pe) * 4);
      unsigned sr[4];
#pragma unroll
      for (int i = 0; i < 4; ++i) sr[i] = *LDSP(unsigned, LIST + (32 * t + 8 * i + drow) * 4);
#pragma unroll
      for (int ks = 0; ks < 4; ++ks) hN[ks] = __builtin_amdgcn_raw_buffer_load_b128(rb_h, se * 128u + 64u * h + 16u * ks, 0, 0);
      asm volatile("s_waitcnt lgkmcnt(0)" ::: "memory");
#pragma unroll
      for (int i = 0; i < 4; ++i) dma16x3(rs_sv, sr[i] * 512u + dchunk, TB + i * 1024, TB + i * 1024 + 4096 - 128, TB + i * 1024 + 8192 - 256);
    };
    issue(0);
    STAMP(5);
#pragma unroll
    for (int i = 0; i < 3; ++i) {
      if (i == 0 || 64 * i < cnt) {
        const int p = 64 * i + lane;
        const bool ok = p < cnt;
        const float d = ok ? hi16(raw[i][2]) : 1.0f;
        const float inv = __builtin_amdgcn_rcpf(d);
        const float a = rb(rb(3.141592653589793f * d) * 0.2f);
        const float f = ok ? 0.5f * rb(rb(__builtin_amdgcn_cosf(a * 0.15915494309189535f)) + 1.0f) : 0.f;
        const unsigned hxy = pk2(lo16(raw[i][1]) * inv, hi16(raw[i][1]) * inv);
        const float hz = rb(lo16(raw[i][2]) * inv);
        const unsigned fxy = ok ? pk2(f * lo16(hxy), f * hi16(hxy)) : 0u;
        const unsigned fzf = ok ? pk2(f * hz, f) : 0u;
        *LDSP(u16, LIST + O_D + p * 2) = (u16)(__float_as_uint(d) >> 16);
        *LDSP(u16, LIST + O_DL + p * 2) = ok ? (u16)(raw[i][0] >> 24) : (u16)0xffff;
        *LDSP(u16, LIST + O_F + p * 2) = (u16)(fzf >> 16);
        *LDSP(u16, LIST + O_FX + p * 2) = (u16)fxy;
        *LDSP(u16, LIST + O_FX + 384 + p * 2) = (u16)(fxy >> 16);
        *LDSP(u16, LIST + O_FX + 768 + p * 2) = (u16)fzf;
      }
    }
    if (first) {
#pragma unroll
      for (int i = 0; i < 16; ++i)
#pragma unroll
        for (int k = 0; k < 4; ++k) {
          acc[i >> 2][(i >> 1) & 1][8 * (i & 1) + 2 * k] = lo16(own[i][k]);
          acc[i >> 2][(i >> 1) & 1][8 * (i & 1) + 2 * k + 1] = hi16(own[i][k]);
        }
      first = false;
    }
    STAMP(6);
    const int q4 = (lane >> 2) & 3, pp = lane & 3, chh = (lane >> 4) & 1;
    const unsigned trb = TB + (8 * h + q4) * 128 + 64 * (pp & 1) + 16 * chh + 8 * (pp >> 1);
    const unsigned trb0 = trb + 32 * (q4 >> 1), trb1 = trb + 32 * ((q4 >> 1) ^ 1);
    const unsigned mypair = (unsigned)(wave * GN + l31) * 0x10001u;
    const unsigned WL = L0 + L_W + lane * 16;
    unsigned la = LIST + 16 * h, ld = LIST + O_D + 2 * pe;
    for (int t = 0; t < ntile; ++t) {
      u32x4 Wb[5];
      auto ldW = [&](int ct) {
        Wb[4] = *LDSP(u32x4, WL + (24 + ct) * 1024);
#pragma unroll
        for (int ks = 0; ks < 4; ++ks) Wb[ks] = *LDSP(u32x4, WL + (4 * ct + ks) * 1024);
      };
      const float d = lo16(*LDSP(u16, ld));
      const f32x4 f0 = *LDSP(f32x4, L0 + L_FR + 32 * h), f1 = *LDSP(f32x4, L0 + L_FR + 32 * h + 16);
      u32x4 msk[2], S1[2];
#pragma unroll
      for (int s = 0; s < 2; ++s) { msk[s] = *LDSP(u32x4, la + O_DL + 32 * s); S1[s] = *LDSP(u32x4, la + O_F + 32 * s); }
      ldW(2);
      SB();
      u32x4 rbf;
      {
        const float inv = __builtin_amdgcn_rcpf(d);
#pragma unroll
        for (int i = 0; i < 4; ++i) {
          const float fa = i < 2 ? f0[2 * i] : f1[2 * i - 4], fb = i < 2 ? f0[2 * i + 1] : f1[2 * i - 3];
          const unsigned x = pk2(d * fa, d * fb);
          float ra = lo16(x) * 0.15915494309189535f, rc = hi16(x) * 0.15915494309189535f;
          ra -= __builtin_rintf(ra); rc -= __builtin_rintf(rc);
          const unsigned sn = pk2(__builtin_amdgcn_sinf(ra), __builtin_amdgcn_sinf(rc));
          rbf[i] = pk2(lo16(sn) * inv, hi16(sn) * inv);
        }
      }
#pragma unroll
      for (int s = 0; s < 2; ++s)
#pragma unroll
        for (int i = 0; i < 4; ++i) { msk[s][i] = eqmask(msk[s][i], mypair, 0x00010001u); S1[s][i] &= msk[s][i]; }
      SB();
      asm volatile("s_waitcnt vmcnt(0)" ::: "memory");
      if (t < 3) STAMP(7 + 2 * t);
      u32x4 hA[4];
#pragma unroll
      for (int ks = 0; ks < 4; ++ks) hA[ks] = hN[ks];
      f32x16 ph, w;
      auto gemm = [&]() {
        w = mfma(rbf, Wb[4], zero16());
        ph = mfma(hA[0], Wb[0], zero16());
#pragma unroll
        for (int ks = 1; ks < 4; ++ks) ph = mfma(hA[ks], Wb[ks], ph);
      };
      u32x2 va[4], vb[4];
      auto ldv = [&](u32x2 (&v)[4], int t2, int k) {
#pragma unroll
        for (int bb = 0; bb < 4; ++bb) v[bb] = trread((t2 ? trb1 : trb0) + k * 4096 + (16 * (bb >> 1) + 4 * (bb & 1)) * 128);
      };
      auto vv = [&](const u32x2 (&v)[4], const f32x16& m, f32x16& ac) {
        u32x4 a[2];
#pragma unroll
        for (int bb = 0; bb < 4; ++bb) {
          a[bb >> 1][2 * (bb & 1)] = pk2(m[4 * bb] * lo16(v[bb][0]), m[4 * bb + 1] * hi16(v[bb][0]));
          a[bb >> 1][2 * (bb & 1) + 1] = pk2(m[4 * bb + 2] * lo16(v[bb][1]), m[4 * bb + 3] * hi16(v[bb][1]));
        }
        ac = mfma(a[0], S1[0], ac);
        ac = mfma(a[1], S1[1], ac);
      };
#pragma unroll
      for (int t2 = 0; t2 < 2; ++t2) {
        ldv(va, t2, 0); ldv(vb, t2, 1);
        gemm();
        SB();
        ldW(t2 == 0 ? 3 : 0);
        SB();
        const f32x16 m = ph * w;
        vv(va, m, acc[1][t2]);
        SB();
        ldv(va, t2, 2);
        SB();
        vv(vb, m, acc[2][t2]);
        vv(va, m, acc[3][t2]);
        SB();
      }
      if (t < 3) STAMP(8 + 2 * t);
      if (t + 1 < ntile) issue(t + 1);
      else {
#pragma unroll
        for (int ks = 0; ks < 4; ++ks) hN[ks] = u32x4{0, 0, 0, 0};
      }
      SB();
#pragma unroll
      for (int t2 = 0; t2 < 2; ++t2) {
        gemm();
        SB();
        ldW(t2 == 0 ? 1 : 4);
        SB();
        const f32x16 m = ph * w;
        acc[0][t2] = mfma(pack8(m, 0), S1[0], acc[0][t2]);
        acc[0][t2] = mfma(pack8(m, 1), S1[1], acc[0][t2]);
        SB();
      }
      {
        gemm();
        SB();
        ldW(5);
        SB();
        const f32x16 m0 = ph * w;
        const u32x4 a00 = pack8(m0, 0), a01 = pack8(m0, 1);
        SB();
        gemm();
        SB();
        u32x4 sr[3][2];
#pragma unroll
        for (int s = 0; s < 2; ++s)
#pragma unroll
          for (int k = 0; k < 3; ++k) sr[k][s] = *LDSP(u32x4, la + O_FX + 384 * k + 32 * s);
        SB();
#pragma unroll
        for (int k = 0; k < 3; ++k) { sr[k][0] &= msk[0]; acc[1 + k][0] = mfma(a00, sr[k][0], acc[1 + k][0]); }
#pragma unroll
        for (int k = 0; k < 3; ++k) { sr[k][1] &= msk[1]; acc[1 + k][0] = mfma(a01, sr[k][1], acc[1 + k][0]); }
        const f32x16 m1 = ph * w;
        const u32x4 a10 = pack8(m1, 0), a11 = pack8(m1, 1);
#pragma unroll
        for (int k = 0; k < 3; ++k) acc[1 + k][1] = mfma(a10, sr[k][0], acc[1 + k][1]);
#pragma unroll
        for (int k = 0; k < 3; ++k) acc[1 + k][1] = mfma(a11, sr[k][1], acc[1 + k][1]);
        SB();
      }
      la += 64; ld += 64;
    }
    asm volatile("s_waitcnt vmcnt(0)" ::: "memory");
    if (more) ++round;
    else if (batch + 1 < nbatch) { ++batch; round = 0; }
    else break;
    __syncthreads();
  }
  STAMP(13);
  {
    FRESH_LANE;
    const unsigned key = (unsigned)(wave * GN + l31) & 15u;
    if (l31 < GN) {
#pragma unroll
      for (int i = 0; i < 16; ++i)
        *LDSP(u32x4, TB + l31 * 512 + 16 * ((unsigned)(8 * (i >> 2) + 4 * h + (i & 3)) ^ key)) = pack8(acc[i >> 2][(i >> 1) & 1], i & 1);
    }
    const int gw = min(nb - wave * GN, GN);
    u32x4 v[(GN + 1) / 2];
#pragma unroll
    for (int i = 0; i < (GN + 1) / 2; ++i) v[i] = *LDSP(u32x4, TB + i * 1024 + lane * 16);
#pragma unroll
    for (int i = 0; i < (GN + 1) / 2; ++i) {
      const int r = 2 * i + h, dl = wave * GN + r;
      if (r < gw) *(u32x4*)((char*)out + (size_t)(node0 + dl) * 512 + 16 * (l31 ^ (dl & 15))) = v[i];
    }
  }
  STAMP(14);
  STAMP(15);
}

extern "C" void kernel_launch(void* const* d_in, const int* in_sizes, int n_in, void* d_out, int out_size, void* d_ws, size_t ws_size,
                              hipStream_t stream) {
  const u16* sv = (const u16*)d_in[0];
  const char* pos = (const char*)d_in[1];
  const int* src = (const int*)d_in[2];
  const int* dst = (const int*)d_in[3];
  const u16* fr = (const u16*)d_in[4];
  const u16* w1 = (const u16*)d_in[5];
  const u16* w2 = (const u16*)d_in[6];
  const u16* wr = (const u16*)d_in[7];
  const int N = in_sizes[1] / 3, E = in_sizes[2];
  const int BN = (N + 255) / 256 < 2 ? 2 : (N + 255) / 256;
  const int B = (N + BN - 1) / BN, bpx = (B + 7) / 8;
  const int W = (E + CH - 1) / CH;
  const int HW = (N + 255) / 256;
  const int G1 = W > HW ? W : HW;
  const unsigned magicBN = (unsigned)(0x100000000ull / (unsigned)BN) + 1u;
  char* ws = (char*)d_ws;
  (void)hipFuncSetAttribute((const void*)k_agg, hipFuncAttributeMaxDynamicSharedMemorySize, L_END);
  k_bin<<<G1 + 2, 1024, 0, stream>>>(src, dst, (unsigned*)(ws + WS_T), (u32x4*)(ws + WS_SEG), sv, w1, w2, wr, (u32x4*)ws, ws + WS_H, N, E, W, G1,
                                     magicBN, BN);
  k_agg<<<8 * bpx, 512, L_END, stream>>>(sv, pos, fr, (const unsigned*)(ws + WS_T), (const u32x4*)(ws + WS_SEG), (const u32x4*)ws, ws + WS_H,
                                         (u16*)d_out, N, W, BN, B, bpx);
}
```

```cpp
#if defined(__HIP_DEVICE_COMPILE__)
#pragma clang attribute push(__attribute__((target("no-packed-fp32-ops"))), apply_to = function)
#endif
#include <hip/hip_runtime.h>
#include <stdint.h>

#define TID __builtin_amdgcn_workitem_id_x()
#define BID __builtin_amdgcn_workgroup_id_x()
typedef unsigned short u16;
typedef __attribute__((ext_vector_type(8))) __bf16 bf16x8;
typedef __attribute__((ext_vector_type(2))) __bf16 bf16x2;
typedef __attribute__((ext_vector_type(4))) short s16x4;
typedef __attribute__((ext_vector_type(2))) float f32x2;
typedef __attribute__((ext_vector_type(4))) float f32x4;
typedef __attribute__((ext_vector_type(16))) float f32x16;
typedef __attribute__((ext_vector_type(4))) unsigned u32x4;
typedef __attribute__((ext_vector_type(3))) unsigned u32x3;
typedef __attribute__((ext_vector_type(2))) unsigned u32x2;
#define LDSP(T, off) ((__attribute__((address_space(3))) T*)(uintptr_t)(off))
#define DI __device__ __forceinline__
#define SB() __builtin_amdgcn_sched_barrier(0)

constexpr int CH = 4096;
constexpr int SEGQ = CH / 4 + 192;
constexpr int GN = 25;
constexpr int NFRAG = 30;
constexpr int SUBC = 32;
constexpr int CAP = 128;
constexpr int BQ = 192;
constexpr unsigned SENT = 0xFFFFFFFFu;
constexpr int L_W = 0;
constexpr int L_TB = NFRAG * 1024;
constexpr int TBS = GN * 512;
constexpr int L_LIST = L_TB + 8 * TBS;
constexpr int O_D = 768, O_DL = O_D + 256, O_F = O_DL + 256, O_FX = O_F + 256;
constexpr int LISTS = O_FX + 768;
constexpr int L_CNT = L_LIST + 8 * LISTS;
constexpr int L_NB = L_CNT + 256;
constexpr int L_FR = L_NB + 64;
constexpr int L_DPOS = L_FR + 64;
constexpr int L_END = L_DPOS + 256 * 16;
static_assert(L_END <= 163840 && L_TB % 512 == 0, "LDS");
constexpr size_t WS_T = 65536;
constexpr size_t WS_SEG = WS_T + 256 * 256 * 4;
constexpr size_t WS_P = WS_SEG + (size_t)256 * SEGQ * 16;
constexpr size_t WS_H = WS_P + 51200 * 8;

#define STAMP(i)
#define STAMP1(i)
#define WAITV
DI float lo16(unsigned x) { return __uint_as_float(x << 16); }
DI float hi16(unsigned x) { return __uint_as_float(x & 0xffff0000u); }
DI unsigned pk2(float a, float b) { f32x2 v = {a, b}; return __builtin_bit_cast(unsigned, __builtin_convertvector(v, bf16x2)); }
DI float rb(float a) { return hi16(pk2(a, a)); }
DI f32x16 mfma(u32x4 a, u32x4 b, f32x16 c) {
  return __builtin_amdgcn_mfma_f32_32x32x16_bf16(__builtin_bit_cast(bf16x8, a), __builtin_bit_cast(bf16x8, b), c, 0, 0, 0);
}
DI f32x16 zero16() { f32x16 z;
#pragma unroll
  for (int r = 0; r < 16; ++r) z[r] = 0.f;
  return z; }
DI int wave_incl_scan(int v) {
  v += __builtin_amdgcn_update_dpp(0, v, 0x111, 0xf, 0xf, false);
  v += __builtin_amdgcn_update_dpp(0, v, 0x112, 0xf, 0xf, false);
  v += __builtin_amdgcn_update_dpp(0, v, 0x114, 0xf, 0xf, false);
  v += __builtin_amdgcn_update_dpp(0, v, 0x118, 0xf, 0xf, false);
  v += __builtin_amdgcn_update_dpp(0, v, 0x142, 0xa, 0xf, false);
  v += __builtin_amdgcn_update_dpp(0, v, 0x143, 0xc, 0xf, false);
  return v;
}
DI int chan_of(int t2, int i32) { return 32 * ((i32 >> 2) & 1) + 16 * t2 + 4 * (i32 >> 3) + (i32 & 3); }
DI u32x4 pack8(const f32x16& x, int s) {
  return u32x4{pk2(x[8 * s], x[8 * s + 1]), pk2(x[8 * s + 2], x[8 * s + 3]), pk2(x[8 * s + 4], x[8 * s + 5]), pk2(x[8 * s + 6], x[8 * s + 7])};
}
DI void setprio(int p) {
  if (p >= 3) __builtin_amdgcn_s_setprio(3);
  else if (p == 2) __builtin_amdgcn_s_setprio(2);
  else if (p == 1) __builtin_amdgcn_s_setprio(1);
  else __builtin_amdgcn_s_setprio(0);
}
DI u32x2 trread(unsigned a) { return __builtin_bit_cast(u32x2, __builtin_amdgcn_ds_read_tr16_b64_v4i16(LDSP(s16x4, a))); }
#define K1AUX 17
#define K2AUX 17
DI void st16(void* base, unsigned off, u32x4 v) {
  const __amdgpu_buffer_rsrc_t r = __builtin_amdgcn_make_buffer_rsrc(base, 0, 0x7fffffff, 0x00020000);
  __builtin_amdgcn_raw_buffer_store_b128(v, r, off, 0, K1AUX);
}
DI void wg_barrier() { asm volatile("s_waitcnt lgkmcnt(0)\n\ts_barrier" ::: "memory"); }

constexpr int K1_CNT = 0, K1_STAGE = 8 * 256 * 4, K1_TOT = K1_STAGE + SEGQ * 16, K1_W1 = 0, K1_HT = 8192, K1_END = K1_HT + 8 * 4096;
__global__ __launch_bounds__(512, 4) void k_bin(const int* __restrict__ src, const int* __restrict__ dst, unsigned* __restrict__ T,
                                                u32x4* __restrict__ seg, const u16* __restrict__ sv, const u16* __restrict__ pos,
                                                const u16* __restrict__ w1, const u16* __restrict__ w2, const u16* __restrict__ wr,
                                                u32x4* __restrict__ wf, u32x2* __restrict__ ptab, char* __restrict__ htab, int N, int E,
                                                int W, int HW, unsigned magicBN, int BN) {
  __shared__ __attribute__((aligned(16))) char lds_raw[K1_END];
  const unsigned L0 = (unsigned)(uintptr_t)(__attribute__((address_space(3))) char*)lds_raw;
  const int tid = TID, lane = tid & 63;
  const int wave = __builtin_amdgcn_readfirstlane(tid >> 6);
  const int bid = BID;
  STAMP1(0);
  if (bid >= W + HW) {
    const int idx = (bid - W - HW) * 512 + tid;
    if (idx >= NFRAG * 64) return;
    const int f = idx >> 6, i32 = lane & 31, h = lane >> 5;
    const u16* p; int s2;
    if (f < 24) { const int ct = f >> 2; p = w2 + (16 * (f & 3) + 4 * h) * 192 + 64 * (ct >> 1) + chan_of(ct & 1, i32); s2 = 8 * 192; }
    else { const int ct = f - 24; p = wr + 8 * h * 192 + 64 * (ct >> 1) + chan_of(ct & 1, i32); s2 = 4 * 192; }
    unsigned v[8];
#pragma unroll
    for (int j = 0; j < 8; ++j) v[j] = p[(j & 3) * 192 + (j >> 2) * s2];
    wf[idx] = u32x4{v[0] | v[1] << 16, v[2] | v[3] << 16, v[4] | v[5] << 16, v[6] | v[7] << 16};
    return;
  }
  if (bid >= W) {
    const int l31 = lane & 31, h = lane >> 5;
    const int n0 = ((bid - W) * 8 + wave) * 32;
    const int nn = min(n0 + l31, N - 1);
    const u32x4 wv = ((const u32x4*)w1)[tid];
    const u32x4* sp = (const u32x4*)(sv + (size_t)nn * 256 + 32 * h);
    u32x4 sf[4];
#pragma unroll
    for (int ks = 0; ks < 4; ++ks) sf[ks] = sp[ks];
    const unsigned px = pos[nn * 3], py = pos[nn * 3 + 1], pz = pos[nn * 3 + 2];
    *LDSP(u32x4, L0 + K1_W1 + tid * 16) = wv;
    wg_barrier();
    STAMP1(1); WAITV; STAMP1(2);
    if (n0 >= N) return;
    const unsigned ta = L0 + K1_W1 + (32 * h + ((lane >> 2) & 3)) * 128 + (16 * ((lane >> 4) & 1) + 4 * (lane & 3)) * 2;
    const unsigned HT = L0 + K1_HT + wave * 4096;
#pragma unroll
    for (int ht = 0; ht < 2; ++ht) {
      f32x16 d1 = zero16();
#pragma unroll
      for (int ks = 0; ks < 4; ++ks) {
        const u32x2 a0 = trread(ta + ks * 1024 + ht * 64), a1 = trread(ta + ks * 1024 + 512 + ht * 64);
        d1 = mfma(u32x4{a0[0], a0[1], a1[0], a1[1]}, sf[ks], d1);
      }
#pragma unroll
      for (int r = 0; r < 16; ++r) { const float a = rb(d1[r]); d1[r] = a * __builtin_amdgcn_rcpf(1.0f + __builtin_amdgcn_exp2f(a * -1.4426950408889634f)); }
#pragma unroll
      for (int s = 0; s < 2; ++s) *LDSP(u32x4, HT + l31 * 128 + 16 * ((4 * h + 2 * ht + s) ^ (l31 & 7))) = pack8(d1, s);
    }
    STAMP1(3);
    if (h == 0 && n0 + l31 < N) ptab[n0 + l31] = u32x2{px | py << 16, pz << 16};
#pragma unroll
    for (int i = 0; i < 4; ++i) {
      const int n = 8 * i + (lane >> 3);
      const u32x4 v = *LDSP(u32x4, HT + i * 1024 + lane * 16);
      if (n0 + n < N) st16(htab, (n0 + n) * 128 + 16 * ((lane & 7) ^ (n & 7)), v);
    }
    STAMP1(4); WAITV; STAMP1(5);
    return;
  }
  const __amdgpu_buffer_rsrc_t rb_s = __builtin_amdgcn_make_buffer_rsrc((void*)src, 0, E * 4, 0x00020000);
  const __amdgpu_buffer_rsrc_t rb_d = __builtin_amdgcn_make_buffer_rsrc((void*)dst, 0, E * 4, 0x00020000);
  const int e0 = bid * CH + tid * 4;
  u32x4 s4[2], d4[2];
#pragma unroll
  for (int u = 0; u < 2; ++u) d4[u] = __builtin_amdgcn_raw_buffer_load_b128(rb_d, (e0 + u * 2048) * 4, 0, 0);
#pragma unroll
  for (int u = 0; u < 2; ++u) s4[u] = __builtin_amdgcn_raw_buffer_load_b128(rb_s, (e0 + u * 2048) * 4, 0, 0);
  *LDSP(u32x4, L0 + K1_CNT + wave * 1024 + lane * 16) = u32x4{0, 0, 0, 0};
#pragma unroll
  for (int i = 0; i < 3; ++i) if (i * 512 + tid < SEGQ) *LDSP(u32x4, L0 + K1_STAGE + (i * 512 + tid) * 16) = u32x4{SENT, SENT, SENT, SENT};
  WAITV; STAMP1(1);
  unsigned ca[8], hi8[8], rank[8];
#pragma unroll
  for (int k = 0; k < 8; ++k) {
    const unsigned d = d4[k >> 2][k & 3];
    const unsigned b = (unsigned)(((uint64_t)d * magicBN) >> 32);
    ca[k] = L0 + K1_CNT + wave * 1024 + b * 4;
    rank[k] = SENT;
    if (e0 + (k >> 2) * 2048 + (k & 3) < E) rank[k] = __hip_atomic_fetch_add(LDSP(unsigned, ca[k]), 1u, __ATOMIC_RELAXED, __HIP_MEMORY_SCOPE_WORKGROUP);
    const unsigned dl = d - b * (unsigned)BN;
    hi8[k] = dl + ((dl * 41u) >> 10) * 7u;
  }
  wg_barrier();
  STAMP1(2);
  if (wave == 0) {
    u32x4 c[8], tot = {0, 0, 0, 0};
#pragma unroll
    for (int w = 0; w < 8; ++w) { c[w] = *LDSP(u32x4, L0 + K1_CNT + w * 1024 + lane * 16); tot += c[w]; }
    const u32x4 q = (tot + 3u) >> 2;
    const unsigned ls = q[0] + q[1] + q[2] + q[3];
    const unsigned incl = wave_incl_scan(ls);
    u32x4 st; st[0] = incl - ls; st[1] = st[0] + q[0]; st[2] = st[1] + q[1]; st[3] = st[2] + q[2];
    u32x4 run = st * 4u;
#pragma unroll
    for (int w = 0; w < 8; ++w) { *LDSP(u32x4, L0 + K1_CNT + w * 1024 + lane * 16) = run; run += c[w]; }
    if (lane == 63) *LDSP(unsigned, L0 + K1_TOT) = incl;
    *(u32x4*)&T[bid * 256 + 4 * lane] = st | (q << 16);
  }
  wg_barrier();
  STAMP1(3);
  unsigned base[8];
#pragma unroll
  for (int k = 0; k < 8; ++k) base[k] = *LDSP(unsigned, ca[k]);
#pragma unroll
  for (int k = 0; k < 8; ++k)
    if (rank[k] != SENT) *LDSP(unsigned, L0 + K1_STAGE + (base[k] + rank[k]) * 4) = s4[k >> 2][k & 3] << 3 | hi8[k] << 24;
  const int total = *LDSP(int, L0 + K1_TOT);
  wg_barrier();
#pragma unroll
  for (int i = 0; i < 3; ++i) {
    const int qd = i * 512 + tid;
    if (qd < total) st16(seg, (bid * SEGQ + qd) * 16, *LDSP(u32x4, L0 + K1_STAGE + qd * 16));
  }
  STAMP1(4); WAITV; STAMP1(5);
}

DI void dma16(u32x4 rsrc, unsigned voff, unsigned ldsaddr) {
  asm volatile("s_mov_b32 m0, %2\n\ts_nop 0\n\tbuffer_load_dwordx4 %0, %1, 0 offen lds" ::"v"(voff), "s"(rsrc), "s"(ldsaddr) : "memory");
}
DI void dma16x4(u32x4 rsrc, const unsigned (&voff)[4], unsigned soff, unsigned m0) {
  asm volatile("s_mov_b32 m0, %6\n\ts_nop 0\n\tbuffer_load_dwordx4 %0, %4, %5 offen lds\n\t"
               "s_add_u32 m0, m0, 0x400\n\ts_nop 0\n\tbuffer_load_dwordx4 %1, %4, %5 offen lds\n\t"
               "s_add_u32 m0, m0, 0x400\n\ts_nop 0\n\tbuffer_load_dwordx4 %2, %4, %5 offen lds\n\t"
               "s_add_u32 m0, m0, 0x400\n\ts_nop 0\n\tbuffer_load_dwordx4 %3, %4, %5 offen lds"
               ::"v"(voff[0]), "v"(voff[1]), "v"(voff[2]), "v"(voff[3]), "s"(rsrc), "s"(soff), "s"(m0) : "memory", "scc");
}
DI void dma16x3(u32x4 rsrc, unsigned voff, unsigned m0, unsigned m1, unsigned m2) {
  asm volatile("s_mov_b32 m0, %2\n\ts_nop 0\n\tbuffer_load_dwordx4 %0, %1, 0 offen lds\n\t"
               "s_mov_b32 m0, %3\n\ts_nop 0\n\tbuffer_load_dwordx4 %0, %1, 0 offen offset:128 lds\n\t"
               "s_mov_b32 m0, %4\n\ts_nop 0\n\tbuffer_load_dwordx4 %0, %1, 0 offen offset:256 lds"
               ::"v"(voff), "s"(rsrc), "s"(m0), "s"(m1), "s"(m2) : "memory");
}
DI u32x4 eqmask4(u32x4 a, unsigned b, unsigned ones) {
  u32x4 x;
#pragma unroll
  for (int i = 0; i < 4; ++i) x[i] = a[i] ^ b;
  asm("v_pk_min_u16 %0, %0, %4\n\tv_pk_min_u16 %1, %1, %4\n\tv_pk_min_u16 %2, %2, %4\n\tv_pk_min_u16 %3, %3, %4\n\t"
      "v_pk_sub_u16 %0, %0, %4\n\tv_pk_sub_u16 %1, %1, %4\n\tv_pk_sub_u16 %2, %2, %4\n\tv_pk_sub_u16 %3, %3, %4"
      : "+v"(x[0]), "+v"(x[1]), "+v"(x[2]), "+v"(x[3]) : "s"(ones));
  return x;
}

__global__ __launch_bounds__(512, 2) void k_agg(const u16* __restrict__ sv, const u32x2* __restrict__ ptab, const u16* __restrict__ freqs,
                                                const unsigned* __restrict__ T, const u32x4* __restrict__ seg, const u32x4* __restrict__ wf,
                                                const char* __restrict__ htab, u16* __restrict__ out, int N, int W, int BN, int B, int bpx) {
  extern __shared__ __attribute__((aligned(512))) char lds_raw[];
  const int tid = TID, lane0 = tid & 63;
  const int wave = __builtin_amdgcn_readfirstlane(tid >> 6);
  const int b = ((int)BID & 7) * bpx + ((int)BID >> 3);
  if (b >= B) return;
  unsigned long long* diag = (unsigned long long*)((char*)wf + (64 << 20)); (void)diag;
  STAMP(0);
  const unsigned L0 = (unsigned)(uintptr_t)(__attribute__((address_space(3))) char*)lds_raw;
  const int node0 = b * BN;
  const int nb = min(BN, N - node0);
  const uint64_t svp = (uint64_t)sv, wfp = (uint64_t)wf, hp = (uint64_t)htab;
  const u32x4 rs_sv = {(unsigned)svp, (unsigned)(svp >> 32), (unsigned)N * 512u, 0x00020000u};
  const u32x4 rs_h = {(unsigned)hp, (unsigned)(hp >> 32), (unsigned)N * 128u, 0x00020000u};
  const __amdgpu_buffer_rsrc_t rb_p = __builtin_amdgcn_make_buffer_rsrc((void*)ptab, 0, N * 8, 0x00020000);
#define FRESH_LANE int lane = lane0; asm volatile("" : "+v"(lane)); const int l31 = lane & 31, h = lane >> 5; (void)l31; (void)h
  const unsigned TB = L0 + L_TB + wave * TBS, LIST = L0 + L_LIST + wave * LISTS;

  f32x16 acc[4][2];
  u32x4 hN[4];
  const int cpw = (W + 7) >> 3;
  auto load_t = [&]() { FRESH_LANE; const int cell = wave * cpw + lane; return (lane < cpw && cell < W) ? T[cell * 256 + b] : 0u; };
  unsigned t = load_t();
  SB();
#pragma unroll
  for (int j = 0; j < 4; ++j) { acc[j][0] = zero16(); acc[j][1] = zero16(); }
  SB();
  int batch = 0, r1 = 0, r2 = 0;
  bool first = true;
  while (true) {
    {
      FRESH_LANE;
      const int cell = wave * cpw + lane;
      if (lane < 8) *LDSP(unsigned, L0 + L_CNT + wave * 32 + lane * 4) = lane >> 3;
      const int q = t >> 16;
      const int incl = wave_incl_scan(q), excl = incl - q;
      const int total = __builtin_amdgcn_readlane(incl, 63);
      STAMP(1);
      const int win0 = batch * BQ;
      const int gq = cell * SEGQ + (int)(t & 0xffffu) - excl;
      const int hi = min(incl, win0 + BQ);
#pragma nounroll
      for (int i = max(excl, win0); i < hi; ++i) *LDSP(int, LIST + (i - win0) * 4) = gq + i;
      if (lane == 0) *LDSP(int, L0 + L_NB + wave * 4) = (total + BQ - 1) / BQ;
      const bool third = total > win0 + 128;
      int mq[3];
#pragma unroll
      for (int s = 0; s < 3; ++s) mq[s] = *LDSP(int, LIST + (64 * s + lane) * 4);
      u32x4 r4[3];
#pragma unroll
      for (int s = 0; s < 3; ++s) {
        const bool ok = win0 + 64 * s + lane < total;
        r4[s] = seg[ok ? mq[s] : 0];
        if (!ok) r4[s] = u32x4{SENT, SENT, SENT, SENT};
      }
      if (first) {
        u32x2 dp[4];
#pragma unroll
        for (int i = 0; i < 4; ++i) dp[i] = __builtin_amdgcn_raw_buffer_load_b64(rb_p, (node0 + (2 * i + h) * GN + l31) * 8, 0, 0);
        const float fq = lo16(freqs[lane & 15]);
        const u32x4 rs_wf = {(unsigned)wfp, (unsigned)(wfp >> 32), NFRAG * 1024u, 0x00020000u};
        for (int i = wave; i < NFRAG; i += 8) dma16(rs_wf, i * 1024u + 16u * lane, L0 + L_W + i * 1024);
#pragma unroll
        for (int i = 0; i < 4; ++i)
          *LDSP(u32x4, L0 + L_DPOS + (64 * i + lane) * 16) = u32x4{l31 < GN ? dp[i][0] << 16 : 0x7fc00000u, dp[i][0] & 0xffff0000u, dp[i][1], 0u};
        *LDSP(float, L0 + L_FR + (lane & 15) * 4) = fq;
        first = false;
      }
      STAMP(2);
      u32x2 ps[12];
#pragma unroll
      for (int c = 0; c < 12; ++c) {
        ps[c] = u32x2{0, 0};
        if (c < 8 || third) ps[c] = __builtin_amdgcn_raw_buffer_load_b64(rb_p, r4[c >> 2][c & 3] & 0xffffffu, 0, 0);
      }
#pragma unroll
      for (int s = 0; s < 3; ++s) {
        if (s == 2 && !third) break;
        f32x4 pd[4];
#pragma unroll
        for (int c = 0; c < 4; ++c) pd[c] = *LDSP(f32x4, L0 + L_DPOS + ((r4[s][c] >> 20) & 0xff0u));
        unsigned rank[4];
#pragma unroll
        for (int c = 0; c < 4; ++c) {
          const u32x2 p = ps[4 * s + c];
          const float x = lo16(p[0]) - pd[c][0], y = hi16(p[0]) - pd[c][1], z = __uint_as_float(p[1]) - pd[c][2];
          rank[c] = SENT;
          if (x * x + y * y + z * z < 25.1f)
            rank[c] = __hip_atomic_fetch_add(LDSP(unsigned, L0 + L_CNT + wave * 32 + (r4[s][c] >> 29) * 4), 1u, __ATOMIC_RELAXED, __HIP_MEMORY_SCOPE_WORKGROUP);
        }
#pragma unroll
        for (int c = 0; c < 4; ++c) {
          const unsigned p = min(rank[c], 0x7fffffffu) - SUBC * r1;
          if (p < (unsigned)SUBC)
            *LDSP(u32x3, L0 + L_TB + (r4[s][c] >> 29) * TBS + (wave * SUBC + p) * 16) = u32x3{r4[s][c], ps[4 * s + c][0], ps[4 * s + c][1]};
        }
      }
    }
    asm volatile("s_waitcnt vmcnt(0)" ::: "memory");
    STAMP(3);
    wg_barrier();
    STAMP(4);
    FRESH_LANE;
    const int cm = *LDSP(int, L0 + L_CNT + lane * 4);
    const int nbv = *LDSP(int, L0 + L_NB + (lane & 7) * 4);
    const bool more1 = __builtin_amdgcn_ballot_w64(cm > SUBC * (r1 + 1)) != 0;
    const int cw = min(max(cm - SUBC * r1, 0), SUBC);
    int tot = 0, off[8], gap[8];
#pragma unroll
    for (int i = 0; i < 8; ++i) {
      const int c = __builtin_amdgcn_readlane(cw, 8 * i + wave);
      tot += c; off[i] = tot; gap[i] = SUBC - c;
    }
    const int cnt = min(max(tot - CAP * r2, 0), CAP);
    const int ntile = (cnt + 31) >> 5;
    u32x3 raw[2];
#pragma unroll
    for (int i = 0; i < 2; ++i) {
      const int p = CAP * r2 + 64 * i + lane;
      int idx = p;
#pragma unroll
      for (int k = 0; k < 7; ++k) idx += p >= off[k] ? gap[k] : 0;
      raw[i] = *LDSP(u32x3, TB + min(idx, 8 * SUBC - 1) * 16);
      if (64 * i + lane >= cnt) raw[i][0] = SENT;
    }
#pragma unroll
    for (int i = 0; i < 2; ++i) *LDSP(unsigned, LIST + (64 * i + lane) * 4) = raw[i][0] & 0xffffffu;
    const int pe = (l31 & 19) | ((l31 & 4) << 1) | ((l31 & 8) >> 1);
    *LDSP(unsigned, LIST + (32 * ntile + pe) * 4) = l31 < GN ? (unsigned)(node0 + wave * GN + l31) * 8u : 0xffffffu;
    const int drow = lane >> 3;
    const unsigned dchunk = 128u + 16u * ((lane & 7) ^ (drow & 2));
    const unsigned WL = L0 + L_W + lane * 16;
    auto issue_h = [&](int t) {
      const unsigned se = *LDSP(unsigned, LIST + (32 * t + pe) * 4);
      asm volatile("buffer_load_dwordx4 %0, %4, %5, 0 offen\n\tbuffer_load_dwordx4 %1, %4, %5, 0 offen offset:16\n\t"
                   "buffer_load_dwordx4 %2, %4, %5, 0 offen offset:32\n\tbuffer_load_dwordx4 %3, %4, %5, 0 offen offset:48"
                   : "=&v"(hN[0]), "=&v"(hN[1]), "=&v"(hN[2]), "=&v"(hN[3]) : "v"((se << (t < ntile ? 4 : 6)) + 64u * h), "s"(t < ntile ? rs_h : rs_sv) : "memory");
    };
    auto wait_h = [&]() {
      asm volatile("s_waitcnt vmcnt(12)" : "+v"(hN[0]), "+v"(hN[1]), "+v"(hN[2]), "+v"(hN[3])::"memory");
    };
    unsigned vo[4];
    auto prep_v = [&](int t) {
#pragma unroll
      for (int i = 0; i < 4; ++i) vo[i] = *LDSP(unsigned, LIST + (32 * t + 8 * i + drow) * 4) * 64u + dchunk;
    };
    auto issue_v = [&](int k) {
      asm volatile("s_waitcnt lgkmcnt(0)" ::: "memory");
      dma16x4(rs_sv, vo, 128 * k, TB + 4096 * k);
    };
    setprio(3);
    prep_v(0);
    issue_h(0);
    issue_v(0); issue_v(1); issue_v(2);
    setprio(ntile);
    STAMP(5);
    SB();
    int gt = cw + __builtin_amdgcn_update_dpp(0, cw, 0x128, 0xf, 0xf, false);
    gt += __builtin_amdgcn_ds_bpermute((lane ^ 16) * 4, gt); gt += __builtin_amdgcn_ds_bpermute((lane ^ 32) * 4, gt);
    const bool more2 = __builtin_amdgcn_ballot_w64(gt > CAP * (r2 + 1)) != 0;
    const bool moreb = __builtin_amdgcn_ballot_w64(nbv > batch + 1) != 0;
#pragma unroll
    for (int i = 0; i < 2; ++i) {
      if (i == 1 && cnt <= 64) break;
      const int p = 64 * i + lane;
      const f32x4 pd = *LDSP(f32x4, L0 + L_DPOS + ((raw[i][0] >> 20) & 0xff0u));
      const unsigned xy = pk2(lo16(raw[i][1]) - pd[0], hi16(raw[i][1]) - pd[1]);
      const float z = rb(__uint_as_float(raw[i][2]) - pd[2]);
      const float x = lo16(xy), y = hi16(xy);
      const unsigned sq = pk2(x * x, y * y);
      const float q2 = rb(rb(lo16(sq) + hi16(sq) + rb(z * z)) + 1e-8f);
      const float dd = rb(__builtin_amdgcn_sqrtf(q2));
      const bool ok = dd < 5.0f;
      const float d = ok ? dd : 1.0f;
      const float inv = __builtin_amdgcn_rcpf(d);
      const float a = rb(rb(3.141592653589793f * d) * 0.2f);
      const float f = 0.5f * rb(rb(__builtin_amdgcn_cosf(a * 0.15915494309189535f)) + 1.0f);
      const unsigned hxy = pk2(x * inv, y * inv);
      const float hz = rb(z * inv);
      const unsigned fxy = pk2(f * lo16(hxy), f * hi16(hxy));
      const unsigned fzf = pk2(f * hz, f);
      *LDSP(u16, LIST + O_D + p * 2) = (u16)(__float_as_uint(d) >> 16);
      *LDSP(u16, LIST + O_DL + p * 2) = ok ? (u16)(raw[i][0] >> 24) : (u16)0xffff;
      *LDSP(u16, LIST + O_F + p * 2) = (u16)(fzf >> 16);
      *LDSP(u16, LIST + O_FX + p * 2) = (u16)fxy;
      *LDSP(u16, LIST + O_FX + 256 + p * 2) = (u16)(fxy >> 16);
      *LDSP(u16, LIST + O_FX + 512 + p * 2) = (u16)fzf;
    }
    SB();
    wait_h();
    STAMP(6);
    const int q4 = (lane >> 2) & 3, pp = lane & 3, chh = (lane >> 4) & 1;
    const unsigned trb = TB + (8 * h + q4) * 128 + 64 * (pp & 1) + 16 * chh + 8 * (pp >> 1);
    const unsigned trb0 = trb + 32 * (q4 >> 1), trb1 = trb + 32 * ((q4 >> 1) ^ 1);
    const unsigned mypair = (unsigned)(wave * 32 + l31) * 0x10001u;
    unsigned la = LIST + 16 * h, ld = LIST + O_D + 2 * pe;
    const unsigned lf = L0 + L_FR + 32 * h;
    for (int t = 0; t < ntile; ++t) {
      u32x4 Wb[5];
      auto ldW = [&](int ct) {
#pragma unroll
        for (int ks = 0; ks < 4; ++ks) Wb[ks] = *LDSP(u32x4, WL + (4 * ct + ks) * 1024);
        Wb[4] = *LDSP(u32x4, WL + (24 + ct) * 1024);
      };
      asm volatile("" : "+v"(la), "+v"(ld));
      const float d = lo16(*LDSP(u16, ld));
      const f32x4 f0 = *LDSP(f32x4, lf), f1 = *LDSP(f32x4, lf + 16);
      ldW(4);
      u32x4 msk[2], S1[2];
#pragma unroll
      for (int s = 0; s < 2; ++s) { msk[s] = *LDSP(u32x4, la + O_DL + 32 * s); S1[s] = *LDSP(u32x4, la + O_F + 32 * s); }
      SB();
      f32x16 ph, w;
      u32x4 rbf;
      auto gemm_p = [&]() {
        ph = mfma(hN[0], Wb[0], zero16());
#pragma unroll
        for (int ks = 1; ks < 4; ++ks) ph = mfma(hN[ks], Wb[ks], ph);
      };
      auto gemm = [&]() { gemm_p(); w = mfma(rbf, Wb[4], zero16()); };
      gemm_p();
      {
        const float inv = __builtin_amdgcn_rcpf(d);
#pragma unroll
        for (int i = 0; i < 4; ++i) {
          const float fa = i < 2 ? f0[2 * i] : f1[2 * i - 4], fb = i < 2 ? f0[2 * i + 1] : f1[2 * i - 3];
          const unsigned x = pk2(d * fa, d * fb);
          float ra = lo16(x) * 0.15915494309189535f, rc = hi16(x) * 0.15915494309189535f;
          ra -= __builtin_rintf(ra); rc -= __builtin_rintf(rc);
          rbf[i] = pk2(__builtin_amdgcn_sinf(ra) * inv, __builtin_amdgcn_sinf(rc) * inv);
        }
      }
#pragma unroll
      for (int i = 0; i < 4; ++i) {
        __builtin_amdgcn_sched_group_barrier(0x8, 1, 0);
        __builtin_amdgcn_sched_group_barrier(0x402, 12, 0);
      }
      SB();
      w = mfma(rbf, Wb[4], zero16());
      SB();
      {
        u32x4 sr[3][2];
#pragma unroll
        for (int s = 0; s < 2; ++s)
#pragma unroll
          for (int k = 0; k < 3; ++k) sr[k][s] = *LDSP(u32x4, la + O_FX + 256 * k + 32 * s);
#pragma unroll
        for (int s = 0; s < 2; ++s) { msk[s] = eqmask4(msk[s], mypair, 0x00010001u); S1[s] &= msk[s]; }
        SB();
#pragma unroll
        for (int t2 = 0; t2 < 2; ++t2) {
          const f32x16 m0 = ph * w;
          const u32x4 a0 = pack8(m0, 0), a1 = pack8(m0, 1);
          if (t2 == 0) { SB(); ldW(5); SB(); }
#pragma unroll
          for (int k = 0; k < 3; ++k) { if (t2 == 0) sr[k][0] &= msk[0]; acc[1 + k][t2] = mfma(a0, sr[k][0], acc[1 + k][t2]); }
#pragma unroll
          for (int k = 0; k < 3; ++k) { if (t2 == 0) sr[k][1] &= msk[1]; acc[1 + k][t2] = mfma(a1, sr[k][1], acc[1 + k][t2]); }
          SB();
          gemm();
          SB();
          ldW(t2);
          SB();
        }
      }
      {
        const f32x16 ms = ph * w;
        const u32x4 b0 = pack8(ms, 0), b1 = pack8(ms, 1);
        SB();
        gemm();
        SB();
        ldW(2);
        SB();
        acc[0][0] = mfma(b0, S1[0], acc[0][0]);
        acc[0][0] = mfma(b1, S1[1], acc[0][0]);
        SB();
      }
      f32x16 mv[2];
      {
        const f32x16 ms = ph * w;
        const u32x4 b0 = pack8(ms, 0), b1 = pack8(ms, 1);
        SB();
        gemm();
        SB();
        ldW(3);
        prep_v(t + 1);
        SB();
        acc[0][1] = mfma(b0, S1[0], acc[0][1]);
        acc[0][1] = mfma(b1, S1[1], acc[0][1]);
        SB();
        mv[0] = ph * w;
        SB();
        gemm();
        SB();
        issue_h(t + 1);
        SB();
      }
      asm volatile("s_waitcnt vmcnt(4)" ::: "memory");
      if (t < 3) STAMP(7 + 2 * t);
      u32x2 va[4], vb[4], vc[4];
      auto ldv = [&](u32x2 (&v)[4], int t2, int k) {
#pragma unroll
        for (int bb = 0; bb < 4; ++bb) v[bb] = trread((t2 ? trb1 : trb0) + k * 4096 + (16 * (bb >> 1) + 4 * (bb & 1)) * 128);
      };
      auto vv = [&](const u32x2 (&v)[4], const f32x16& m, f32x16& ac) {
        u32x4 a[2];
#pragma unroll
        for (int bb = 0; bb < 4; ++bb) {
          a[bb >> 1][2 * (bb & 1)] = pk2(m[4 * bb] * lo16(v[bb][0]), m[4 * bb + 1] * hi16(v[bb][0]));
          a[bb >> 1][2 * (bb & 1) + 1] = pk2(m[4 * bb + 2] * lo16(v[bb][1]), m[4 * bb + 3] * hi16(v[bb][1]));
        }
        ac = mfma(a[0], S1[0], ac);
        ac = mfma(a[1], S1[1], ac);
      };
      ldv(va, 0, 0); ldv(vb, 1, 0); ldv(vc, 0, 1);
      mv[1] = ph * w;
      SB();
      issue_v(0);
      vv(va, mv[0], acc[1][0]);
      SB();
      ldv(va, 1, 1);
      SB();
      vv(vb, mv[1], acc[1][1]);
      SB();
      issue_v(1);
      ldv(vb, 0, 2);
      SB();
      vv(vc, mv[0], acc[2][0]);
      SB();
      ldv(vc, 1, 2);
      SB();
      vv(va, mv[1], acc[2][1]);
      SB();
      issue_v(2);
      setprio(ntile - 1 - t);
      if (t < 3) STAMP(8 + 2 * t);
      vv(vb, mv[0], acc[3][0]);
      vv(vc, mv[1], acc[3][1]);
      SB();
      wait_h();
      la += 64; ld += 64;
    }
    if (more2) ++r2;
    else if (more1) { ++r1; r2 = 0; }
    else if (moreb) { ++batch; r1 = 0; r2 = 0; }
    else break;
    t = load_t();
    asm volatile("s_waitcnt vmcnt(0)" ::: "memory");
    wg_barrier();
  }
  STAMP(13);
  {
    FRESH_LANE;
    const int q4 = (lane >> 2) & 3, pp = lane & 3, chh = (lane >> 4) & 1;
    const unsigned trb = TB + (8 * h + q4) * 128 + 64 * (pp & 1) + 16 * chh + 8 * (pp >> 1);
    const unsigned trb0 = trb + 32 * (q4 >> 1), trb1 = trb + 32 * ((q4 >> 1) ^ 1);
    const int pe = (l31 & 19) | ((l31 & 4) << 1) | ((l31 & 8) >> 1);
    u32x4 id[2];
#pragma unroll
    for (int s = 0; s < 2; ++s) {
      const int e = pe - 8 * h - 16 * s;
      const unsigned one = (e & 1) ? 0x3f800000u : 0x3f80u;
#pragma unroll
      for (int i = 0; i < 4; ++i) id[s][i] = (e >> 1) == i ? one : 0u;
    }
#pragma unroll
    for (int i = 0; i < 4; ++i)
#pragma unroll
      for (int k = 0; k < 4; ++k) { acc[0][i >> 1][8 * (i & 1) + 2 * k] += lo16(hN[i][k]); acc[0][i >> 1][8 * (i & 1) + 2 * k + 1] += hi16(hN[i][k]); }
    SB();
    asm volatile("s_waitcnt vmcnt(0)" ::: "memory");
    u32x2 ov[3][2][4];
#pragma unroll
    for (int k = 0; k < 3; ++k)
#pragma unroll
      for (int t2 = 0; t2 < 2; ++t2)
#pragma unroll
        for (int bb = 0; bb < 4; ++bb) ov[k][t2][bb] = trread((t2 ? trb1 : trb0) + k * 4096 + (16 * (bb >> 1) + 4 * (bb & 1)) * 128);
    SB();
#pragma unroll
    for (int k = 0; k < 3; ++k)
#pragma unroll
      for (int t2 = 0; t2 < 2; ++t2)
#pragma unroll
        for (int s = 0; s < 2; ++s)
          acc[1 + k][t2] = mfma(u32x4{ov[k][t2][2 * s][0], ov[k][t2][2 * s][1], ov[k][t2][2 * s + 1][0], ov[k][t2][2 * s + 1][1]}, id[s], acc[1 + k][t2]);
    u32x4 o[16];
#pragma unroll
    for (int i = 0; i < 16; ++i) o[i] = pack8(acc[i >> 2][(i >> 1) & 1], i & 1);
    if (l31 < GN) {
      const unsigned wa = TB + l31 * 512 + 16 * ((4 * h) ^ (l31 & 7));
#pragma unroll
      for (int i = 0; i < 16; ++i) *LDSP(u32x4, (wa ^ (16 * (i & 3))) + 128 * (i >> 2)) = o[i];
    }
    const int gw = __builtin_amdgcn_readfirstlane(max(min(nb - wave * GN, GN), 0));
    const uint64_t op = (uint64_t)out + (size_t)(node0 + wave * GN) * 512;
    const __amdgpu_buffer_rsrc_t rb_o = __builtin_amdgcn_make_buffer_rsrc((void*)op, 0, gw * 512, 0x00020000);
    u32x4 v[(GN + 1) / 2];
#pragma unroll
    for (int i = 0; i < (GN + 1) / 2; ++i) v[i] = *LDSP(u32x4, TB + i * 1024 + lane * 16);
    const unsigned oa = h * 512 + 16 * (l31 ^ h);
#pragma unroll
    for (int i = 0; i < (GN + 1) / 2; ++i) __builtin_amdgcn_raw_buffer_store_b128(v[i], rb_o, oa ^ (32 * (i & 3)), i * 1024, K2AUX);
  }
  STAMP(14);
  STAMP(15);
}

#if defined(__HIP_DEVICE_COMPILE__)
#pragma clang attribute pop
#endif
extern "C" void kernel_launch(void* const* d_in, const int* in_sizes, int n_in, void* d_out, int out_size, void* d_ws, size_t ws_size,
                              hipStream_t stream) {
  const u16* sv = (const u16*)d_in[0];
  const u16* pos = (const u16*)d_in[1];
  const int* src = (const int*)d_in[2];
  const int* dst = (const int*)d_in[3];
  const u16* fr = (const u16*)d_in[4];
  const u16* w1 = (const u16*)d_in[5];
  const u16* w2 = (const u16*)d_in[6];
  const u16* wr = (const u16*)d_in[7];
  const int N = in_sizes[1] / 3, E = in_sizes[2];
  const int BN = (N + 255) / 256 < 2 ? 2 : (N + 255) / 256;
  const int B = (N + BN - 1) / BN, bpx = (B + 7) / 8;
  const int W = (E + CH - 1) / CH;
  const int HW = (N + 255) / 256;
  const unsigned magicBN = (unsigned)(0x100000000ull / (unsigned)BN) + 1u;
  char* ws = (char*)d_ws;
  (void)hipFuncSetAttribute((const void*)k_agg, hipFuncAttributeMaxDynamicSharedMemorySize, L_END);
  k_bin<<<W + HW + 4, 512, 0, stream>>>(src, dst, (unsigned*)(ws + WS_T), (u32x4*)(ws + WS_SEG), sv, pos, w1, w2, wr, (u32x4*)ws,
                                        (u32x2*)(ws + WS_P), ws + WS_H, N, E, W, HW, magicBN, BN);
  k_agg<<<8 * bpx, 512, L_END, stream>>>(sv, (const u32x2*)(ws + WS_P), fr, (const unsigned*)(ws + WS_T), (const u32x4*)(ws + WS_SEG),
                                         (const u32x4*)ws, ws + WS_H, (u16*)d_out, N, W, BN, B, bpx);
}
```

```cpp
#define CODEPF
#if defined(__HIP_DEVICE_COMPILE__)
#pragma clang attribute push(__attribute__((target("no-packed-fp32-ops"))), apply_to = function)
#endif
#include <hip/hip_runtime.h>
#include <stdint.h>
#include <type_traits>

#define STAG 2
#define PMODE 3
#define P_START 2
#define P_NEXT (1 - t)
#define P_PH2 __builtin_amdgcn_s_setprio(3)
#define TID __builtin_amdgcn_workitem_id_x()
#define BID __builtin_amdgcn_workgroup_id_x()
typedef unsigned short u16;
typedef __attribute__((ext_vector_type(8))) __bf16 bf16x8;
typedef __attribute__((ext_vector_type(2))) __bf16 bf16x2;
typedef __attribute__((ext_vector_type(4))) short s16x4;
typedef __attribute__((ext_vector_type(2))) float f32x2;
typedef __attribute__((ext_vector_type(4))) float f32x4;
typedef __attribute__((ext_vector_type(16))) float f32x16;
typedef __attribute__((ext_vector_type(4))) unsigned u32x4;
typedef __attribute__((ext_vector_type(3))) unsigned u32x3;
typedef __attribute__((ext_vector_type(2))) unsigned u32x2;
#define LDSP(T, off) ((__attribute__((address_space(3))) T*)(uintptr_t)(off))
#define DI __device__ __forceinline__
#define SB() __builtin_amdgcn_sched_barrier(0)

constexpr int CH = 4096;
constexpr int SEGQ = CH / 4 + 192;
constexpr int GN = 25;
constexpr int NFRAG = 30;
constexpr int SUBC = 32;
constexpr int CAP = 128;
constexpr int BQ = 192;
constexpr unsigned SENT = 0xFFFFFFFFu;
constexpr int L_W = 0;
constexpr int L_TB = NFRAG * 1024;
constexpr int TBS = GN * 512;
constexpr int L_LIST = L_TB + 8 * TBS;
constexpr int O_D = 640, O_DL = O_D + 256, O_F = O_DL + 256, O_FX = O_F + 256;
constexpr int O_RAW = O_FX + 768;
constexpr int LISTS = O_RAW + 8 * SUBC * 4;
constexpr int L_CNT = L_LIST + 8 * LISTS;
constexpr int L_NB = L_CNT + 256;
constexpr int L_FR = L_NB + 64;
constexpr int L_DPOS = L_FR + 64;
constexpr int L_END = L_DPOS + 256 * 16;
static_assert(L_END <= 163840 && L_TB % 512 == 0 && L_DPOS % 16 == 0 && LISTS % 16 == 0, "LDS");
constexpr size_t WS_T = 65536;
constexpr size_t WS_SEG = WS_T + 256 * 256 * 4;
constexpr size_t WS_P = WS_SEG + (size_t)256 * SEGQ * 16;
constexpr size_t WS_C = WS_P + 51200 * 8;
constexpr size_t WS_H = WS_C + 51200 * 2;
constexpr size_t WS_END = WS_H + 51200 * 128;
constexpr size_t WS_F = NFRAG * 1024;
static_assert(WS_END < 0xffffff, "ws");

#define STAMP(i)
#define STAMP1(i)
#define WAITV
DI float lo16(unsigned x) { return __uint_as_float(x << 16); }
DI float hi16(unsigned x) { return __uint_as_float(x & 0xffff0000u); }
DI unsigned pk2(float a, float b) { f32x2 v = {a, b}; return __builtin_bit_cast(unsigned, __builtin_convertvector(v, bf16x2)); }
DI float rb(float a) { return hi16(pk2(a, a)); }
DI f32x16 mfma(u32x4 a, u32x4 b, f32x16 c) {
  return __builtin_amdgcn_mfma_f32_32x32x16_bf16(__builtin_bit_cast(bf16x8, a), __builtin_bit_cast(bf16x8, b), c, 0, 0, 0);
}
DI f32x16 zero16() { f32x16 z;
#pragma unroll
  for (int r = 0; r < 16; ++r) z[r] = 0.f;
  return z; }
DI int wave_incl_scan(int v) {
  v += __builtin_amdgcn_update_dpp(0, v, 0x111, 0xf, 0xf, false);
  v += __builtin_amdgcn_update_dpp(0, v, 0x112, 0xf, 0xf, false);
  v += __builtin_amdgcn_update_dpp(0, v, 0x114, 0xf, 0xf, false);
  v += __builtin_amdgcn_update_dpp(0, v, 0x118, 0xf, 0xf, false);
  v += __builtin_amdgcn_update_dpp(0, v, 0x142, 0xa, 0xf, false);
  v += __builtin_amdgcn_update_dpp(0, v, 0x143, 0xc, 0xf, false);
  return v;
}
DI int chan_of(int t2, int i32) { return 32 * ((i32 >> 2) & 1) + 16 * t2 + 4 * (i32 >> 3) + (i32 & 3); }
DI u32x4 pack8(const f32x16& x, int s) {
  return u32x4{pk2(x[8 * s], x[8 * s + 1]), pk2(x[8 * s + 2], x[8 * s + 3]), pk2(x[8 * s + 4], x[8 * s + 5]), pk2(x[8 * s + 6], x[8 * s + 7])};
}
DI void setprio(int p) {
  if (p >= 3) __builtin_amdgcn_s_setprio(3);
  else if (p == 2) __builtin_amdgcn_s_setprio(2);
  else if (p == 1) __builtin_amdgcn_s_setprio(1);
  else __builtin_amdgcn_s_setprio(0);
}
DI u32x2 trread(unsigned a) { return __builtin_bit_cast(u32x2, __builtin_amdgcn_ds_read_tr16_b64_v4i16(LDSP(s16x4, a))); }
#define K1AUX 17
#define IDXAUX 0
#define K2AUX 17
DI void st16(void* base, unsigned off, u32x4 v) {
  const __amdgpu_buffer_rsrc_t r = __builtin_amdgcn_make_buffer_rsrc(base, 0, 0x7fffffff, 0x00020000);
  __builtin_amdgcn_raw_buffer_store_b128(v, r, off, 0, K1AUX);
}
DI unsigned qmod(float x) { return min((unsigned)(__builtin_amdgcn_fractf(x * 0.0625f) * 65536.0f), 65535u); }
DI unsigned opq(unsigned c) { asm volatile("" : "+v"(c)); return c; }
DI unsigned swap23(unsigned p) { return (p & ~12u) | ((p & 4u) << 1) | ((p & 8u) >> 1); }
DI void wg_barrier() { asm volatile("s_waitcnt lgkmcnt(0)\n\ts_barrier" ::: "memory"); }

DI void dma16(u32x4 rsrc, unsigned voff, unsigned ldsaddr) {
  asm volatile("s_mov_b32 m0, %2\n\ts_nop 3\n\tbuffer_load_dwordx4 %0, %1, 0 offen lds" ::"v"(voff), "s"(rsrc), "s"(ldsaddr) : "memory");
}
DI void dma16x4(u32x4 rsrc, const unsigned (&voff)[4], unsigned soff, unsigned m0) {
  asm volatile("s_mov_b32 m0, %6\n\ts_nop 3\n\tbuffer_load_dwordx4 %0, %4, %5 offen lds\n\t"
               "s_add_u32 m0, m0, 0x400\n\ts_nop 0\n\tbuffer_load_dwordx4 %1, %4, %5 offen lds\n\t"
               "s_add_u32 m0, m0, 0x400\n\ts_nop 0\n\tbuffer_load_dwordx4 %2, %4, %5 offen lds\n\t"
               "s_add_u32 m0, m0, 0x400\n\ts_nop 0\n\tbuffer_load_dwordx4 %3, %4, %5 offen lds"
               ::"v"(voff[0]), "v"(voff[1]), "v"(voff[2]), "v"(voff[3]), "s"(rsrc), "s"(soff), "s"(m0) : "memory", "scc");
}
constexpr int K1_CNT = 0, K1_STAGE = 8 * 256 * 4, K1_TOT = K1_STAGE + SEGQ * 16, K1_W1 = 0, K1_HT = 8192, K1_END = K1_HT + 8 * 4096;
extern "C" __global__ __launch_bounds__(512, 4) void k_bin(const int* __restrict__ src, const int* __restrict__ dst, unsigned* __restrict__ T,
                                                u32x4* __restrict__ seg, const u16* __restrict__ sv, const u16* __restrict__ pos,
                                                const u16* __restrict__ w1, const u16* __restrict__ w2, const u16* __restrict__ wr, const u16* __restrict__ freqs,
                                                u32x4* __restrict__ wf, u32x2* __restrict__ ptab, u16* __restrict__ ctab, char* __restrict__ htab, const char* outp, int N, int E,
                                                int W, int HW, unsigned magicBN, int BN, int WP, int NPX) {
  __shared__ __attribute__((aligned(16))) char lds_raw[K1_END];
  const unsigned L0 = (unsigned)(uintptr_t)(__attribute__((address_space(3))) char*)lds_raw;
  const int tid = TID, lane = tid & 63;
  const int wave = __builtin_amdgcn_readfirstlane(tid >> 6);
  int bid = BID;
  if (bid < WP + HW) { if (bid < WP) { if (bid >= W) return; bid += HW; } else bid -= WP; } else bid += W - WP;
  STAMP1(0);
  if (bid >= W + HW) {
    {
      uint64_t pc; unsigned len;
      asm volatile("s_getpc_b64 %0\npainn_code_lo:\n\ts_mov_b32 %1, painn_code_hi-painn_code_lo" : "=s"(pc), "=s"(len));
      const __amdgpu_buffer_rsrc_t rc = __builtin_amdgcn_make_buffer_rsrc((void*)pc, 0, len, 0x00020000);
      const unsigned x = __builtin_amdgcn_raw_buffer_load_b32(rc, tid * 64, 0, 0);
      asm volatile("" ::"v"(x));
    }
    const int idx = (bid - W - HW) * 512 + tid;
    if (idx >= NFRAG * 64) {
      if (idx < NFRAG * 64 + 16) ((unsigned*)wf)[WS_F / 4 + idx - NFRAG * 64] = (unsigned)freqs[idx - NFRAG * 64] << 16;
      return;
    }
    const int f = idx >> 6, i32 = lane & 31, h = lane >> 5;
    const u16* p; int s2;
    if (f < 24) { const int ct = f >> 2; p = w2 + (16 * (f & 3) + 4 * h) * 192 + 64 * (ct >> 1) + chan_of(ct & 1, i32); s2 = 8 * 192; }
    else { const int ct = f - 24; p = wr + 8 * h * 192 + 64 * (ct >> 1) + chan_of(ct & 1, i32); s2 = 4 * 192; }
    unsigned v[8];
#pragma unroll
    for (int j = 0; j < 8; ++j) v[j] = p[(j & 3) * 192 + (j >> 2) * s2];
    wf[idx] = u32x4{v[0] | v[1] << 16, v[2] | v[3] << 16, v[4] | v[5] << 16, v[6] | v[7] << 16};
    return;
  }
  if (bid < HW) {
    const int l31 = lane & 31, h = lane >> 5;
    const int nl = ((bid >> 3) * 8 + wave) * 32, nend = min(N, ((bid & 7) + 1) * NPX);
    const int n0 = nl < NPX ? (bid & 7) * NPX + nl : N;
    const int nn = min(n0 + l31, N - 1);
    const unsigned HT = L0 + K1_HT + wave * 4096;
    {
      const uint64_t sp = (uint64_t)sv, wp = (uint64_t)w1;
      const u32x4 rs_sv = {(unsigned)sp, (unsigned)(sp >> 32), (unsigned)N * 512u, 0x00020000u};
      const u32x4 rs_w1 = {(unsigned)wp, (unsigned)(wp >> 32), 8192u, 0x00020000u};
      const unsigned o = (unsigned)(n0 + (lane >> 3)) * 512u + 16u * ((lane & 7) ^ (lane >> 3));
      const unsigned v4[4] = {o, o + 4096u, o + 8192u, o + 12288u};
      dma16x4(rs_sv, v4, 0, HT);
      dma16(rs_w1, tid * 16u, L0 + K1_W1 + wave * 1024);
    }
    unsigned px = pos[nn * 3], py = pos[nn * 3 + 1], pz = pos[nn * 3 + 2];
    asm volatile("s_waitcnt vmcnt(0)\n\ts_barrier" : "+v"(px), "+v"(py), "+v"(pz)::"memory");
    if (h == 0 && n0 + l31 < nend) {
      ptab[n0 + l31] = u32x2{px | py << 16, pz << 16};
      ctab[n0 + l31] = (u16)(qmod(lo16(px)) >> 11 | (qmod(lo16(py)) >> 11) << 5 | (qmod(lo16(pz)) & 0xfc00u));
    }
    STAMP1(1); STAMP1(2);
    if (n0 >= N) return;
    u32x4 sf[4];
#pragma unroll
    for (int ks = 0; ks < 4; ++ks) sf[ks] = *LDSP(u32x4, HT + l31 * 128 + 16 * ((4 * h + ks) ^ (l31 & 7)));
    const unsigned ta = L0 + K1_W1 + (32 * h + ((lane >> 2) & 3)) * 128 + (16 * ((lane >> 4) & 1) + 4 * (lane & 3)) * 2;
#pragma unroll
    for (int ht = 0; ht < 2; ++ht) {
      f32x16 d1 = zero16();
#pragma unroll
      for (int ks = 0; ks < 4; ++ks) {
        const u32x2 a0 = trread(ta + ks * 1024 + ht * 64), a1 = trread(ta + ks * 1024 + 512 + ht * 64);
        d1 = mfma(u32x4{a0[0], a0[1], a1[0], a1[1]}, sf[ks], d1);
      }
#pragma unroll
      for (int r = 0; r < 16; ++r) { const float a = rb(d1[r]); d1[r] = a * __builtin_amdgcn_rcpf(1.0f + __builtin_amdgcn_exp2f(a * -1.4426950408889634f)); }
#pragma unroll
      for (int s = 0; s < 2; ++s) *LDSP(u32x4, HT + l31 * 128 + 16 * ((4 * h + 2 * ht + s) ^ (l31 & 7))) = pack8(d1, s);
    }
    STAMP1(3);
#pragma unroll
    for (int i = 0; i < 4; ++i) {
      const int n = 8 * i + (lane >> 3);
      const u32x4 v = *LDSP(u32x4, HT + i * 1024 + lane * 16);
      if (n0 + n < nend) st16(htab, (n0 + n) * 128 + 16 * ((lane & 7) ^ (n & 7)), v);
    }
    STAMP1(4); WAITV; STAMP1(5);
    return;
  }
  const __amdgpu_buffer_rsrc_t rb_s = __builtin_amdgcn_make_buffer_rsrc((void*)src, 0, E * 4, 0x00020000);
  const __amdgpu_buffer_rsrc_t rb_d = __builtin_amdgcn_make_buffer_rsrc((void*)dst, 0, E * 4, 0x00020000);
  const int cw = bid - HW;
  const int e0 = cw * CH + tid * 4;
  u32x4 s4[2], d4[2];
#pragma unroll
  for (int u = 0; u < 2; ++u) d4[u] = __builtin_amdgcn_raw_buffer_load_b128(rb_d, (e0 + u * 2048) * 4, 0, IDXAUX);
#pragma unroll
  for (int u = 0; u < 2; ++u) s4[u] = __builtin_amdgcn_raw_buffer_load_b128(rb_s, (e0 + u * 2048) * 4, 0, IDXAUX);
  *LDSP(u32x4, L0 + K1_CNT + wave * 1024 + lane * 16) = u32x4{0, 0, 0, 0};
#pragma unroll
  for (int i = 0; i < 3; ++i) if (i * 512 + tid < SEGQ) *LDSP(u32x4, L0 + K1_STAGE + (i * 512 + tid) * 16) = u32x4{SENT, SENT, SENT, SENT};
  WAITV; STAMP1(1);
  unsigned ca[8], hi8[8], rank[8];
#pragma unroll
  for (int k = 0; k < 8; ++k) {
    const unsigned d = d4[k >> 2][k & 3];
    const unsigned b = (unsigned)(((uint64_t)d * magicBN) >> 32);
    ca[k] = L0 + K1_CNT + wave * 1024 + b * 4;
    rank[k] = SENT;
    if (e0 + (k >> 2) * 2048 + (k & 3) < E) rank[k] = __hip_atomic_fetch_add(LDSP(unsigned, ca[k]), 1u, __ATOMIC_RELAXED, __HIP_MEMORY_SCOPE_WORKGROUP);
    const unsigned dl = d - b * (unsigned)BN;
    hi8[k] = dl + ((dl * 41u) >> 10) * 7u;
  }
  wg_barrier();
  STAMP1(2);
  if (wave == 0) {
    u32x4 c[8], tot = {0, 0, 0, 0};
#pragma unroll
    for (int w = 0; w < 8; ++w) { c[w] = *LDSP(u32x4, L0 + K1_CNT + w * 1024 + lane * 16); tot += c[w]; }
    const u32x4 q = (tot + 3u) >> 2;
    const unsigned ls = q[0] + q[1] + q[2] + q[3];
    const unsigned incl = wave_incl_scan(ls);
    u32x4 st; st[0] = incl - ls; st[1] = st[0] + q[0]; st[2] = st[1] + q[1]; st[3] = st[2] + q[2];
    u32x4 run = st * 4u;
#pragma unroll
    for (int w = 0; w < 8; ++w) { *LDSP(u32x4, L0 + K1_CNT + w * 1024 + lane * 16) = run; run += c[w]; }
    if (lane == 63) *LDSP(unsigned, L0 + K1_TOT) = incl;
    st16(T, cw * 1024 + 16 * lane, st | (q << 16));
  }
  wg_barrier();
  STAMP1(3);
  unsigned base[8];
#pragma unroll
  for (int k = 0; k < 8; ++k) base[k] = *LDSP(unsigned, ca[k]);
#pragma unroll
  for (int k = 0; k < 8; ++k)
    if (rank[k] != SENT) *LDSP(unsigned, L0 + K1_STAGE + (base[k] + rank[k]) * 4) = s4[k >> 2][k & 3] << 3 | hi8[k] << 24;
  const int total = *LDSP(int, L0 + K1_TOT);
  wg_barrier();
#pragma unroll
  for (int i = 0; i < 3; ++i) {
    const int qd = i * 512 + tid;
    if (qd < total) st16(seg, (cw * SEGQ + qd) * 16, *LDSP(u32x4, L0 + K1_STAGE + qd * 16));
  }
  STAMP1(4); WAITV; STAMP1(5);
}

DI u32x4 eqmask4(u32x4 a, unsigned b, unsigned ones) {
  u32x4 x;
#pragma unroll
  for (int i = 0; i < 4; ++i) x[i] = a[i] ^ b;
  asm("v_pk_min_u16 %0, %0, %4\n\tv_pk_min_u16 %1, %1, %4\n\tv_pk_min_u16 %2, %2, %4\n\tv_pk_min_u16 %3, %3, %4\n\t"
      "v_pk_sub_u16 %0, %0, %4\n\tv_pk_sub_u16 %1, %1, %4\n\tv_pk_sub_u16 %2, %2, %4\n\tv_pk_sub_u16 %3, %3, %4"
      : "+v"(x[0]), "+v"(x[1]), "+v"(x[2]), "+v"(x[3]) : "s"(ones));
  return x;
}

typedef __attribute__((ext_vector_type(2))) short s16x2;
extern "C" __global__ __launch_bounds__(512, 2) void k_agg(const u16* __restrict__ sv, const char* __restrict__ wf, u16* __restrict__ out, int N,
                                                           int W, int BN, int B, int bpx) {
  extern __shared__ __attribute__((aligned(512))) char lds_raw[];
  const int tid = TID, lane0 = tid & 63; (void)lane0;
  const int wave = __builtin_amdgcn_readfirstlane(tid >> 6);
  const int b = ((int)BID & 7) * bpx + ((int)BID >> 3);
  if (b >= B) return;
  unsigned long long* diag = (unsigned long long*)((char*)wf + (64 << 20)); (void)diag;
  STAMP(0);
  const unsigned L0 = (unsigned)(uintptr_t)(__attribute__((address_space(3))) char*)lds_raw;
  const int node0 = b * BN;
  const int nb = min(BN, N - node0);
  const uint64_t svp = (uint64_t)sv, wfp = (uint64_t)wf;
  const u32x4 rs_sv = {(unsigned)svp, (unsigned)(svp >> 32), (unsigned)N * 512u, 0x00020000u};
  const u32x4 rs_ws = {(unsigned)wfp, (unsigned)(wfp >> 32), (unsigned)WS_END, 0x00020000u};
#define FRESH_LANE int lane; asm volatile("v_mbcnt_lo_u32_b32 %0, -1, 0\n\tv_mbcnt_hi_u32_b32 %0, -1, %0" : "=v"(lane)); const int l31 = lane & 31, h = lane >> 5; (void)l31; (void)h
  const unsigned TB = L0 + L_TB + wave * TBS;

  f32x16 acc[4][2];
  u32x4 hN[4];
  int batch = 0, r1 = 0;
  bool first = true;
  const int grp = wave;
  while (true) {
    u32x4 ol[13];
    {
      FRESH_LANE;
      const unsigned MAP = L0 + L_LIST + wave * LISTS;
      const int cpw = (W + 7) >> 3;
      const int cell = wave * cpw + lane;
      unsigned t;
      asm volatile("s_nop 4\n\tbuffer_load_dword %0, %1, %2, 0 offen" : "=v"(t) : "v"((lane < cpw && cell < W) ? (int)WS_T + (cell * 256 + b) * 4 : 0x7fffffff), "s"(rs_ws) : "memory");
      __builtin_amdgcn_s_barrier();
      const unsigned lo = 16u * lane;
      const unsigned v4[4] = {lo, lo + 1024u, lo + 2048u, lo + 3072u};
      dma16x4(rs_ws, v4, WS_C + wave * 3 * 4096, L0 + L_TB + wave * 3 * 4096);
      u32x2 dp;
      asm volatile("s_nop 4\n\tbuffer_load_dwordx2 %0, %1, %2, 0 offen" : "=&v"(dp) : "v"((int)WS_P + (node0 + wave * GN + lane) * 8), "s"(rs_ws) : "memory");
      if (lane < 8) *LDSP(unsigned, L0 + L_CNT + lane * 32 + wave * 4) = lane >> 3;
      asm volatile("s_waitcnt vmcnt(5)" : "+v"(t)::"memory");
      const int q = t >> 16;
      const int incl = wave_incl_scan(q), excl = incl - q;
      const int total = __builtin_amdgcn_readlane(incl, 63);
      STAMP(1);
      const int win0 = batch * BQ;
      const int gq = (int)(WS_SEG / 16) + cell * SEGQ + (int)(t & 0xffffu) - excl;
      const int hi = min(incl, win0 + BQ);
#pragma nounroll
      for (int i = max(excl, win0); i < hi; ++i) *LDSP(int, MAP + (i - win0) * 4) = (gq + i) * 16;
      if (lane == 0) *LDSP(int, L0 + L_NB + wave * 4) = (total + BQ - 1) / BQ;
      const bool third = total > win0 + 128;
      int mq[3];
#pragma unroll
      for (int s = 0; s < 3; ++s) mq[s] = *LDSP(int, MAP + (64 * s + lane) * 4);
      u32x4 r4[3];
#pragma unroll
      for (int s = 0; s < 3; ++s) if (win0 + 64 * s + lane >= total) mq[s] = 0x7fffffff;
      asm volatile("s_nop 4\n\tbuffer_load_dwordx4 %0, %3, %6, 0 offen\n\tbuffer_load_dwordx4 %1, %4, %6, 0 offen\n\tbuffer_load_dwordx4 %2, %5, %6, 0 offen"
                   : "=&v"(r4[0]), "=&v"(r4[1]), "=&v"(r4[2]) : "v"(mq[0]), "v"(mq[1]), "v"(mq[2]), "s"(rs_ws) : "memory");
#pragma unroll
      for (int i = 1; i < 3; ++i) dma16x4(rs_ws, v4, WS_C + (wave * 3 + i) * 4096, L0 + L_TB + (wave * 3 + i) * 4096);
      dma16x4(rs_ws, v4, wave == 7 ? WS_C + 24 * 4096 : wave * 4096, L0 + (wave == 7 ? L_TB + 24 * 4096 : L_W + wave * 4096));
      asm volatile("s_waitcnt vmcnt(15)" : "+v"(dp)::"memory");
      if (wave < 2) dma16(rs_ws, (28 + wave) * 1024u + lo, L0 + L_W + (28 + wave) * 1024);
      if (wave == 2 && lane < 4) dma16(rs_ws, WS_F + lo, L0 + L_FR);
      const unsigned oo = first ? (unsigned)(node0 + wave * GN + h) * 512u + 16u * (l31 ^ h) : 0x7fff0000u;
#define OWNA(i) ((oo ^ (32u * ((i) & 7))) + 1024u * (i))
#define OWN3(j) asm volatile("s_nop 4\n\tbuffer_load_dwordx4 %0, %3, %6, 0 offen\n\tbuffer_load_dwordx4 %1, %4, %6, 0 offen\n\tbuffer_load_dwordx4 %2, %5, %6, 0 offen" \
                             : "=&v"(ol[3 * j]), "=&v"(ol[3 * j + 1]), "=&v"(ol[3 * j + 2]) \
                             : "v"(OWNA(3 * j)), "v"(OWNA(3 * j + 1)), "v"(OWNA(3 * j + 2)), "s"(rs_sv) : "memory")
      OWN3(0);
      if (lane < 32) {
        const unsigned qx = (qmod(lo16(dp[0])) - 1024u) & 0xffffu, qy = qmod(hi16(dp[0])) - 1024u, qz = (qmod(__uint_as_float(dp[1])) - 512u) & 0xffffu;
        *LDSP(u32x4, L0 + L_DPOS + (32 * wave + lane) * 16) = lane < GN ? u32x4{dp[0], dp[1], qx | qy << 16, qz} : u32x4{0x7fc07fc0u, 0x7fc00000u, 0x80008000u, 0x8000u};
      }
      asm volatile("s_waitcnt vmcnt(3)" : "+v"(r4[0]), "+v"(r4[1]), "+v"(r4[2])::"memory");
#pragma unroll
      for (int s = 0; s < 3; ++s) if (win0 + 64 * s + lane >= total) r4[s] = u32x4{SENT, SENT, SENT, SENT};
      STAMP(2);
      wg_barrier();
      OWN3(1);
      const unsigned one = opq(1u);
#pragma unroll
      for (int s = 0; s < 3; ++s) {
        if (s == 2 && !third) break;
        if (s == 1) OWN3(2);
        unsigned cd[4];
        u32x2 dq[4];
#pragma unroll
        for (int c = 0; c < 4; ++c) {
          cd[c] = *LDSP(u16, L0 + L_TB + min((r4[s][c] >> 2) & 0x3fffffu, 102398u));
          dq[c] = *LDSP(u32x2, L0 + L_DPOS + 8 + ((r4[s][c] >> 20) & 0xff0u));
        }
        unsigned rank[4];
#pragma unroll
        for (int c = 0; c < 4; ++c) {
          const unsigned cxy = ((cd[c] & 0x3ffu) * 0x400800u) & 0xf800f800u, cz = cd[c] & 0xfc00u;
          const unsigned qxy = dq[c][0], qz = dq[c][1];
          const s16x2 dxy = __builtin_bit_cast(s16x2, qxy) - __builtin_bit_cast(s16x2, cxy);
          const s16x2 dz = __builtin_bit_cast(s16x2, qz) - __builtin_bit_cast(s16x2, cz);
          const unsigned d2 = __builtin_amdgcn_sdot2(dxy, dxy, __builtin_amdgcn_sdot2(dz, dz, 0, false), false);
          rank[c] = 0x7fffffffu;
          if ((d2 < 488400000u) & (r4[s][c] != SENT))
            rank[c] = __hip_atomic_fetch_add(LDSP(unsigned, L0 + L_CNT + (r4[s][c] >> 29) * 32 + wave * 4), one, __ATOMIC_RELAXED, __HIP_MEMORY_SCOPE_WORKGROUP);
        }
#pragma unroll
        for (int c = 0; c < 4; ++c) {
          const unsigned p = rank[c] - SUBC * r1;
          if (p < (unsigned)SUBC) *LDSP(unsigned, L0 + L_LIST + O_RAW + (r4[s][c] >> 29) * LISTS + (wave * SUBC + p) * 4) = r4[s][c];
        }
      }
      OWN3(3);
      asm volatile("s_nop 4\n\tbuffer_load_dwordx4 %0, %1, %2, 0 offen" : "=&v"(ol[12]) : "v"(h ? 0x7fff0000u : OWNA(12)), "s"(rs_sv) : "memory");
    }
    STAMP(3);
    wg_barrier();
    STAMP(4);
    bool more1, moreb;
    for (int r2 = 0;; ++r2) {
    FRESH_LANE;
    int tot = 0, off[8], gap[8];
    {
      u32x4 c0 = *LDSP(u32x4, L0 + L_CNT + (lane & 7) * 32), c1 = *LDSP(u32x4, L0 + L_CNT + (lane & 7) * 32 + 16);
      const int nbv = *LDSP(int, L0 + L_NB + (lane & 7) * 4);
      const unsigned mx = max(max(max(c0[0], c0[1]), max(c0[2], c0[3])), max(max(c1[0], c1[1]), max(c1[2], c1[3])));
      more1 = __builtin_amdgcn_ballot_w64(mx > (unsigned)(SUBC * (r1 + 1))) != 0;
      moreb = __builtin_amdgcn_ballot_w64(nbv > batch + 1) != 0;
#pragma unroll
      for (int i = 0; i < 8; ++i) {
        const int c = min(max(__builtin_amdgcn_readlane(i < 4 ? c0[i & 3] : c1[i & 3], grp) - SUBC * r1, 0), SUBC);
        tot += c; off[i] = tot; gap[i] = SUBC - c;
      }
      tot = __builtin_amdgcn_readfirstlane(tot);
    }
    const unsigned LIST = L0 + L_LIST + grp * LISTS;
    int nact = 0;
    unsigned exy[4], ezd[4], ep[4];
    u32x4 own[16];
    {
      unsigned rec[4];
      u32x2 ps[4], pd[4];
#pragma unroll
      for (int i = 0; i < 4; ++i) {
        rec[i] = SENT;
        if (i < 2 || 64 * i < tot) {
          const int p = 64 * i + lane;
          int idx = p;
#pragma unroll
          for (int k = 0; k < 7; ++k) idx += p >= off[k] ? gap[k] : 0;
          rec[i] = *LDSP(unsigned, LIST + O_RAW + min(idx, 8 * SUBC - 1) * 4);
          if (p >= tot) rec[i] = SENT;
        }
      }
      {
        asm volatile("s_nop 4\n\tbuffer_load_dwordx2 %0, %4, %8, %9 offen\n\tbuffer_load_dwordx2 %1, %5, %8, %9 offen\n\t"
                     "buffer_load_dwordx2 %2, %6, %8, %9 offen\n\tbuffer_load_dwordx2 %3, %7, %8, %9 offen"
                     : "=&v"(ps[0]), "=&v"(ps[1]), "=&v"(ps[2]), "=&v"(ps[3])
                     : "v"(rec[0] & 0xffffffu), "v"(rec[1] & 0xffffffu), "v"(rec[2] & 0xffffffu), "v"(rec[3] & 0xffffffu), "s"(rs_ws), "s"((unsigned)WS_P) : "memory");
      }
#pragma unroll
      for (int i = 0; i < 4; ++i) pd[i] = *LDSP(u32x2, L0 + L_DPOS + ((rec[i] >> 20) & 0xff0u));
#pragma unroll
      for (int i = 0; i < 2; ++i) *LDSP(unsigned, LIST + (64 * i + lane) * 4) = opq(0xffffffu);
      if (lane < 32) *LDSP(unsigned, LIST + (128 + lane) * 4) = opq(0xffffffu);
      *LDSP(unsigned, LIST + O_D + lane * 4) = 0x3f803f80u;
      *LDSP(unsigned, LIST + O_DL + lane * 4) = 0xffffffffu;
      asm volatile("s_waitcnt vmcnt(4)" : "+v"(ol[0]), "+v"(ol[1]), "+v"(ol[2]), "+v"(ol[3]), "+v"(ol[4]), "+v"(ol[5]), "+v"(ol[6]), "+v"(ol[7]),
                   "+v"(ol[8]), "+v"(ol[9]), "+v"(ol[10]), "+v"(ol[11]), "+v"(ol[12])::"memory");
#pragma unroll
      for (int i = 0; i < 12; ++i) *LDSP(u32x4, TB + i * 1024 + lane * 16) = ol[i];
      if (h == 0) *LDSP(u32x4, TB + 12 * 1024 + lane * 16) = ol[12];
      {
        const unsigned ra = TB + l31 * 512 + 16 * ((4 * h) ^ (l31 & 15));
#pragma unroll
        for (int i = 0; i < 16; ++i) own[i] = *LDSP(u32x4, (ra ^ (16 * (8 * ((i >> 2) & 1) + (i & 3)))) + 256 * (i >> 3));
      }
    if (first) {
#pragma unroll
      for (int i = 0; i < 16; ++i)
#pragma unroll
        for (int k = 0; k < 4; ++k) {
          acc[i >> 2][(i >> 1) & 1][8 * (i & 1) + 2 * k] = lo16(own[i][k]);
          acc[i >> 2][(i >> 1) & 1][8 * (i & 1) + 2 * k + 1] = hi16(own[i][k]);
        }
      first = false;
    }
      SB();
      asm volatile("s_waitcnt vmcnt(0)" : "+v"(ps[0]), "+v"(ps[1]), "+v"(ps[2]), "+v"(ps[3])::"memory");
#pragma unroll
      for (int i = 0; i < 4; ++i) {
        exy[i] = 0; ezd[i] = 0; ep[i] = SENT;
        if (i >= 2 && 64 * i >= tot) continue;
        const unsigned xy = pk2(lo16(ps[i][0]) - lo16(pd[i][0]), hi16(ps[i][0]) - hi16(pd[i][0]));
        const float z = rb(__uint_as_float(ps[i][1]) - __uint_as_float(pd[i][1]));
        const float x = lo16(xy), y = hi16(xy);
        const unsigned sq = pk2(x * x, y * y);
        const float q2 = rb(rb(lo16(sq) + hi16(sq) + rb(z * z)) + 1e-8f);
        const unsigned zd = pk2(z, __builtin_amdgcn_sqrtf(q2));
        const bool ok = hi16(zd) < 5.0f;
        const uint64_t bm = __builtin_amdgcn_ballot_w64(ok);
        const unsigned p = __builtin_amdgcn_mbcnt_hi((unsigned)(bm >> 32), __builtin_amdgcn_mbcnt_lo((unsigned)bm, nact - CAP * r2));
        nact += __builtin_popcountll(bm);
        exy[i] = xy; ezd[i] = zd;
        if (ok & (p < (unsigned)CAP)) {
          ep[i] = p;
          *LDSP(unsigned, LIST + swap23(p) * 4) = rec[i] & 0xffffffu;
          *LDSP(u16, LIST + O_D + swap23(p) * 2) = (u16)(zd >> 16);
          *LDSP(u16, LIST + O_DL + p * 2) = (u16)(rec[i] >> 24);
        }
      }
    }
    const int cnt = min(nact - CAP * r2, CAP);
    const int ntile = (cnt + 31) >> 5;
    auto issue_h = [&](int t) {
      FRESH_LANE;
      const unsigned se = *LDSP(unsigned, LIST + (32 * t + l31) * 4);
      asm volatile("s_nop 4\n\tbuffer_load_dwordx4 %0, %4, %5, %6 offen\n\tbuffer_load_dwordx4 %1, %4, %5, %6 offen offset:16\n\t"
                   "buffer_load_dwordx4 %2, %4, %5, %6 offen offset:32\n\tbuffer_load_dwordx4 %3, %4, %5, %6 offen offset:48"
                   : "=&v"(hN[0]), "=&v"(hN[1]), "=&v"(hN[2]), "=&v"(hN[3]) : "v"(se * 16u + 64u * h), "s"(rs_ws), "s"((unsigned)WS_H) : "memory");
    };
    auto wait_h = [&]() {
      asm volatile("s_waitcnt vmcnt(12)" : "+v"(hN[0]), "+v"(hN[1]), "+v"(hN[2]), "+v"(hN[3])::"memory");
    };
    unsigned vo[4];
    auto prep_v = [&](int t) {
      FRESH_LANE;
      const int drow = lane >> 3;
      const unsigned dchunk = 16u * (((lane & 7) ^ (drow & 2)) + 8);
#pragma unroll
      for (int i = 0; i < 4; ++i) vo[i] = *LDSP(unsigned, LIST + (32 * t + 8 * i + drow) * 4) * 64u + dchunk;
    };
    auto issue_v = [&](int k) {
      asm volatile("s_waitcnt lgkmcnt(0)" ::: "memory");
      dma16x4(rs_sv, vo, 128 * k, TB + 4096 * k);
    };
    setprio(3);
    prep_v(0);
    issue_h(0);
    STAMP(5);
    if (r2 == 0) __builtin_amdgcn_s_barrier();
    const bool late = STAG >= 1 && r2 == 0 && wave >= 4;
    if (!late) {
      issue_v(0); issue_v(1); issue_v(2);
      if (STAG >= 1 && r2 == 0) __builtin_amdgcn_s_barrier();
    }
    setprio(P_START);
    SB();
#pragma unroll
    for (int i = 0; i < 4; ++i) {
      if (i >= 2 && 64 * i >= tot) break;
      const float d = hi16(ezd[i]);
      const float inv = __builtin_amdgcn_rcpf(d);
      const float a = rb(rb(3.141592653589793f * d) * 0.2f);
      const float f = 0.5f * rb(rb(__builtin_amdgcn_cosf(a * 0.15915494309189535f)) + 1.0f);
      const unsigned hxy = pk2(lo16(exy[i]) * inv, hi16(exy[i]) * inv);
      const float hz = rb(lo16(ezd[i]) * inv);
      const unsigned fxy = pk2(f * lo16(hxy), f * hi16(hxy));
      const unsigned fzf = pk2(f * hz, f);
      if (ep[i] != SENT) {
        const unsigned a2 = LIST + ep[i] * 2;
        *LDSP(u16, a2 + O_F) = (u16)(fzf >> 16);
        *LDSP(u16, a2 + O_FX) = (u16)fxy;
        *LDSP(u16, a2 + O_FX + 256) = (u16)(fxy >> 16);
        *LDSP(u16, a2 + O_FX + 512) = (u16)fzf;
      }
    }
    SB();
    if (late) {
      __builtin_amdgcn_s_barrier();
      issue_v(0); issue_v(1); issue_v(2);
    }
    wait_h();
    STAMP(6);
    auto tile = [&](int t, auto nsc) {
      constexpr int NS = decltype(nsc)::value;
      FRESH_LANE;
      const unsigned WL = L0 + L_W + lane * 16, la = LIST + 64 * t + 16 * h, lf = L0 + L_FR + 32 * h;
      const unsigned mypair = (unsigned)(grp * 32 + l31) * 0x10001u;
      u32x4 Wb[5];
      auto ldW = [&](int ct) {
#pragma unroll
        for (int ks = 0; ks < 4; ++ks) Wb[ks] = *LDSP(u32x4, WL + (4 * ct + ks) * 1024);
        Wb[4] = *LDSP(u32x4, WL + (24 + ct) * 1024);
      };
      const float d = lo16(*LDSP(u16, LIST + O_D + 64 * t + 2 * l31));
      const f32x4 f0 = *LDSP(f32x4, lf), f1 = *LDSP(f32x4, lf + 16);
      ldW(4);
      u32x4 msk[2], S1[2];
#pragma unroll
      for (int s = 0; s < NS; ++s) { msk[s] = *LDSP(u32x4, la + O_DL + 32 * s); S1[s] = *LDSP(u32x4, la + O_F + 32 * s); }
      SB();
      f32x16 ph, w;
      u32x4 rbf;
      auto gemm_p = [&]() {
        ph = mfma(hN[0], Wb[0], zero16());
#pragma unroll
        for (int ks = 1; ks < 4; ++ks) ph = mfma(hN[ks], Wb[ks], ph);
      };
      auto gemm = [&]() { gemm_p(); w = mfma(rbf, Wb[4], zero16()); };
      gemm_p();
      {
        const float inv = __builtin_amdgcn_rcpf(d);
#pragma unroll
        for (int i = 0; i < 4; ++i) {
          const float fa = i < 2 ? f0[2 * i] : f1[2 * i - 4], fb = i < 2 ? f0[2 * i + 1] : f1[2 * i - 3];
          const unsigned x = pk2(d * fa, d * fb);
          float ra = lo16(x) * 0.15915494309189535f, rc = hi16(x) * 0.15915494309189535f;
          ra -= __builtin_rintf(ra); rc -= __builtin_rintf(rc);
          rbf[i] = pk2(__builtin_amdgcn_sinf(ra) * inv, __builtin_amdgcn_sinf(rc) * inv);
        }
      }
#pragma unroll
      for (int i = 0; i < 4; ++i) {
        __builtin_amdgcn_sched_group_barrier(0x8, 1, 0);
        __builtin_amdgcn_sched_group_barrier(0x402, 12, 0);
      }
      SB();
      w = mfma(rbf, Wb[4], zero16());
      SB();
      {
        u32x4 sr[3][2];
#pragma unroll
        for (int s = 0; s < NS; ++s)
#pragma unroll
          for (int k = 0; k < 3; ++k) sr[k][s] = *LDSP(u32x4, la + O_FX + 256 * k + 32 * s);
#pragma unroll
        for (int s = 0; s < NS; ++s) { msk[s] = eqmask4(msk[s], mypair, 0x00010001u); S1[s] &= msk[s]; }
        SB();
#pragma unroll
        for (int t2 = 0; t2 < 2; ++t2) {
          const f32x16 m0 = ph * w;
          u32x4 a[2];
#pragma unroll
          for (int s = 0; s < NS; ++s) a[s] = pack8(m0, s);
          if (t2 == 0) { SB(); ldW(5); SB(); }
#pragma unroll
          for (int s = 0; s < NS; ++s)
#pragma unroll
            for (int k = 0; k < 3; ++k) { if (t2 == 0) sr[k][s] &= msk[s]; acc[1 + k][t2] = mfma(a[s], sr[k][s], acc[1 + k][t2]); }
          SB();
          gemm();
          SB();
          ldW(t2);
          SB();
        }
      }
      f32x16 mv[2];
#pragma unroll
      for (int t2 = 0; t2 < 2; ++t2) {
        const f32x16 ms = ph * w;
        u32x4 a[2];
#pragma unroll
        for (int s = 0; s < NS; ++s) a[s] = pack8(ms, s);
        SB();
        gemm();
        SB();
        ldW(2 + t2);
        SB();
#pragma unroll
        for (int s = 0; s < NS; ++s) acc[0][t2] = mfma(a[s], S1[s], acc[0][t2]);
        SB();
      }
      mv[0] = ph * w;
      SB();
      gemm();
      SB();
      issue_h(t + 1);
      prep_v(t + 1);
      unsigned tra;
      {
        FRESH_LANE;
        const int q4 = (lane >> 2) & 3, pp = lane & 3;
        tra = (q4 & 1) ? TB + (4 * h + (q4 >> 1)) * 128 + 64 * (pp & 1) + (lane & 16) + 8 * (pp >> 1) : 0x100000u;
      }
      SB();
      P_PH2;
      asm volatile("s_waitcnt vmcnt(4)" ::: "memory");
      if (t < 3) STAMP(7 + 2 * t);
      u32x2 va[8], vb[8];
      auto ldv = [&](u32x2 (&v)[8], int t2, int k) {
#pragma unroll
        for (int i = 0; i < 4 * NS; ++i)
          v[i] = trread(tra + k * 4096 + (8 * (i >> 1) + 2 * (i & 1)) * 128 + 32 * (t2 ^ (i & 1)));
      };
      auto vv = [&](const u32x2 (&v)[8], const f32x16& m, f32x16& ac) {
        u32x4 a[2];
#pragma unroll
        for (int i = 0; i < 4 * NS; ++i) {
          const unsigned v0 = v[i][0], v1 = v[i][1];
          a[i >> 2][i & 3] = pk2(m[2 * i] * __uint_as_float(v0), m[2 * i + 1] * __uint_as_float(v1));
        }
#pragma unroll
        for (int s = 0; s < NS; ++s) ac = mfma(a[s], S1[s], ac);
      };
      ldv(va, 0, 0); ldv(vb, 1, 0);
      mv[1] = ph * w;
      SB();
      vv(va, mv[0], acc[1][0]);
      SB();
      issue_v(0);
      ldv(va, 0, 1);
      SB();
      vv(vb, mv[1], acc[1][1]);
      SB();
      ldv(vb, 1, 1);
      SB();
      vv(va, mv[0], acc[2][0]);
      SB();
      issue_v(1);
      ldv(va, 0, 2);
      SB();
      vv(vb, mv[1], acc[2][1]);
      SB();
      ldv(vb, 1, 2);
      SB();
      vv(va, mv[0], acc[3][0]);
      SB();
      issue_v(2);
      if (t < 3) STAMP(8 + 2 * t);
      vv(vb, mv[1], acc[3][1]);
      SB();
      wait_h();
    };
    const int nfull = (cnt + 15) >> 5;
    for (int t = 0; t < nfull; ++t) { tile(t, std::integral_constant<int, 2>{}); setprio(P_NEXT); }
    if (nfull < ntile) tile(nfull, std::integral_constant<int, 1>{});
    if (nact <= CAP * (r2 + 1)) break;
    asm volatile("s_waitcnt vmcnt(0)" ::: "memory");
#pragma unroll
    for (int i = 0; i < 13; ++i) asm volatile("" : "=v"(ol[i]));
    }
    if (more1) ++r1;
    else if (moreb) { ++batch; r1 = 0; }
    else break;
    asm volatile("s_waitcnt vmcnt(0)" ::: "memory");
    wg_barrier();
  }
  STAMP(13);
  {
    FRESH_LANE;
    u32x4 o[16];
#pragma unroll
    for (int i = 0; i < 16; ++i) o[i] = pack8(acc[i >> 2][(i >> 1) & 1], i & 1);
    asm volatile("s_waitcnt vmcnt(0)" ::: "memory");
    if (l31 < GN) {
      const unsigned wa = TB + l31 * 512 + 16 * ((4 * h) ^ (l31 & 7));
#pragma unroll
      for (int i = 0; i < 16; ++i) *LDSP(u32x4, (wa ^ (16 * (i & 3))) + 128 * (i >> 2)) = o[i];
    }
    const int gw = __builtin_amdgcn_readfirstlane(max(min(nb - grp * GN, GN), 0));
    const uint64_t op = (uint64_t)out + (size_t)(node0 + grp * GN) * 512;
    const __amdgpu_buffer_rsrc_t rb_o = __builtin_amdgcn_make_buffer_rsrc((void*)op, 0, gw * 512, 0x00020000);
    u32x4 v[(GN + 1) / 2];
#pragma unroll
    for (int i = 0; i < (GN + 1) / 2; ++i) v[i] = *LDSP(u32x4, TB + i * 1024 + lane * 16);
    const unsigned oa = h * 512 + 16 * (l31 ^ h);
#pragma unroll
    for (int i = 0; i < (GN + 1) / 2; ++i) __builtin_amdgcn_raw_buffer_store_b128(v[i], rb_o, oa ^ (32 * (i & 3)), i * 1024, K2AUX);
  }
  asm volatile("painn_code_hi:");
  STAMP(14);
  STAMP(15);
}

#if defined(__HIP_DEVICE_COMPILE__)
#pragma clang attribute pop
#endif
extern "C" void kernel_launch(void* const* d_in, const int* in_sizes, int n_in, void* d_out, int out_size, void* d_ws, size_t ws_size,
                              hipStream_t stream) {
  const u16* sv = (const u16*)d_in[0];
  const u16* pos = (const u16*)d_in[1];
  const int* src = (const int*)d_in[2];
  const int* dst = (const int*)d_in[3];
  const u16* fr = (const u16*)d_in[4];
  const u16* w1 = (const u16*)d_in[5];
  const u16* w2 = (const u16*)d_in[6];
  const u16* wr = (const u16*)d_in[7];
  const int N = in_sizes[1] / 3, E = in_sizes[2];
  const int BN = (N + 255) / 256 < 2 ? 2 : (N + 255) / 256;
  const int B = (N + BN - 1) / BN, bpx = (B + 7) / 8;
  const int W = (E + CH - 1) / CH;
  const int NPX = bpx * BN, HW = 8 * ((NPX + 255) / 256), WP = (W + 7) & ~7;
  const unsigned magicBN = (unsigned)(0x100000000ull / (unsigned)BN) + 1u;
  char* ws = (char*)d_ws;
  (void)hipFuncSetAttribute((const void*)k_agg, hipFuncAttributeMaxDynamicSharedMemorySize, L_END);
  k_bin<<<WP + HW + 8, 512, 0, stream>>>(src, dst, (unsigned*)(ws + WS_T), (u32x4*)(ws + WS_SEG), sv, pos, w1, w2, wr, fr, (u32x4*)ws,
                                        (u32x2*)(ws + WS_P), (u16*)(ws + WS_C), ws + WS_H, (const char*)d_out, N, E, W, HW, magicBN, BN, WP, NPX);
  k_agg<<<8 * bpx, 512, L_END, stream>>>(sv, ws, (u16*)d_out, N, W, BN, B, bpx);
}
```

```cpp
#if defined(__HIP_DEVICE_COMPILE__)
#pragma clang attribute push(__attribute__((target("no-packed-fp32-ops"))), apply_to = function)
#endif
#include <hip/hip_runtime.h>
#include <stdint.h>
#include <type_traits>

#define STAG 2
#define PMODE 3
#define P_START 2
#define P_NEXT (1 - t)
#define P_PH2 __builtin_amdgcn_s_setprio(3)
#define TID __builtin_amdgcn_workitem_id_x()
#define BID __builtin_amdgcn_workgroup_id_x()
typedef unsigned short u16;
typedef __attribute__((ext_vector_type(8))) __bf16 bf16x8;
typedef __attribute__((ext_vector_type(2))) __bf16 bf16x2;
typedef __attribute__((ext_vector_type(4))) short s16x4;
typedef __attribute__((ext_vector_type(2))) float f32x2;
typedef __attribute__((ext_vector_type(4))) float f32x4;
typedef __attribute__((ext_vector_type(16))) float f32x16;
typedef __attribute__((ext_vector_type(4))) unsigned u32x4;
typedef __attribute__((ext_vector_type(3))) unsigned u32x3;
typedef __attribute__((ext_vector_type(2))) unsigned u32x2;
#define LDSP(T, off) ((__attribute__((address_space(3))) T*)(uintptr_t)(off))
#define DI __device__ __forceinline__
#define SB() __builtin_amdgcn_sched_barrier(0)

constexpr int CH = 4096;
constexpr int SEGQ = CH / 4 + 192;
constexpr int GN = 25;
constexpr int NFRAG = 30;
constexpr int SUBC = 32;
constexpr int CAP = 128;
constexpr int BQ = 192;
constexpr unsigned SENT = 0xFFFFFFFFu;
constexpr int L_W = 0;
constexpr int L_TB = NFRAG * 1024;
constexpr int TBS = GN * 512;
constexpr int L_LIST = L_TB + 8 * TBS;
constexpr int O_D = 640, O_DL = O_D + 256, O_F = O_DL + 256, O_FX = O_F + 256;
constexpr int O_RAW = O_FX + 768;
constexpr int LISTS = O_RAW + 8 * SUBC * 4;
constexpr int L_CNT = L_LIST + 8 * LISTS;
constexpr int L_NB = L_CNT + 256;
constexpr int L_FR = L_NB + 64;
constexpr int L_DPOS = L_FR + 64;
constexpr int L_END = L_DPOS + 256 * 16;
static_assert(L_END <= 163840 && L_TB % 512 == 0 && L_DPOS % 16 == 0 && LISTS % 16 == 0, "LDS");
constexpr size_t WS_T = 65536;
constexpr size_t WS_SEG = WS_T + 256 * 256 * 4;
constexpr size_t WS_P = WS_SEG + (size_t)256 * SEGQ * 16;
constexpr size_t WS_C = WS_P + 51200 * 8;
constexpr size_t WS_H = WS_C + 51200 * 2;
constexpr size_t WS_END = WS_H + 51200 * 128;
constexpr size_t WS_F = NFRAG * 1024;
static_assert(WS_END < 0xffffff, "ws");

#define STAMP(i)
#define STAMP1(i)
#define WAITV
DI float lo16(unsigned x) { return __uint_as_float(x << 16); }
DI float hi16(unsigned x) { return __uint_as_float(x & 0xffff0000u); }
DI unsigned pk2(float a, float b) { f32x2 v = {a, b}; return __builtin_bit_cast(unsigned, __builtin_convertvector(v, bf16x2)); }
DI float rb(float a) { return hi16(pk2(a, a)); }
DI f32x16 mfma(u32x4 a, u32x4 b, f32x16 c) {
  return __builtin_amdgcn_mfma_f32_32x32x16_bf16(__builtin_bit_cast(bf16x8, a), __builtin_bit_cast(bf16x8, b), c, 0, 0, 0);
}
DI f32x16 zero16() { f32x16 z;
#pragma unroll
  for (int r = 0; r < 16; ++r) z[r] = 0.f;
  return z; }
DI int wave_incl_scan(int v) {
  v += __builtin_amdgcn_update_dpp(0, v, 0x111, 0xf, 0xf, false);
  v += __builtin_amdgcn_update_dpp(0, v, 0x112, 0xf, 0xf, false);
  v += __builtin_amdgcn_update_dpp(0, v, 0x114, 0xf, 0xf, false);
  v += __builtin_amdgcn_update_dpp(0, v, 0x118, 0xf, 0xf, false);
  v += __builtin_amdgcn_update_dpp(0, v, 0x142, 0xa, 0xf, false);
  v += __builtin_amdgcn_update_dpp(0, v, 0x143, 0xc, 0xf, false);
  return v;
}
DI int chan_of(int t2, int i32) { return 32 * ((i32 >> 2) & 1) + 16 * t2 + 4 * (i32 >> 3) + (i32 & 3); }
DI u32x4 pack8(const f32x16& x, int s) {
  return u32x4{pk2(x[8 * s], x[8 * s + 1]), pk2(x[8 * s + 2], x[8 * s + 3]), pk2(x[8 * s + 4], x[8 * s + 5]), pk2(x[8 * s + 6], x[8 * s + 7])};
}
DI void setprio(int p) {
  if (p >= 3) __builtin_amdgcn_s_setprio(3);
  else if (p == 2) __builtin_amdgcn_s_setprio(2);
  else if (p == 1) __builtin_amdgcn_s_setprio(1);
  else __builtin_amdgcn_s_setprio(0);
}
DI u32x2 trread(unsigned a) { return __builtin_bit_cast(u32x2, __builtin_amdgcn_ds_read_tr16_b64_v4i16(LDSP(s16x4, a))); }
#define K1AUX 17
#define IDXAUX 0
#define K2AUX 17
DI void st16(void* base, unsigned off, u32x4 v) {
  const __amdgpu_buffer_rsrc_t r = __builtin_amdgcn_make_buffer_rsrc(base, 0, 0x7fffffff, 0x00020000);
  __builtin_amdgcn_raw_buffer_store_b128(v, r, off, 0, K1AUX);
}
DI unsigned qmod(float x) { return min((unsigned)(__builtin_amdgcn_fractf(x * 0.0625f) * 65536.0f), 65535u); }
DI unsigned opq(unsigned c) { asm volatile("" : "+v"(c)); return c; }
DI unsigned swap23(unsigned p) { return (p & ~12u) | ((p & 4u) << 1) | ((p & 8u) >> 1); }
DI void wg_barrier() { asm volatile("s_waitcnt lgkmcnt(0)\n\ts_barrier" ::: "memory"); }

DI void dma16(u32x4 rsrc, unsigned voff, unsigned ldsaddr) {
  asm volatile("s_mov_b32 m0, %2\n\ts_nop 3\n\tbuffer_load_dwordx4 %0, %1, 0 offen lds" ::"v"(voff), "s"(rsrc), "s"(ldsaddr) : "memory");
}
DI void dma16x4(u32x4 rsrc, const unsigned (&voff)[4], unsigned soff, unsigned m0) {
  asm volatile("s_mov_b32 m0, %6\n\ts_nop 3\n\tbuffer_load_dwordx4 %0, %4, %5 offen lds\n\t"
               "s_add_u32 m0, m0, 0x400\n\ts_nop 0\n\tbuffer_load_dwordx4 %1, %4, %5 offen lds\n\t"
               "s_add_u32 m0, m0, 0x400\n\ts_nop 0\n\tbuffer_load_dwordx4 %2, %4, %5 offen lds\n\t"
               "s_add_u32 m0, m0, 0x400\n\ts_nop 0\n\tbuffer_load_dwordx4 %3, %4, %5 offen lds"
               ::"v"(voff[0]), "v"(voff[1]), "v"(voff[2]), "v"(voff[3]), "s"(rsrc), "s"(soff), "s"(m0) : "memory", "scc");
}
constexpr int K1_CNT = 0, K1_STAGE = 8 * 256 * 4, K1_TOT = K1_STAGE + SEGQ * 16, K1_W1 = 0, K1_HT = 8192, K1_END = K1_HT + 8 * 4096;
extern "C" __global__ __launch_bounds__(512, 4) void k_bin(const int* __restrict__ src, const int* __restrict__ dst, unsigned* __restrict__ T,
                                                u32x4* __restrict__ seg, const u16* __restrict__ sv, const u16* __restrict__ pos,
                                                const u16* __restrict__ w1, const u16* __restrict__ w2, const u16* __restrict__ wr, const u16* __restrict__ freqs,
                                                u32x4* __restrict__ wf, u32x2* __restrict__ ptab, u16* __restrict__ ctab, char* __restrict__ htab, const char* outp, int N, int E,
                                                int W, int HW, unsigned magicBN, int BN, int WP, int NPX) {
  __shared__ __attribute__((aligned(16))) char lds_raw[K1_END];
  const unsigned L0 = (unsigned)(uintptr_t)(__attribute__((address_space(3))) char*)lds_raw;
  const int tid = TID, lane = tid & 63;
  const int wave = __builtin_amdgcn_readfirstlane(tid >> 6);
  int bid = BID;
  if (bid < WP + HW) { if (bid < WP) { if (bid >= W) return; bid += HW; } else bid -= WP; } else bid += W - WP;
  STAMP1(0);
  if (bid >= W + HW) {
    const int idx = (bid - W - HW) * 512 + tid;
    if (idx >= NFRAG * 64) {
      if (idx < NFRAG * 64 + 16) ((unsigned*)wf)[WS_F / 4 + idx - NFRAG * 64] = (unsigned)freqs[idx - NFRAG * 64] << 16;
      return;
    }
    const int f = idx >> 6, i32 = lane & 31, h = lane >> 5;
    const u16* p; int s2;
    if (f < 24) { const int ct = f >> 2; p = w2 + (16 * (f & 3) + 4 * h) * 192 + 64 * (ct >> 1) + chan_of(ct & 1, i32); s2 = 8 * 192; }
    else { const int ct = f - 24; p = wr + 8 * h * 192 + 64 * (ct >> 1) + chan_of(ct & 1, i32); s2 = 4 * 192; }
    unsigned v[8];
#pragma unroll
    for (int j = 0; j < 8; ++j) v[j] = p[(j & 3) * 192 + (j >> 2) * s2];
    wf[idx] = u32x4{v[0] | v[1] << 16, v[2] | v[3] << 16, v[4] | v[5] << 16, v[6] | v[7] << 16};
    return;
  }
  if (bid < HW) {
    const int l31 = lane & 31, h = lane >> 5;
    const int nl = ((bid >> 3) * 8 + wave) * 32, nend = min(N, ((bid & 7) + 1) * NPX);
    const int n0 = nl < NPX ? (bid & 7) * NPX + nl : N;
    const int nn = min(n0 + l31, N - 1);
    const unsigned HT = L0 + K1_HT + wave * 4096;
    {
      const uint64_t sp = (uint64_t)sv, wp = (uint64_t)w1;
      const u32x4 rs_sv = {(unsigned)sp, (unsigned)(sp >> 32), (unsigned)N * 512u, 0x00020000u};
      const u32x4 rs_w1 = {(unsigned)wp, (unsigned)(wp >> 32), 8192u, 0x00020000u};
      const unsigned o = (unsigned)(n0 + (lane >> 3)) * 512u + 16u * ((lane & 7) ^ (lane >> 3));
      const unsigned v4[4] = {o, o + 4096u, o + 8192u, o + 12288u};
      dma16x4(rs_sv, v4, 0, HT);
      dma16(rs_w1, tid * 16u, L0 + K1_W1 + wave * 1024);
    }
    unsigned px = pos[nn * 3], py = pos[nn * 3 + 1], pz = pos[nn * 3 + 2];
    asm volatile("s_waitcnt vmcnt(0)\n\ts_barrier" : "+v"(px), "+v"(py), "+v"(pz)::"memory");
    if (h == 0 && n0 + l31 < nend) {
      ptab[n0 + l31] = u32x2{px | py << 16, pz << 16};
      ctab[n0 + l31] = (u16)(qmod(lo16(px)) >> 11 | (qmod(lo16(py)) >> 11) << 5 | (qmod(lo16(pz)) & 0xfc00u));
    }
    STAMP1(1); STAMP1(2);
    if (n0 >= N) return;
    u32x4 sf[4];
#pragma unroll
    for (int ks = 0; ks < 4; ++ks) sf[ks] = *LDSP(u32x4, HT + l31 * 128 + 16 * ((4 * h + ks) ^ (l31 & 7)));
    const unsigned ta = L0 + K1_W1 + (32 * h + ((lane >> 2) & 3)) * 128 + (16 * ((lane >> 4) & 1) + 4 * (lane & 3)) * 2;
#pragma unroll
    for (int ht = 0; ht < 2; ++ht) {
      f32x16 d1 = zero16();
#pragma unroll
      for (int ks = 0; ks < 4; ++ks) {
        const u32x2 a0 = trread(ta + ks * 1024 + ht * 64), a1 = trread(ta + ks * 1024 + 512 + ht * 64);
        d1 = mfma(u32x4{a0[0], a0[1], a1[0], a1[1]}, sf[ks], d1);
      }
#pragma unroll
      for (int r = 0; r < 16; ++r) { const float a = rb(d1[r]); d1[r] = a * __builtin_amdgcn_rcpf(1.0f + __builtin_amdgcn_exp2f(a * -1.4426950408889634f)); }
#pragma unroll
      for (int s = 0; s < 2; ++s) *LDSP(u32x4, HT + l31 * 128 + 16 * ((4 * h + 2 * ht + s) ^ (l31 & 7))) = pack8(d1, s);
    }
    STAMP1(3);
#pragma unroll
    for (int i = 0; i < 4; ++i) {
      const int n = 8 * i + (lane >> 3);
      const u32x4 v = *LDSP(u32x4, HT + i * 1024 + lane * 16);
      if (n0 + n < nend) st16(htab, (n0 + n) * 128 + 16 * ((lane & 7) ^ (n & 7)), v);
    }
    STAMP1(4); WAITV; STAMP1(5);
    return;
  }
  const __amdgpu_buffer_rsrc_t rb_s = __builtin_amdgcn_make_buffer_rsrc((void*)src, 0, E * 4, 0x00020000);
  const __amdgpu_buffer_rsrc_t rb_d = __builtin_amdgcn_make_buffer_rsrc((void*)dst, 0, E * 4, 0x00020000);
  const int cw = bid - HW;
  const int e0 = cw * CH + tid * 4;
  u32x4 s4[2], d4[2];
#pragma unroll
  for (int u = 0; u < 2; ++u) d4[u] = __builtin_amdgcn_raw_buffer_load_b128(rb_d, (e0 + u * 2048) * 4, 0, IDXAUX);
#pragma unroll
  for (int u = 0; u < 2; ++u) s4[u] = __builtin_amdgcn_raw_buffer_load_b128(rb_s, (e0 + u * 2048) * 4, 0, IDXAUX);
  *LDSP(u32x4, L0 + K1_CNT + wave * 1024 + lane * 16) = u32x4{0, 0, 0, 0};
#pragma unroll
  for (int i = 0; i < 3; ++i) if (i * 512 + tid < SEGQ) *LDSP(u32x4, L0 + K1_STAGE + (i * 512 + tid) * 16) = u32x4{SENT, SENT, SENT, SENT};
  WAITV; STAMP1(1);
  unsigned ca[8], hi8[8], rank[8];
#pragma unroll
  for (int k = 0; k < 8; ++k) {
    const unsigned d = d4[k >> 2][k & 3];
    const unsigned b = (unsigned)(((uint64_t)d * magicBN) >> 32);
    ca[k] = L0 + K1_CNT + wave * 1024 + b * 4;
    rank[k] = SENT;
    if (e0 + (k >> 2) * 2048 + (k & 3) < E) rank[k] = __hip_atomic_fetch_add(LDSP(unsigned, ca[k]), 1u, __ATOMIC_RELAXED, __HIP_MEMORY_SCOPE_WORKGROUP);
    const unsigned dl = d - b * (unsigned)BN;
    hi8[k] = dl + ((dl * 41u) >> 10) * 7u;
  }
  wg_barrier();
  STAMP1(2);
  if (wave == 0) {
    u32x4 c[8], tot = {0, 0, 0, 0};
#pragma unroll
    for (int w = 0; w < 8; ++w) { c[w] = *LDSP(u32x4, L0 + K1_CNT + w * 1024 + lane * 16); tot += c[w]; }
    const u32x4 q = (tot + 3u) >> 2;
    const unsigned ls = q[0] + q[1] + q[2] + q[3];
    const unsigned incl = wave_incl_scan(ls);
    u32x4 st; st[0] = incl - ls; st[1] = st[0] + q[0]; st[2] = st[1] + q[1]; st[3] = st[2] + q[2];
    u32x4 run = st * 4u;
#pragma unroll
    for (int w = 0; w < 8; ++w) { *LDSP(u32x4, L0 + K1_CNT + w * 1024 + lane * 16) = run; run += c[w]; }
    if (lane == 63) *LDSP(unsigned, L0 + K1_TOT) = incl;
    st16(T, cw * 1024 + 16 * lane, st | (q << 16));
  }
  wg_barrier();
  STAMP1(3);
  unsigned base[8];
#pragma unroll
  for (int k = 0; k < 8; ++k) base[k] = *LDSP(unsigned, ca[k]);
#pragma unroll
  for (int k = 0; k < 8; ++k)
    if (rank[k] != SENT) *LDSP(unsigned, L0 + K1_STAGE + (base[k] + rank[k]) * 4) = s4[k >> 2][k & 3] << 3 | hi8[k] << 24;
  const int total = *LDSP(int, L0 + K1_TOT);
  wg_barrier();
#pragma unroll
  for (int i = 0; i < 3; ++i) {
    const int qd = i * 512 + tid;
    if (qd < total) st16(seg, (cw * SEGQ + qd) * 16, *LDSP(u32x4, L0 + K1_STAGE + qd * 16));
  }
  STAMP1(4); WAITV; STAMP1(5);
}

DI u32x4 eqmask4(u32x4 a, unsigned b, unsigned ones) {
  u32x4 x;
#pragma unroll
  for (int i = 0; i < 4; ++i) x[i] = a[i] ^ b;
  asm("v_pk_min_u16 %0, %0, %4\n\tv_pk_min_u16 %1, %1, %4\n\tv_pk_min_u16 %2, %2, %4\n\tv_pk_min_u16 %3, %3, %4\n\t"
      "v_pk_sub_u16 %0, %0, %4\n\tv_pk_sub_u16 %1, %1, %4\n\tv_pk_sub_u16 %2, %2, %4\n\tv_pk_sub_u16 %3, %3, %4"
      : "+v"(x[0]), "+v"(x[1]), "+v"(x[2]), "+v"(x[3]) : "s"(ones));
  return x;
}

typedef __attribute__((ext_vector_type(2))) short s16x2;
extern "C" __global__ __launch_bounds__(512, 2) void k_agg(const u16* __restrict__ sv, const char* __restrict__ wf, u16* __restrict__ out, int N,
                                                           int W, int BN, int B, int bpx) {
  extern __shared__ __attribute__((aligned(512))) char lds_raw[];
  const int tid = TID, lane0 = tid & 63; (void)lane0;
  const int wave = __builtin_amdgcn_readfirstlane(tid >> 6);
  const int b = ((int)BID & 7) * bpx + ((int)BID >> 3);
  if (b >= B) return;
  unsigned long long* diag = (unsigned long long*)((char*)wf + (64 << 20)); (void)diag;
  STAMP(0);
  const unsigned L0 = (unsigned)(uintptr_t)(__attribute__((address_space(3))) char*)lds_raw;
  const int node0 = b * BN;
  const int nb = min(BN, N - node0);
  const uint64_t svp = (uint64_t)sv, wfp = (uint64_t)wf;
  const u32x4 rs_sv = {(unsigned)svp, (unsigned)(svp >> 32), (unsigned)N * 512u, 0x00020000u};
  const u32x4 rs_ws = {(unsigned)wfp, (unsigned)(wfp >> 32), (unsigned)WS_END, 0x00020000u};
#define FRESH_LANE int lane; asm volatile("v_mbcnt_lo_u32_b32 %0, -1, 0\n\tv_mbcnt_hi_u32_b32 %0, -1, %0" : "=v"(lane)); const int l31 = lane & 31, h = lane >> 5; (void)l31; (void)h
  const unsigned TB = L0 + L_TB + wave * TBS;

  f32x16 acc[4][2];
  u32x4 hN[4];
  int batch = 0, r1 = 0;
  bool first = true;
  const int grp = wave;
  while (true) {
    u32x4 ol[13];
    {
      FRESH_LANE;
      const unsigned MAP = L0 + L_LIST + wave * LISTS;
      const int cpw = (W + 7) >> 3;
      const int cell = wave * cpw + lane;
      unsigned t;
      asm volatile("s_nop 4\n\tbuffer_load_dword %0, %1, %2, 0 offen" : "=v"(t) : "v"((lane < cpw && cell < W) ? (int)WS_T + (cell * 256 + b) * 4 : 0x7fffffff), "s"(rs_ws) : "memory");
      __builtin_amdgcn_s_barrier();
      const unsigned lo = 16u * lane;
      const unsigned v4[4] = {lo, lo + 1024u, lo + 2048u, lo + 3072u};
      dma16x4(rs_ws, v4, WS_C + wave * 3 * 4096, L0 + L_TB + wave * 3 * 4096);
      u32x2 dp;
      asm volatile("s_nop 4\n\tbuffer_load_dwordx2 %0, %1, %2, 0 offen" : "=&v"(dp) : "v"((int)WS_P + (node0 + wave * GN + lane) * 8), "s"(rs_ws) : "memory");
      if (lane < 8) *LDSP(unsigned, L0 + L_CNT + lane * 32 + wave * 4) = lane >> 3;
      asm volatile("s_waitcnt vmcnt(5)" : "+v"(t)::"memory");
      const int q = t >> 16;
      const int incl = wave_incl_scan(q), excl = incl - q;
      const int total = __builtin_amdgcn_readlane(incl, 63);
      STAMP(1);
      const int win0 = batch * BQ;
      const int gq = (int)(WS_SEG / 16) + cell * SEGQ + (int)(t & 0xffffu) - excl;
      const int hi = min(incl, win0 + BQ);
#pragma nounroll
      for (int i = max(excl, win0); i < hi; ++i) *LDSP(int, MAP + (i - win0) * 4) = (gq + i) * 16;
      if (lane == 0) *LDSP(int, L0 + L_NB + wave * 4) = (total + BQ - 1) / BQ;
      const bool third = total > win0 + 128;
      int mq[3];
#pragma unroll
      for (int s = 0; s < 3; ++s) mq[s] = *LDSP(int, MAP + (64 * s + lane) * 4);
      u32x4 r4[3];
#pragma unroll
      for (int s = 0; s < 3; ++s) if (win0 + 64 * s + lane >= total) mq[s] = 0x7fffffff;
      asm volatile("s_nop 4\n\tbuffer_load_dwordx4 %0, %3, %6, 0 offen\n\tbuffer_load_dwordx4 %1, %4, %6, 0 offen\n\tbuffer_load_dwordx4 %2, %5, %6, 0 offen"
                   : "=&v"(r4[0]), "=&v"(r4[1]), "=&v"(r4[2]) : "v"(mq[0]), "v"(mq[1]), "v"(mq[2]), "s"(rs_ws) : "memory");
#pragma unroll
      for (int i = 1; i < 3; ++i) dma16x4(rs_ws, v4, WS_C + (wave * 3 + i) * 4096, L0 + L_TB + (wave * 3 + i) * 4096);
      dma16x4(rs_ws, v4, wave == 7 ? WS_C + 24 * 4096 : wave * 4096, L0 + (wave == 7 ? L_TB + 24 * 4096 : L_W + wave * 4096));
      asm volatile("s_waitcnt vmcnt(15)" : "+v"(dp)::"memory");
      if (wave < 2) dma16(rs_ws, (28 + wave) * 1024u + lo, L0 + L_W + (28 + wave) * 1024);
      if (wave == 2 && lane < 4) dma16(rs_ws, WS_F + lo, L0 + L_FR);
      const unsigned oo = first ? (unsigned)(node0 + wave * GN + h) * 512u + 16u * (l31 ^ h) : 0x7fff0000u;
#define OWNA(i) ((oo ^ (32u * ((i) & 7))) + 1024u * (i))
#define OWN3(j) asm volatile("s_nop 4\n\tbuffer_load_dwordx4 %0, %3, %6, 0 offen\n\tbuffer_load_dwordx4 %1, %4, %6, 0 offen\n\tbuffer_load_dwordx4 %2, %5, %6, 0 offen" \
                             : "=&v"(ol[3 * j]), "=&v"(ol[3 * j + 1]), "=&v"(ol[3 * j + 2]) \
                             : "v"(OWNA(3 * j)), "v"(OWNA(3 * j + 1)), "v"(OWNA(3 * j + 2)), "s"(rs_sv) : "memory")
      OWN3(0);
      if (lane < 32) {
        const unsigned qx = (qmod(lo16(dp[0])) - 1024u) & 0xffffu, qy = qmod(hi16(dp[0])) - 1024u, qz = (qmod(__uint_as_float(dp[1])) - 512u) & 0xffffu;
        *LDSP(u32x4, L0 + L_DPOS + (32 * wave + lane) * 16) = lane < GN ? u32x4{dp[0], dp[1], qx | qy << 16, qz} : u32x4{0x7fc07fc0u, 0x7fc00000u, 0x80008000u, 0x8000u};
      }
      asm volatile("s_waitcnt vmcnt(3)" : "+v"(r4[0]), "+v"(r4[1]), "+v"(r4[2])::"memory");
#pragma unroll
      for (int s = 0; s < 3; ++s) if (win0 + 64 * s + lane >= total) r4[s] = u32x4{SENT, SENT, SENT, SENT};
      STAMP(2);
      wg_barrier();
      OWN3(1);
      const unsigned one = opq(1u);
#pragma unroll
      for (int s = 0; s < 3; ++s) {
        if (s == 2 && !third) break;
        if (s == 1) OWN3(2);
        unsigned cd[4];
        u32x2 dq[4];
#pragma unroll
        for (int c = 0; c < 4; ++c) {
          cd[c] = *LDSP(u16, L0 + L_TB + min((r4[s][c] >> 2) & 0x3fffffu, 102398u));
          dq[c] = *LDSP(u32x2, L0 + L_DPOS + 8 + ((r4[s][c] >> 20) & 0xff0u));
        }
        unsigned rank[4];
#pragma unroll
        for (int c = 0; c < 4; ++c) {
          const unsigned cxy = ((cd[c] & 0x3ffu) * 0x400800u) & 0xf800f800u, cz = cd[c] & 0xfc00u;
          const unsigned qxy = dq[c][0], qz = dq[c][1];
          const s16x2 dxy = __builtin_bit_cast(s16x2, qxy) - __builtin_bit_cast(s16x2, cxy);
          const s16x2 dz = __builtin_bit_cast(s16x2, qz) - __builtin_bit_cast(s16x2, cz);
          const unsigned d2 = __builtin_amdgcn_sdot2(dxy, dxy, __builtin_amdgcn_sdot2(dz, dz, 0, false), false);
          rank[c] = 0x7fffffffu;
          if ((d2 < 488400000u) & (r4[s][c] != SENT))
            rank[c] = __hip_atomic_fetch_add(LDSP(unsigned, L0 + L_CNT + (r4[s][c] >> 29) * 32 + wave * 4), one, __ATOMIC_RELAXED, __HIP_MEMORY_SCOPE_WORKGROUP);
        }
#pragma unroll
        for (int c = 0; c < 4; ++c) {
          const unsigned p = rank[c] - SUBC * r1;
          if (p < (unsigned)SUBC) *LDSP(unsigned, L0 + L_LIST + O_RAW + (r4[s][c] >> 29) * LISTS + (wave * SUBC + p) * 4) = r4[s][c];
        }
      }
      OWN3(3);
      asm volatile("s_nop 4\n\tbuffer_load_dwordx4 %0, %1, %2, 0 offen" : "=&v"(ol[12]) : "v"(h ? 0x7fff0000u : OWNA(12)), "s"(rs_sv) : "memory");
    }
    STAMP(3);
    wg_barrier();
    STAMP(4);
    bool more1, moreb;
    for (int r2 = 0;; ++r2) {
    FRESH_LANE;
    int tot = 0, off[8], gap[8];
    {
      u32x4 c0 = *LDSP(u32x4, L0 + L_CNT + (lane & 7) * 32), c1 = *LDSP(u32x4, L0 + L_CNT + (lane & 7) * 32 + 16);
      const int nbv = *LDSP(int, L0 + L_NB + (lane & 7) * 4);
      const unsigned mx = max(max(max(c0[0], c0[1]), max(c0[2], c0[3])), max(max(c1[0], c1[1]), max(c1[2], c1[3])));
      more1 = __builtin_amdgcn_ballot_w64(mx > (unsigned)(SUBC * (r1 + 1))) != 0;
      moreb = __builtin_amdgcn_ballot_w64(nbv > batch + 1) != 0;
#pragma unroll
      for (int i = 0; i < 8; ++i) {
        const int c = min(max(__builtin_amdgcn_readlane(i < 4 ? c0[i & 3] : c1[i & 3], grp) - SUBC * r1, 0), SUBC);
        tot += c; off[i] = tot; gap[i] = SUBC - c;
      }
      tot = __builtin_amdgcn_readfirstlane(tot);
    }
    const unsigned LIST = L0 + L_LIST + grp * LISTS;
    int nact = 0;
    unsigned exy[4], ezd[4], ep[4];
    u32x4 own[16];
    {
      unsigned rec[4];
      u32x2 ps[4], pd[4];
#pragma unroll
      for (int i = 0; i < 4; ++i) {
        rec[i] = SENT;
        if (i < 2 || 64 * i < tot) {
          const int p = 64 * i + lane;
          int idx = p;
#pragma unroll
          for (int k = 0; k < 7; ++k) idx += p >= off[k] ? gap[k] : 0;
          rec[i] = *LDSP(unsigned, LIST + O_RAW + min(idx, 8 * SUBC - 1) * 4);
          if (p >= tot) rec[i] = SENT;
        }
      }
      {
        asm volatile("s_nop 4\n\tbuffer_load_dwordx2 %0, %4, %8, %9 offen\n\tbuffer_load_dwordx2 %1, %5, %8, %9 offen\n\t"
                     "buffer_load_dwordx2 %2, %6, %8, %9 offen\n\tbuffer_load_dwordx2 %3, %7, %8, %9 offen"
                     : "=&v"(ps[0]), "=&v"(ps[1]), "=&v"(ps[2]), "=&v"(ps[3])
                     : "v"(rec[0] & 0xffffffu), "v"(rec[1] & 0xffffffu), "v"(rec[2] & 0xffffffu), "v"(rec[3] & 0xffffffu), "s"(rs_ws), "s"((unsigned)WS_P) : "memory");
      }
#pragma unroll
      for (int i = 0; i < 4; ++i) pd[i] = *LDSP(u32x2, L0 + L_DPOS + ((rec[i] >> 20) & 0xff0u));
#pragma unroll
      for (int i = 0; i < 2; ++i) *LDSP(unsigned, LIST + (64 * i + lane) * 4) = opq(0xffffffu);
      if (lane < 32) *LDSP(unsigned, LIST + (128 + lane) * 4) = opq(0xffffffu);
      *LDSP(unsigned, LIST + O_D + lane * 4) = 0x3f803f80u;
      *LDSP(unsigned, LIST + O_DL + lane * 4) = 0xffffffffu;
      asm volatile("s_waitcnt vmcnt(4)" : "+v"(ol[0]), "+v"(ol[1]), "+v"(ol[2]), "+v"(ol[3]), "+v"(ol[4]), "+v"(ol[5]), "+v"(ol[6]), "+v"(ol[7]),
                   "+v"(ol[8]), "+v"(ol[9]), "+v"(ol[10]), "+v"(ol[11]), "+v"(ol[12])::"memory");
#pragma unroll
      for (int i = 0; i < 12; ++i) *LDSP(u32x4, TB + i * 1024 + lane * 16) = ol[i];
      if (h == 0) *LDSP(u32x4, TB + 12 * 1024 + lane * 16) = ol[12];
      {
        const unsigned ra = TB + l31 * 512 + 16 * ((4 * h) ^ (l31 & 15));
#pragma unroll
        for (int i = 0; i < 16; ++i) own[i] = *LDSP(u32x4, (ra ^ (16 * (8 * ((i >> 2) & 1) + (i & 3)))) + 256 * (i >> 3));
      }
    if (first) {
#pragma unroll
      for (int i = 0; i < 16; ++i)
#pragma unroll
        for (int k = 0; k < 4; ++k) {
          acc[i >> 2][(i >> 1) & 1][8 * (i & 1) + 2 * k] = lo16(own[i][k]);
          acc[i >> 2][(i >> 1) & 1][8 * (i & 1) + 2 * k + 1] = hi16(own[i][k]);
        }
      first = false;
    }
      SB();
      asm volatile("s_waitcnt vmcnt(0)" : "+v"(ps[0]), "+v"(ps[1]), "+v"(ps[2]), "+v"(ps[3])::"memory");
#pragma unroll
      for (int i = 0; i < 4; ++i) {
        exy[i] = 0; ezd[i] = 0; ep[i] = SENT;
        if (i >= 2 && 64 * i >= tot) continue;
        const unsigned xy = pk2(lo16(ps[i][0]) - lo16(pd[i][0]), hi16(ps[i][0]) - hi16(pd[i][0]));
        const float z = rb(__uint_as_float(ps[i][1]) - __uint_as_float(pd[i][1]));
        const float x = lo16(xy), y = hi16(xy);
        const unsigned sq = pk2(x * x, y * y);
        const float q2 = rb(rb(lo16(sq) + hi16(sq) + rb(z * z)) + 1e-8f);
        const unsigned zd = pk2(z, __builtin_amdgcn_sqrtf(q2));
        const bool ok = hi16(zd) < 5.0f;
        const uint64_t bm = __builtin_amdgcn_ballot_w64(ok);
        const unsigned p = __builtin_amdgcn_mbcnt_hi((unsigned)(bm >> 32), __builtin_amdgcn_mbcnt_lo((unsigned)bm, nact - CAP * r2));
        nact += __builtin_popcountll(bm);
        exy[i] = xy; ezd[i] = zd;
        if (ok & (p < (unsigned)CAP)) {
          ep[i] = p;
          *LDSP(unsigned, LIST + swap23(p) * 4) = rec[i] & 0xffffffu;
          *LDSP(u16, LIST + O_D + swap23(p) * 2) = (u16)(zd >> 16);
          *LDSP(u16, LIST + O_DL + p * 2) = (u16)(rec[i] >> 24);
        }
      }
    }
    const int cnt = min(nact - CAP * r2, CAP);
    const int ntile = (cnt + 31) >> 5;
    auto issue_h = [&](int t) {
      FRESH_LANE;
      const unsigned se = *LDSP(unsigned, LIST + (32 * t + l31) * 4);
      asm volatile("s_nop 4\n\tbuffer_load_dwordx4 %0, %4, %5, %6 offen\n\tbuffer_load_dwordx4 %1, %4, %5, %6 offen offset:16\n\t"
                   "buffer_load_dwordx4 %2, %4, %5, %6 offen offset:32\n\tbuffer_load_dwordx4 %3, %4, %5, %6 offen offset:48"
                   : "=&v"(hN[0]), "=&v"(hN[1]), "=&v"(hN[2]), "=&v"(hN[3]) : "v"(se * 16u + 64u * h), "s"(rs_ws), "s"((unsigned)WS_H) : "memory");
    };
    auto wait_h = [&]() {
      asm volatile("s_waitcnt vmcnt(12)" : "+v"(hN[0]), "+v"(hN[1]), "+v"(hN[2]), "+v"(hN[3])::"memory");
    };
    unsigned vo[4];
    auto prep_v = [&](int t) {
      FRESH_LANE;
      const int drow = lane >> 3;
      const unsigned dchunk = 16u * (((lane & 7) ^ (drow & 2)) + 8);
#pragma unroll
      for (int i = 0; i < 4; ++i) vo[i] = *LDSP(unsigned, LIST + (32 * t + 8 * i + drow) * 4) * 64u + dchunk;
    };
    auto issue_v = [&](int k) {
      asm volatile("s_waitcnt lgkmcnt(0)" ::: "memory");
      dma16x4(rs_sv, vo, 128 * k, TB + 4096 * k);
    };
    setprio(3);
    prep_v(0);
    issue_h(0);
    STAMP(5);
    if (r2 == 0) __builtin_amdgcn_s_barrier();
    const bool late = STAG >= 1 && r2 == 0 && wave >= 4;
    if (!late) {
      issue_v(0); issue_v(1); issue_v(2);
      if (STAG >= 1 && r2 == 0) __builtin_amdgcn_s_barrier();
    }
    setprio(P_START);
    SB();
#pragma unroll
    for (int i = 0; i < 4; ++i) {
      if (i >= 2 && 64 * i >= tot) break;
      const float d = hi16(ezd[i]);
      const float inv = __builtin_amdgcn_rcpf(d);
      const float a = rb(rb(3.141592653589793f * d) * 0.2f);
      const float f = 0.5f * rb(rb(__builtin_amdgcn_cosf(a * 0.15915494309189535f)) + 1.0f);
      const unsigned hxy = pk2(lo16(exy[i]) * inv, hi16(exy[i]) * inv);
      const float hz = rb(lo16(ezd[i]) * inv);
      const unsigned fxy = pk2(f * lo16(hxy), f * hi16(hxy));
      const unsigned fzf = pk2(f * hz, f);
      if (ep[i] != SENT) {
        const unsigned a2 = LIST + ep[i] * 2;
        *LDSP(u16, a2 + O_F) = (u16)(fzf >> 16);
        *LDSP(u16, a2 + O_FX) = (u16)fxy;
        *LDSP(u16, a2 + O_FX + 256) = (u16)(fxy >> 16);
        *LDSP(u16, a2 + O_FX + 512) = (u16)fzf;
      }
    }
    SB();
    if (late) {
      __builtin_amdgcn_s_barrier();
      issue_v(0); issue_v(1); issue_v(2);
    }
    wait_h();
    STAMP(6);
    auto tile = [&](int t, auto nsc) {
      constexpr int NS = decltype(nsc)::value;
      FRESH_LANE;
      const unsigned WL = L0 + L_W + lane * 16, la = LIST + 64 * t + 16 * h, lf = L0 + L_FR + 32 * h;
      const unsigned mypair = (unsigned)(grp * 32 + l31) * 0x10001u;
      u32x4 Wb[5];
      auto ldW = [&](int ct) {
#pragma unroll
        for (int ks = 0; ks < 4; ++ks) Wb[ks] = *LDSP(u32x4, WL + (4 * ct + ks) * 1024);
        Wb[4] = *LDSP(u32x4, WL + (24 + ct) * 1024);
      };
      const float d = lo16(*LDSP(u16, LIST + O_D + 64 * t + 2 * l31));
      const f32x4 f0 = *LDSP(f32x4, lf), f1 = *LDSP(f32x4, lf + 16);
      ldW(4);
      u32x4 msk[2], S1[2];
#pragma unroll
      for (int s = 0; s < NS; ++s) { msk[s] = *LDSP(u32x4, la + O_DL + 32 * s); S1[s] = *LDSP(u32x4, la + O_F + 32 * s); }
      SB();
      f32x16 ph, w;
      u32x4 rbf;
      auto gemm_p = [&]() {
        ph = mfma(hN[0], Wb[0], zero16());
#pragma unroll
        for (int ks = 1; ks < 4; ++ks) ph = mfma(hN[ks], Wb[ks], ph);
      };
      auto gemm = [&]() { gemm_p(); w = mfma(rbf, Wb[4], zero16()); };
      gemm_p();
      {
        const float inv = __builtin_amdgcn_rcpf(d);
#pragma unroll
        for (int i = 0; i < 4; ++i) {
          const float fa = i < 2 ? f0[2 * i] : f1[2 * i - 4], fb = i < 2 ? f0[2 * i + 1] : f1[2 * i - 3];
          const unsigned x = pk2(d * fa, d * fb);
          float ra = lo16(x) * 0.15915494309189535f, rc = hi16(x) * 0.15915494309189535f;
          ra -= __builtin_rintf(ra); rc -= __builtin_rintf(rc);
          rbf[i] = pk2(__builtin_amdgcn_sinf(ra) * inv, __builtin_amdgcn_sinf(rc) * inv);
        }
      }
#pragma unroll
      for (int i = 0; i < 4; ++i) {
        __builtin_amdgcn_sched_group_barrier(0x8, 1, 0);
        __builtin_amdgcn_sched_group_barrier(0x402, 12, 0);
      }
      SB();
      w = mfma(rbf, Wb[4], zero16());
      SB();
      {
        u32x4 sr[3][2];
#pragma unroll
        for (int s = 0; s < NS; ++s)
#pragma unroll
          for (int k = 0; k < 3; ++k) sr[k][s] = *LDSP(u32x4, la + O_FX + 256 * k + 32 * s);
#pragma unroll
        for (int s = 0; s < NS; ++s) { msk[s] = eqmask4(msk[s], mypair, 0x00010001u); S1[s] &= msk[s]; }
        SB();
#pragma unroll
        for (int t2 = 0; t2 < 2; ++t2) {
          const f32x16 m0 = ph * w;
          u32x4 a[2];
#pragma unroll
          for (int s = 0; s < NS; ++s) a[s] = pack8(m0, s);
          if (t2 == 0) { SB(); ldW(5); SB(); }
#pragma unroll
          for (int s = 0; s < NS; ++s)
#pragma unroll
            for (int k = 0; k < 3; ++k) { if (t2 == 0) sr[k][s] &= msk[s]; acc[1 + k][t2] = mfma(a[s], sr[k][s], acc[1 + k][t2]); }
          SB();
          gemm();
          SB();
          ldW(t2);
          SB();
        }
      }
      f32x16 mv[2];
#pragma unroll
      for (int t2 = 0; t2 < 2; ++t2) {
        const f32x16 ms = ph * w;
        u32x4 a[2];
#pragma unroll
        for (int s = 0; s < NS; ++s) a[s] = pack8(ms, s);
        SB();
        gemm();
        SB();
        ldW(2 + t2);
        SB();
#pragma unroll
        for (int s = 0; s < NS; ++s) acc[0][t2] = mfma(a[s], S1[s], acc[0][t2]);
        SB();
      }
      mv[0] = ph * w;
      SB();
      gemm();
      SB();
      issue_h(t + 1);
      prep_v(t + 1);
      unsigned tra;
      {
        FRESH_LANE;
        const int q4 = (lane >> 2) & 3, pp = lane & 3;
        tra = (q4 & 1) ? TB + (4 * h + (q4 >> 1)) * 128 + 64 * (pp & 1) + (lane & 16) + 8 * (pp >> 1) : 0x100000u;
      }
      SB();
      P_PH2;
      asm volatile("s_waitcnt vmcnt(4)" ::: "memory");
      if (t < 3) STAMP(7 + 2 * t);
      u32x2 va[8], vb[8];
      auto ldv = [&](u32x2 (&v)[8], int t2, int k) {
#pragma unroll
        for (int i = 0; i < 4 * NS; ++i)
          v[i] = trread(tra + k * 4096 + (8 * (i >> 1) + 2 * (i & 1)) * 128 + 32 * (t2 ^ (i & 1)));
      };
      auto vv = [&](const u32x2 (&v)[8], const f32x16& m, f32x16& ac) {
        u32x4 a[2];
#pragma unroll
        for (int i = 0; i < 4 * NS; ++i) {
          const unsigned v0 = v[i][0], v1 = v[i][1];
          a[i >> 2][i & 3] = pk2(m[2 * i] * __uint_as_float(v0), m[2 * i + 1] * __uint_as_float(v1));
        }
#pragma unroll
        for (int s = 0; s < NS; ++s) ac = mfma(a[s], S1[s], ac);
      };
      ldv(va, 0, 0); ldv(vb, 1, 0);
      mv[1] = ph * w;
      SB();
      vv(va, mv[0], acc[1][0]);
      SB();
      issue_v(0);
      ldv(va, 0, 1);
      SB();
      vv(vb, mv[1], acc[1][1]);
      SB();
      ldv(vb, 1, 1);
      SB();
      vv(va, mv[0], acc[2][0]);
      SB();
      issue_v(1);
      ldv(va, 0, 2);
      SB();
      vv(vb, mv[1], acc[2][1]);
      SB();
      ldv(vb, 1, 2);
      SB();
      vv(va, mv[0], acc[3][0]);
      SB();
      issue_v(2);
      if (t < 3) STAMP(8 + 2 * t);
      vv(vb, mv[1], acc[3][1]);
      SB();
      wait_h();
    };
    const int nfull = (cnt + 15) >> 5;
    for (int t = 0; t < nfull; ++t) { tile(t, std::integral_constant<int, 2>{}); setprio(P_NEXT); }
    if (nfull < ntile) tile(nfull, std::integral_constant<int, 1>{});
    if (nact <= CAP * (r2 + 1)) break;
    asm volatile("s_waitcnt vmcnt(0)" ::: "memory");
#pragma unroll
    for (int i = 0; i < 13; ++i) asm volatile("" : "=v"(ol[i]));
    }
    if (more1) ++r1;
    else if (moreb) { ++batch; r1 = 0; }
    else break;
    asm volatile("s_waitcnt vmcnt(0)" ::: "memory");
    wg_barrier();
  }
  STAMP(13);
  {
    FRESH_LANE;
    u32x4 o[16];
#pragma unroll
    for (int i = 0; i < 16; ++i) o[i] = pack8(acc[i >> 2][(i >> 1) & 1], i & 1);
    asm volatile("s_waitcnt vmcnt(0)" ::: "memory");
    if (l31 < GN) {
      const unsigned wa = TB + l31 * 512 + 16 * ((4 * h) ^ (l31 & 7));
#pragma unroll
      for (int i = 0; i < 16; ++i) *LDSP(u32x4, (wa ^ (16 * (i & 3))) + 128 * (i >> 2)) = o[i];
    }
    const int gw = __builtin_amdgcn_readfirstlane(max(min(nb - grp * GN, GN), 0));
    const uint64_t op = (uint64_t)out + (size_t)(node0 + grp * GN) * 512;
    const __amdgpu_buffer_rsrc_t rb_o = __builtin_amdgcn_make_buffer_rsrc((void*)op, 0, gw * 512, 0x00020000);
    u32x4 v[(GN + 1) / 2];
#pragma unroll
    for (int i = 0; i < (GN + 1) / 2; ++i) v[i] = *LDSP(u32x4, TB + i * 1024 + lane * 16);
    const unsigned oa = h * 512 + 16 * (l31 ^ h);
#pragma unroll
    for (int i = 0; i < (GN + 1) / 2; ++i) __builtin_amdgcn_raw_buffer_store_b128(v[i], rb_o, oa ^ (32 * (i & 3)), i * 1024, K2AUX);
  }
  STAMP(14);
  STAMP(15);
}

extern "C" __global__ __launch_bounds__(512) void k_cal(const u32x4* sv, const u32x4* src, const u32x4* dst, const u32x4* pos, unsigned* sink, int nsv, int ne, int np, int mode) {
  const unsigned long long t0 = __builtin_amdgcn_s_memrealtime();
  const int g = BID * 512 + TID, st = 256 * 512;
  unsigned x = 0;
  if (mode & 1) { for (int i = g; i < nsv; i += st) x ^= sv[i][0]; }
  if (mode & 2) { for (int i = g; i < ne; i += st) x ^= src[i][0] ^ dst[i][0]; for (int i = g; i < np; i += st) x ^= pos[i][0]; }
  if (x == 0x12345) *sink = x;
  while (__builtin_amdgcn_s_memrealtime() - t0 < 3000ull) __builtin_amdgcn_s_sleep(2);
}
#if defined(__HIP_DEVICE_COMPILE__)
#pragma clang attribute pop
#endif
extern "C" void kernel_launch(void* const* d_in, const int* in_sizes, int n_in, void* d_out, int out_size, void* d_ws, size_t ws_size,
                              hipStream_t stream) {
  const u16* sv = (const u16*)d_in[0];
  const u16* pos = (const u16*)d_in[1];
  const int* src = (const int*)d_in[2];
  const int* dst = (const int*)d_in[3];
  const u16* fr = (const u16*)d_in[4];
  const u16* w1 = (const u16*)d_in[5];
  const u16* w2 = (const u16*)d_in[6];
  const u16* wr = (const u16*)d_in[7];
  const int N = in_sizes[1] / 3, E = in_sizes[2];
  const int BN = (N + 255) / 256 < 2 ? 2 : (N + 255) / 256;
  const int B = (N + BN - 1) / BN, bpx = (B + 7) / 8;
  const int W = (E + CH - 1) / CH;
  const int NPX = bpx * BN, HW = 8 * ((NPX + 255) / 256), WP = (W + 7) & ~7;
  const unsigned magicBN = (unsigned)(0x100000000ull / (unsigned)BN) + 1u;
  char* ws = (char*)d_ws;
  (void)hipFuncSetAttribute((const void*)k_agg, hipFuncAttributeMaxDynamicSharedMemorySize, L_END);
  k_cal<<<256, 512, 0, stream>>>((const u32x4*)sv, (const u32x4*)src, (const u32x4*)dst, (const u32x4*)pos, (unsigned*)(ws + (100 << 20)), N * 32, E / 4, N * 6 / 16, 3);
  k_bin<<<WP + HW + 8, 512, 0, stream>>>(src, dst, (unsigned*)(ws + WS_T), (u32x4*)(ws + WS_SEG), sv, pos, w1, w2, wr, fr, (u32x4*)ws,
                                        (u32x2*)(ws + WS_P), (u16*)(ws + WS_C), ws + WS_H, (const char*)d_out, N, E, W, HW, magicBN, BN, WP, NPX);
  k_agg<<<8 * bpx, 512, L_END, stream>>>(sv, ws, (u16*)d_out, N, W, BN, B, bpx);
}
```

```cpp
#define CODEPF
#define SELFPF
#if defined(__HIP_DEVICE_COMPILE__)
#pragma clang attribute push(__attribute__((target("no-packed-fp32-ops"))), apply_to = function)
#endif
#include <hip/hip_runtime.h>
#include <stdint.h>
#include <type_traits>

#define STAG 2
#define HEAVYFIRST 1
#define NOST 1
#define NEARLY 1
#define NODEPRIO 0
#define K1ORD 1
#define BINPRIO 0
#define PMODE 6
#define P_START min(ntile - 1, 2)
#define P_NEXT min(ntile - 2 - t, 2)
#define P_PH2 __builtin_amdgcn_s_setprio(3)
#define TID __builtin_amdgcn_workitem_id_x()
#define BID __builtin_amdgcn_workgroup_id_x()
typedef unsigned short u16;
typedef __attribute__((ext_vector_type(8))) __bf16 bf16x8;
typedef __attribute__((ext_vector_type(2))) __bf16 bf16x2;
typedef __attribute__((ext_vector_type(4))) short s16x4;
typedef __attribute__((ext_vector_type(2))) float f32x2;
typedef __attribute__((ext_vector_type(4))) float f32x4;
typedef __attribute__((ext_vector_type(16))) float f32x16;
typedef __attribute__((ext_vector_type(4))) unsigned u32x4;
typedef __attribute__((ext_vector_type(3))) unsigned u32x3;
typedef __attribute__((ext_vector_type(2))) unsigned u32x2;
#define LDSP(T, off) ((__attribute__((address_space(3))) T*)(uintptr_t)(off))
#define DI __device__ __forceinline__
#define SB() __builtin_amdgcn_sched_barrier(0)

constexpr int CH = 4096;
constexpr int SEGQ = CH / 4 + 192;
constexpr int GN = 25;
constexpr int NFRAG = 30;
constexpr int SUBC = 32;
constexpr int CAP = 128;
constexpr int BQ = 192;
constexpr unsigned SENT = 0xFFFFFFFFu;
constexpr int L_DPOS = 0;
constexpr int L_RAW = 4096;
constexpr int L_CNT = L_RAW + 8192;
constexpr int L_NB = L_CNT + 256;
constexpr int L_FR = L_NB + 64;
constexpr int L_LIST = L_FR + 64;
constexpr int O_D = 640, O_DL = O_D + 256, O_F = O_DL + 256, O_FX = O_F + 256;
constexpr int LISTS = O_FX + 768;
constexpr int L_W = L_LIST + 8 * LISTS;
constexpr int L_TB = (L_W + NFRAG * 1024 + 511) / 512 * 512;
constexpr int TBS = GN * 512;
constexpr int L_END = L_TB + 8 * TBS;
static_assert(L_END <= 163840 && L_TB < 65536 && L_W % 16 == 0 && LISTS % 16 == 0 && SUBC == 32, "LDS");
constexpr size_t WS_T = 65536;
constexpr size_t WS_SEG = WS_T + 256 * 256 * 4;
constexpr size_t WS_P = WS_SEG + (size_t)256 * SEGQ * 16;
constexpr size_t WS_C = WS_P + 51200 * 8;
constexpr size_t WS_H = WS_C + 51200 * 2;
constexpr size_t WS_END = WS_H + 51200 * 128;
constexpr size_t WS_F = NFRAG * 1024;
static_assert(WS_END < 0xffffff, "ws");

#define STAMP(i)
#define STAMP1(i)
#define WAITV
DI float lo16(unsigned x) { return __uint_as_float(x << 16); }
DI float hi16(unsigned x) { return __uint_as_float(x & 0xffff0000u); }
DI unsigned pk2(float a, float b) { f32x2 v = {a, b}; return __builtin_bit_cast(unsigned, __builtin_convertvector(v, bf16x2)); }
DI float rb(float a) { return hi16(pk2(a, a)); }
DI f32x16 mfma(u32x4 a, u32x4 b, f32x16 c) {
  return __builtin_amdgcn_mfma_f32_32x32x16_bf16(__builtin_bit_cast(bf16x8, a), __builtin_bit_cast(bf16x8, b), c, 0, 0, 0);
}
DI f32x16 zero16() { f32x16 z;
#pragma unroll
  for (int r = 0; r < 16; ++r) z[r] = 0.f;
  return z; }
DI int wave_incl_scan(int v) {
  v += __builtin_amdgcn_update_dpp(0, v, 0x111, 0xf, 0xf, false);
  v += __builtin_amdgcn_update_dpp(0, v, 0x112, 0xf, 0xf, false);
  v += __builtin_amdgcn_update_dpp(0, v, 0x114, 0xf, 0xf, false);
  v += __builtin_amdgcn_update_dpp(0, v, 0x118, 0xf, 0xf, false);
  v += __builtin_amdgcn_update_dpp(0, v, 0x142, 0xa, 0xf, false);
  v += __builtin_amdgcn_update_dpp(0, v, 0x143, 0xc, 0xf, false);
  return v;
}
DI int chan_of(int t2, int i32) { return 32 * ((i32 >> 2) & 1) + 16 * t2 + 4 * (i32 >> 3) + (i32 & 3); }
DI u32x4 pack8(const f32x16& x, int s) {
  return u32x4{pk2(x[8 * s], x[8 * s + 1]), pk2(x[8 * s + 2], x[8 * s + 3]), pk2(x[8 * s + 4], x[8 * s + 5]), pk2(x[8 * s + 6], x[8 * s + 7])};
}
DI void setprio(int p) {
  if (p >= 3) __builtin_amdgcn_s_setprio(3);
  else if (p == 2) __builtin_amdgcn_s_setprio(2);
  else if (p == 1) __builtin_amdgcn_s_setprio(1);
  else __builtin_amdgcn_s_setprio(0);
}
DI u32x2 trread(unsigned a) { return __builtin_bit_cast(u32x2, __builtin_amdgcn_ds_read_tr16_b64_v4i16(LDSP(s16x4, a))); }
#define K1AUX 17
#define IDXAUX 0
#define K2AUX 17
DI void st16(void* base, unsigned off, u32x4 v) {
  const __amdgpu_buffer_rsrc_t r = __builtin_amdgcn_make_buffer_rsrc(base, 0, 0x7fffffff, 0x00020000);
  __builtin_amdgcn_raw_buffer_store_b128(v, r, off, 0, K1AUX);
}
DI unsigned qmod(float x) { return min((unsigned)(__builtin_amdgcn_fractf(x * 0.0625f) * 65536.0f), 65535u); }
DI unsigned opq(unsigned c) { asm volatile("" : "+v"(c)); return c; }
DI unsigned swap23(unsigned p) { return (p & ~12u) | ((p & 4u) << 1) | ((p & 8u) >> 1); }
DI void wg_barrier() { asm volatile("s_waitcnt lgkmcnt(0)\n\ts_barrier" ::: "memory"); }

DI void dma16(u32x4 rsrc, unsigned voff, unsigned ldsaddr) {
  asm volatile("s_mov_b32 m0, %2\n\ts_nop 3\n\tbuffer_load_dwordx4 %0, %1, 0 offen lds" ::"v"(voff), "s"(rsrc), "s"(ldsaddr) : "memory");
}
DI void dma16x4(u32x4 rsrc, const unsigned (&voff)[4], unsigned soff, unsigned m0) {
  asm volatile("s_mov_b32 m0, %6\n\ts_nop 3\n\tbuffer_load_dwordx4 %0, %4, %5 offen lds\n\t"
               "s_add_u32 m0, m0, 0x400\n\ts_nop 0\n\tbuffer_load_dwordx4 %1, %4, %5 offen lds\n\t"
               "s_add_u32 m0, m0, 0x400\n\ts_nop 0\n\tbuffer_load_dwordx4 %2, %4, %5 offen lds\n\t"
               "s_add_u32 m0, m0, 0x400\n\ts_nop 0\n\tbuffer_load_dwordx4 %3, %4, %5 offen lds"
               ::"v"(voff[0]), "v"(voff[1]), "v"(voff[2]), "v"(voff[3]), "s"(rsrc), "s"(soff), "s"(m0) : "memory", "scc");
}
constexpr int K1_CNT = 0, K1_STAGE = 8 * 256 * 4, K1_TOT = K1_STAGE + SEGQ * 16, K1_W1 = 0, K1_HT = 8192, K1_END = K1_HT + 8 * 4096;
#if defined(__HIP_DEVICE_COMPILE__)
#pragma clang attribute pop
#endif
extern "C" __global__ __launch_bounds__(512, 4) void k_bin(const int* __restrict__ src, const int* __restrict__ dst, unsigned* __restrict__ T,
                                                u32x4* __restrict__ seg, const u16* __restrict__ sv, const u16* __restrict__ pos,
                                                const u16* __restrict__ w1, const u16* __restrict__ w2, const u16* __restrict__ wr, const u16* __restrict__ freqs,
                                                u32x4* __restrict__ wf, u32x2* __restrict__ ptab, u16* __restrict__ ctab, char* __restrict__ htab, int N, int E,
                                                int W, int HW, unsigned magicBN, int BN, int WP, int NPX, int chunk) {
  __shared__ __attribute__((aligned(16))) char lds_raw[K1_END + 256];
  const unsigned L0 = (unsigned)(uintptr_t)(__attribute__((address_space(3))) char*)lds_raw;
  const int tid = TID, lane = tid & 63;
  const int wave = __builtin_amdgcn_readfirstlane(tid >> 6);
  int bid = BID;
  if (bid < WP + HW) { if (bid < WP) { if (bid >= W) return; bid += HW; } else bid -= WP; } else bid += W - WP;
  STAMP1(0);
  if (wave == 0) {
    uint64_t pc; unsigned len;
    asm volatile("s_getpc_b64 %0\npainn_self_lo:\n\ts_mov_b32 %1, painn_code_hi-painn_self_lo" : "=s"(pc), "=s"(len));
    const u32x4 rc = {(unsigned)pc, (unsigned)(pc >> 32) & 0xffffu, len, 0x00020000u};
    asm volatile("s_mov_b32 m0, %2\n\ts_nop 3\n\tbuffer_load_dword %0, %1, 0 offen lds" ::"v"(lane * 128), "s"(rc), "s"(L0 + K1_END) : "memory");
  }
  if (bid >= W + HW) {
    {
      uint64_t pc; unsigned len;
      asm volatile("s_getpc_b64 %0\npainn_code_lo:\n\ts_mov_b32 %1, painn_code_hi-painn_code_lo" : "=s"(pc), "=s"(len));
      const __amdgpu_buffer_rsrc_t rc = __builtin_amdgcn_make_buffer_rsrc((void*)pc, 0, len, 0x00020000);
      const unsigned x = __builtin_amdgcn_raw_buffer_load_b32(rc, tid * 64, 0, 0);
      asm volatile("" ::"v"(x));
    }
    const int idx = (bid - W - HW) * 512 + tid;
    if (idx >= NFRAG * 64) {
      if (idx < NFRAG * 64 + 16) ((unsigned*)wf)[WS_F / 4 + idx - NFRAG * 64] = (unsigned)freqs[idx - NFRAG * 64] << 16;
      return;
    }
    const int f = idx >> 6, i32 = lane & 31, h = lane >> 5;
    const u16* p; int s2;
    if (f < 24) { const int ct = f >> 2; p = w2 + (16 * (f & 3) + 4 * h) * 192 + 64 * (ct >> 1) + chan_of(ct & 1, i32); s2 = 8 * 192; }
    else { const int ct = f - 24; p = wr + 8 * h * 192 + 64 * (ct >> 1) + chan_of(ct & 1, i32); s2 = 4 * 192; }
    unsigned v[8];
#pragma unroll
    for (int j = 0; j < 8; ++j) v[j] = p[(j & 3) * 192 + (j >> 2) * s2];
    wf[idx] = u32x4{v[0] | v[1] << 16, v[2] | v[3] << 16, v[4] | v[5] << 16, v[6] | v[7] << 16};
    return;
  }
  if (bid < HW) {
    const int l31 = lane & 31, h = lane >> 5;
    const int nl = (wave * (HW >> 3) + (bid >> 3)) * 32, nend = min(N, ((bid & 7) + 1) * NPX);
    const int n0 = nl < NPX ? (bid & 7) * NPX + nl : N;
    const int nn = min(n0 + l31, N - 1);
    __builtin_amdgcn_s_setprio(NODEPRIO);
    const unsigned HT = L0 + K1_HT + wave * 4096;
    {
      const uint64_t sp = (uint64_t)sv, wp = (uint64_t)w1;
      const u32x4 rs_sv = {(unsigned)sp, (unsigned)(sp >> 32), (unsigned)N * 512u, 0x00020000u};
      const u32x4 rs_w1 = {(unsigned)wp, (unsigned)(wp >> 32), 8192u, 0x00020000u};
      const unsigned o = (unsigned)(n0 + (lane >> 3)) * 512u + 16u * ((lane & 7) ^ (lane >> 3));
      const unsigned v4[4] = {o, o + 4096u, o + 8192u, o + 12288u};
      dma16x4(rs_sv, v4, 0, HT);
      dma16(rs_w1, tid * 16u, L0 + K1_W1 + wave * 1024);
    }
    unsigned px = pos[nn * 3], py = pos[nn * 3 + 1], pz = pos[nn * 3 + 2];
    asm volatile("s_waitcnt vmcnt(0)\n\ts_barrier" : "+v"(px), "+v"(py), "+v"(pz)::"memory");
    STAMP1(1); STAMP1(2);
    if (n0 >= N) return;
    u32x4 sf[4];
#pragma unroll
    for (int ks = 0; ks < 4; ++ks) sf[ks] = *LDSP(u32x4, HT + l31 * 128 + 16 * ((4 * h + ks) ^ (l31 & 7)));
    const unsigned ta = L0 + K1_W1 + (32 * h + ((lane >> 2) & 3)) * 128 + (16 * ((lane >> 4) & 1) + 4 * (lane & 3)) * 2;
    f32x16 d1[2];
#pragma unroll
    for (int ht = 0; ht < 2; ++ht) {
      d1[ht] = zero16();
#pragma unroll
      for (int ks = 0; ks < 4; ++ks) {
        const u32x2 a0 = trread(ta + ks * 1024 + ht * 64), a1 = trread(ta + ks * 1024 + 512 + ht * 64);
        d1[ht] = mfma(u32x4{a0[0], a0[1], a1[0], a1[1]}, sf[ks], d1[ht]);
      }
    }
#pragma unroll
    for (int ht = 0; ht < 2; ++ht) {
      f32x16 e = d1[ht] * -1.4426950408889634f;
#pragma unroll
      for (int r = 0; r < 16; ++r) e[r] = __builtin_amdgcn_exp2f(e[r]);
      e += 1.0f;
#pragma unroll
      for (int r = 0; r < 16; ++r) e[r] = __builtin_amdgcn_rcpf(e[r]);
      e *= d1[ht];
#pragma unroll
      for (int s = 0; s < 2; ++s) *LDSP(u32x4, HT + l31 * 128 + 16 * ((4 * h + 2 * ht + s) ^ (l31 & 7))) = pack8(e, s);
    }
    STAMP1(3);
#pragma unroll
    for (int i = 0; i < 4; ++i) {
      const int n = 8 * i + (lane >> 3);
      const u32x4 v = *LDSP(u32x4, HT + i * 1024 + lane * 16);
      if (n0 + n < nend) st16(htab, (n0 + n) * 128 + 16 * ((lane & 7) ^ (n & 7)), v);
    }
    if (h == 0 && n0 + l31 < nend) {
      ptab[n0 + l31] = u32x2{px | py << 16, pz << 16};
      ctab[n0 + l31] = (u16)(qmod(lo16(px)) >> 11 | (qmod(lo16(py)) >> 11) << 5 | (qmod(lo16(pz)) & 0xfc00u));
    }
    STAMP1(4); WAITV; STAMP1(5);
    return;
  }
  __builtin_amdgcn_s_setprio(BINPRIO);
  const int cw = bid - HW;
  const int eend = min(E, (cw + 1) * chunk);
  const __amdgpu_buffer_rsrc_t rb_s = __builtin_amdgcn_make_buffer_rsrc((void*)src, 0, eend * 4, 0x00020000);
  const __amdgpu_buffer_rsrc_t rb_d = __builtin_amdgcn_make_buffer_rsrc((void*)dst, 0, eend * 4, 0x00020000);
  const int e0 = cw * chunk + tid * 4;
  u32x4 s4[2], d4[2];
#pragma unroll
  for (int u = 0; u < 2; ++u) d4[u] = __builtin_amdgcn_raw_buffer_load_b128(rb_d, (e0 + u * 2048) * 4, 0, IDXAUX);
#pragma unroll
  for (int u = 0; u < 2; ++u) s4[u] = __builtin_amdgcn_raw_buffer_load_b128(rb_s, (e0 + u * 2048) * 4, 0, IDXAUX);
  *LDSP(u32x4, L0 + K1_CNT + wave * 1024 + lane * 16) = u32x4{0, 0, 0, 0};
#pragma unroll
  for (int i = 0; i < 3; ++i) if (i * 512 + tid < SEGQ) *LDSP(u32x4, L0 + K1_STAGE + (i * 512 + tid) * 16) = u32x4{0, 0, 0, 0};
  WAITV; STAMP1(1);
  unsigned ca[8], hi8[8], rank[8];
#pragma unroll
  for (int k = 0; k < 8; ++k) {
    const unsigned d = d4[k >> 2][k & 3];
    const unsigned b = (unsigned)(((uint64_t)d * magicBN) >> 32);
    ca[k] = L0 + K1_CNT + wave * 1024 + b * 4;
    rank[k] = SENT;
    if (e0 + (k >> 2) * 2048 + (k & 3) < eend) rank[k] = __hip_atomic_fetch_add(LDSP(unsigned, ca[k]), 1u, __ATOMIC_RELAXED, __HIP_MEMORY_SCOPE_WORKGROUP);
    const unsigned dl = d - b * (unsigned)BN;
    hi8[k] = dl + ((dl * 41u) >> 10) * 7u + 1u;
  }
  wg_barrier();
  STAMP1(2);
  if (wave == 0) {
    u32x4 c[8], tot = {0, 0, 0, 0};
#pragma unroll
    for (int w = 0; w < 8; ++w) { c[w] = *LDSP(u32x4, L0 + K1_CNT + w * 1024 + lane * 16); tot += c[w]; }
    const u32x4 q = (tot + 3u) >> 2;
    const unsigned ls = q[0] + q[1] + q[2] + q[3];
    const unsigned incl = wave_incl_scan(ls);
    u32x4 st; st[0] = incl - ls; st[1] = st[0] + q[0]; st[2] = st[1] + q[1]; st[3] = st[2] + q[2];
    u32x4 run = st * 4u;
#pragma unroll
    for (int w = 0; w < 8; ++w) { *LDSP(u32x4, L0 + K1_CNT + w * 1024 + lane * 16) = run; run += c[w]; }
    if (lane == 63) *LDSP(unsigned, L0 + K1_TOT) = incl;
    st16(T, cw * 1024 + 16 * lane, st | (q << 16));
  }
  wg_barrier();
  STAMP1(3);
  unsigned base[8];
#pragma unroll
  for (int k = 0; k < 8; ++k) base[k] = *LDSP(unsigned, ca[k]);
#pragma unroll
  for (int k = 0; k < 8; ++k)
    if (rank[k] != SENT) *LDSP(unsigned, L0 + K1_STAGE + (base[k] + rank[k]) * 4) = s4[k >> 2][k & 3] << 3 | hi8[k] << 24;
  const int total = *LDSP(int, L0 + K1_TOT);
  wg_barrier();
#pragma unroll
  for (int i = 0; i < 3; ++i) {
    const int qd = i * 512 + tid;
    if (qd < total) st16(seg, (cw * SEGQ + qd) * 16, *LDSP(u32x4, L0 + K1_STAGE + qd * 16));
  }
  STAMP1(4); WAITV; STAMP1(5);
}

#if defined(__HIP_DEVICE_COMPILE__)
#pragma clang attribute push(__attribute__((target("no-packed-fp32-ops"))), apply_to = function)
#endif
DI u32x4 eqmask4(u32x4 a, unsigned b, unsigned ones) {
  u32x4 x;
#pragma unroll
  for (int i = 0; i < 4; ++i) x[i] = a[i] ^ b;
  asm("v_pk_min_u16 %0, %0, %4\n\tv_pk_min_u16 %1, %1, %4\n\tv_pk_min_u16 %2, %2, %4\n\tv_pk_min_u16 %3, %3, %4\n\t"
      "v_pk_sub_u16 %0, %0, %4\n\tv_pk_sub_u16 %1, %1, %4\n\tv_pk_sub_u16 %2, %2, %4\n\tv_pk_sub_u16 %3, %3, %4"
      : "+v"(x[0]), "+v"(x[1]), "+v"(x[2]), "+v"(x[3]) : "s"(ones));
  return x;
}

typedef __attribute__((ext_vector_type(2))) short s16x2;
extern "C" __global__ __launch_bounds__(512, 2) void k_agg(const u16* __restrict__ sv, const char* __restrict__ wf, u16* __restrict__ out, int N,
                                                           int W, int BN, int B, int bpx) {
  const int tid = TID, lane0 = tid & 63; (void)lane0;
  const int wave = __builtin_amdgcn_readfirstlane(tid >> 6);
  const int b = ((int)BID & 7) * bpx + ((int)BID >> 3);
  if (b >= B) return;
  unsigned long long* diag = (unsigned long long*)((char*)wf + (64 << 20)); (void)diag;
  STAMP(0);
  constexpr unsigned L0 = 0;
  const int node0 = b * BN;
  const int nb = min(BN, N - node0);
  const uint64_t svp = (uint64_t)sv, wfp = (uint64_t)wf;
  const u32x4 rs_sv = {(unsigned)svp, (unsigned)(svp >> 32), (unsigned)N * 512u, 0x00020000u};
  const u32x4 rs_ws = {(unsigned)wfp, (unsigned)(wfp >> 32), (unsigned)WS_END, 0x00020000u};
#define FRESH_LANE int lane; asm volatile("v_mbcnt_lo_u32_b32 %0, -1, 0\n\tv_mbcnt_hi_u32_b32 %0, -1, %0" : "=v"(lane)); const int l31 = lane & 31, h = lane >> 5; (void)l31; (void)h
  const unsigned TB = L0 + L_TB + wave * TBS;

  f32x16 acc[4][2];
  u32x4 hN[4];
  int batch = 0, r1 = 0;
  bool first = true;
  const int grp = wave;
  while (true) {
    u32x4 ol[13];
    {
      FRESH_LANE;
      const unsigned MAP = L0 + L_LIST + wave * LISTS;
      const int cpw = (W + 7) >> 3;
      const int cell = wave * cpw + lane;
      unsigned t;
      asm volatile("s_nop 4\n\tbuffer_load_dword %0, %1, %2, 0 offen" : "=v"(t) : "v"((lane < cpw && cell < W) ? (int)WS_T + (cell * 256 + b) * 4 : 0x7fffffff), "s"(rs_ws) : "memory");
      __builtin_amdgcn_s_barrier();
      const unsigned lo = 16u * lane;
      const unsigned v4[4] = {lo, lo + 1024u, lo + 2048u, lo + 3072u};
#pragma unroll
      for (int i = 0; i < NEARLY; ++i) dma16x4(rs_ws, v4, WS_C + (wave * 3 + i) * 4096, L_TB + (wave * 3 + i) * 4096);
      u32x2 dp;
      asm volatile("s_nop 4\n\tbuffer_load_dwordx2 %0, %1, %2, 0 offen" : "=&v"(dp) : "v"((int)WS_P + (node0 + wave * GN + lane - 1) * 8), "s"(rs_ws) : "memory");
      if (lane < 8) *LDSP(unsigned, L_CNT + lane * 32 + wave * 4) = lane >> 3;
      { const unsigned z = opq(0u); *LDSP(u32x4, L_RAW + wave * 1024 + lo) = u32x4{z, z, z, z}; }
      asm volatile("s_waitcnt vmcnt(%1)" : "+v"(t) : "i"(4 * NEARLY + 1) : "memory");
      const int q = t >> 16;
      const int incl = wave_incl_scan(q), excl = incl - q;
      const int total = __builtin_amdgcn_readlane(incl, 63);
      STAMP(1);
      const int win0 = batch * BQ;
      const int gq = (int)(WS_SEG / 16) + cell * SEGQ + (int)(t & 0xffffu) - excl;
      const int hi = min(incl, win0 + BQ);
#pragma nounroll
      for (int i = max(excl, win0); i < hi; ++i) *LDSP(int, MAP + (i - win0) * 4) = (gq + i) * 16;
      if (lane == 0) *LDSP(int, L0 + L_NB + wave * 4) = (total + BQ - 1) / BQ;
      const bool third = total > win0 + 128;
      int mq[3];
#pragma unroll
      for (int s = 0; s < 3; ++s) mq[s] = *LDSP(int, MAP + (64 * s + lane) * 4);
      u32x4 r4[3];
#pragma unroll
      for (int s = 0; s < 3; ++s) if (win0 + 64 * s + lane >= total) mq[s] = 0x7fffffff;
      asm volatile("s_nop 4\n\tbuffer_load_dwordx4 %0, %3, %6, 0 offen\n\tbuffer_load_dwordx4 %1, %4, %6, 0 offen\n\tbuffer_load_dwordx4 %2, %5, %6, 0 offen"
                   : "=&v"(r4[0]), "=&v"(r4[1]), "=&v"(r4[2]) : "v"(mq[0]), "v"(mq[1]), "v"(mq[2]), "s"(rs_ws) : "memory");
#pragma unroll
      for (int i = NEARLY; i < 3; ++i) dma16x4(rs_ws, v4, WS_C + (wave * 3 + i) * 4096, L_TB + (wave * 3 + i) * 4096);
      asm volatile("s_waitcnt vmcnt(%1)" : "+v"(dp) : "i"(15 - 4 * NEARLY) : "memory");
      if (wave < 4) dma16(rs_ws, WS_C + (96 + wave) * 1024u + lo, L_TB + (96 + wave) * 1024);
      const unsigned oo = first ? (unsigned)(node0 + wave * GN + h) * 512u + 16u * (l31 ^ h) : 0x7fff0000u;
#define OWNA(i) ((oo ^ (32u * ((i) & 7))) + 1024u * (i))
#define OWN4(j) asm volatile("s_nop 4\n\tbuffer_load_dwordx4 %0, %4, %8, 0 offen\n\tbuffer_load_dwordx4 %1, %5, %8, 0 offen\n\t" \
                             "buffer_load_dwordx4 %2, %6, %8, 0 offen\n\tbuffer_load_dwordx4 %3, %7, %8, 0 offen" \
                             : "=&v"(ol[4 * j]), "=&v"(ol[4 * j + 1]), "=&v"(ol[4 * j + 2]), "=&v"(ol[4 * j + 3]) \
                             : "v"(OWNA(4 * j)), "v"(OWNA(4 * j + 1)), "v"(OWNA(4 * j + 2)), "v"(OWNA(4 * j + 3)), "s"(rs_sv) : "memory")
      OWN4(0);
      if (lane < 32) {
        const unsigned qx = (qmod(lo16(dp[0])) - 1024u) & 0xffffu, qy = qmod(hi16(dp[0])) - 1024u, qz = (qmod(__uint_as_float(dp[1])) - 512u) & 0xffffu;
        *LDSP(u32x4, L_DPOS + (32 * wave + lane) * 16) = (unsigned)(lane - 1) < (unsigned)GN ? u32x4{dp[0], dp[1], qx | qy << 16, qz} : u32x4{0x7fc07fc0u, 0x7fc00000u, 0u, 0x7fff0000u};
      }
      asm volatile("s_waitcnt vmcnt(4)" : "+v"(r4[0]), "+v"(r4[1]), "+v"(r4[2])::"memory");
      STAMP(2);
      wg_barrier();
      OWN4(1);
      const unsigned one = opq(1u);
#pragma unroll
      for (int s = 0; s < 3; ++s) {
        if (s == 2 && !third) break;
        unsigned cd[4];
        u32x2 dq[4];
#pragma unroll
        for (int c = 0; c < 4; ++c) {
          cd[c] = *LDSP(u16, L_TB + ((r4[s][c] >> 2) & 0x3fffffu));
          dq[c] = *LDSP(u32x2, L_DPOS + 8 + ((r4[s][c] >> 20) & 0xff0u));
        }
        unsigned rank[4];
#pragma unroll
        for (int c = 0; c < 4; ++c) {
          const unsigned cxy = ((cd[c] & 0x3ffu) * 0x400800u) & 0xf800f800u, cz = cd[c] & 0xfc00u;
          const unsigned qxy = dq[c][0], qz = dq[c][1];
          const s16x2 dxy = __builtin_bit_cast(s16x2, qxy) - __builtin_bit_cast(s16x2, cxy);
          const s16x2 dz = __builtin_bit_cast(s16x2, qz) - __builtin_bit_cast(s16x2, cz);
          const unsigned d2 = __builtin_amdgcn_sdot2(dxy, dxy, __builtin_amdgcn_sdot2(dz, dz, 0, false), false);
          rank[c] = 0x7fffffffu;
          if (d2 < 488400000u)
            rank[c] = __hip_atomic_fetch_add(LDSP(unsigned, L_CNT + (((r4[s][c] >> 24) & 0xe0u) | wave * 4)), one, __ATOMIC_RELAXED, __HIP_MEMORY_SCOPE_WORKGROUP);
        }
        if (s == 0) {
          OWN4(2);
          asm volatile("s_nop 4\n\tbuffer_load_dwordx4 %0, %1, %2, 0 offen" : "=&v"(ol[12]) : "v"(h ? 0x7fff0000u : OWNA(12)), "s"(rs_sv) : "memory");
        }
#pragma unroll
        for (int c = 0; c < 4; ++c) {
          const unsigned p = rank[c] - SUBC * r1;
          if (p < (unsigned)SUBC) *LDSP(unsigned, L_RAW + (((r4[s][c] >> 19) & 0x1c00u) | wave * 4) + p * 32) = r4[s][c];
        }
      }
      if (wave < 7) dma16x4(rs_ws, v4, wave * 4096, L_W + wave * 4096);
      else {
        dma16(rs_ws, 28 * 1024u + lo, L_W + 28 * 1024); dma16(rs_ws, 29 * 1024u + lo, L_W + 29 * 1024);
        if (lane < 4) dma16(rs_ws, WS_F + lo, L_FR);
      }
    }
    STAMP(3);
    wg_barrier();
    STAMP(4);
    bool more1, moreb;
    for (int r2 = 0;; ++r2) {
    FRESH_LANE;
    const unsigned LIST = L0 + L_LIST + grp * LISTS;
    int nact = 0, nstep;
    bool lighter;
    unsigned exy[4], ezd[4], ep[4];
    float ef[4];
    u32x4 own[16];
    {
      unsigned rec[4];
      u32x2 ps[4], pd[4];
#pragma unroll
      for (int i = 0; i < 4; ++i) rec[i] = *LDSP(unsigned, L_RAW + grp * 1024 + lane * 4 + i * 256);
      {
        asm volatile("s_nop 4\n\tbuffer_load_dwordx2 %0, %4, %8, %9 offen\n\tbuffer_load_dwordx2 %1, %5, %8, %9 offen\n\t"
                     "buffer_load_dwordx2 %2, %6, %8, %9 offen\n\tbuffer_load_dwordx2 %3, %7, %8, %9 offen"
                     : "=&v"(ps[0]), "=&v"(ps[1]), "=&v"(ps[2]), "=&v"(ps[3])
                     : "v"(rec[0] & 0xffffffu), "v"(rec[1] & 0xffffffu), "v"(rec[2] & 0xffffffu), "v"(rec[3] & 0xffffffu), "s"(rs_ws), "s"((unsigned)WS_P) : "memory");
      }
#pragma unroll
      for (int i = 0; i < 4; ++i) pd[i] = *LDSP(u32x2, L0 + L_DPOS + ((rec[i] >> 20) & 0xff0u));
      nstep = __builtin_amdgcn_ballot_w64(rec[3] != 0) ? 4 : __builtin_amdgcn_ballot_w64(rec[2] != 0) ? 3 : 2;
      {
        const u32x4 c0 = *LDSP(u32x4, L_CNT + (lane & 7) * 32), c1 = *LDSP(u32x4, L_CNT + (lane & 7) * 32 + 16);
        const int nbv = *LDSP(int, L_NB + (lane & 7) * 4);
        const unsigned mx = max(max(max(c0[0], c0[1]), max(c0[2], c0[3])), max(max(c1[0], c1[1]), max(c1[2], c1[3])));
        more1 = __builtin_amdgcn_ballot_w64(mx > (unsigned)(SUBC * (r1 + 1))) != 0;
        moreb = __builtin_amdgcn_ballot_w64(nbv > batch + 1) != 0;
        const int key = (c0[0] + c0[1] + c0[2] + c0[3] + c1[0] + c1[1] + c1[2] + c1[3]) * 2 + (((lane & 7) >> 2) ^ 1);
        lighter = HEAVYFIRST ? __builtin_amdgcn_readlane(key, grp) < __builtin_amdgcn_readlane(key, grp ^ 4) : wave >= 4;
      }
#pragma unroll
      for (int i = 0; i < 2; ++i) *LDSP(unsigned, LIST + (64 * i + lane) * 4) = opq(0xffffffu);
      if (lane < 32) *LDSP(unsigned, LIST + (128 + lane) * 4) = opq(0xffffffu);
      *LDSP(unsigned, LIST + O_D + lane * 4) = 0x3f803f80u;
      *LDSP(unsigned, LIST + O_DL + lane * 4) = 0xffffffffu;
      STAMP(11);
      asm volatile("s_waitcnt vmcnt(4)" : "+v"(ol[0]), "+v"(ol[1]), "+v"(ol[2]), "+v"(ol[3]), "+v"(ol[4]),
                   "+v"(ol[5]), "+v"(ol[6]), "+v"(ol[7]), "+v"(ol[8]), "+v"(ol[9]), "+v"(ol[10]), "+v"(ol[11]), "+v"(ol[12])::"memory");
#pragma unroll
      for (int i = 0; i < 12; ++i) *LDSP(u32x4, TB + i * 1024 + lane * 16) = ol[i];
      if (h == 0) *LDSP(u32x4, TB + 12 * 1024 + lane * 16) = ol[12];
      {
        const unsigned ra = TB + l31 * 512 + 16 * ((4 * h) ^ (l31 & 15));
#pragma unroll
        for (int i = 0; i < 16; ++i) own[i] = *LDSP(u32x4, (ra ^ (16 * (8 * ((i >> 2) & 1) + (i & 3)))) + 256 * (i >> 3));
      }
      asm volatile("s_waitcnt vmcnt(0)" : "+v"(ps[0]), "+v"(ps[1]), "+v"(ps[2]), "+v"(ps[3])::"memory");
      STAMP(12);
#pragma unroll
      for (int i = 0; i < 4; ++i) {
        exy[i] = 0; ezd[i] = 0; ef[i] = 0.f; ep[i] = SENT;
        if (i >= nstep) continue;
        const unsigned xy = pk2(lo16(ps[i][0]) - lo16(pd[i][0]), hi16(ps[i][0]) - hi16(pd[i][0]));
        const float z = rb(__uint_as_float(ps[i][1]) - __uint_as_float(pd[i][1]));
        const float x = lo16(xy), y = hi16(xy);
        const unsigned sq = pk2(x * x, y * y);
        const float q2 = rb(rb(lo16(sq) + hi16(sq) + rb(z * z)) + 1e-8f);
        const unsigned zd = pk2(z, __builtin_amdgcn_sqrtf(q2));
        const float d = hi16(zd);
        const float f = 0.5f * rb(rb(__builtin_amdgcn_cosf(rb(rb(3.141592653589793f * d) * 0.2f) * 0.15915494309189535f)) + 1.0f);
        const bool ok = (d < 5.0f) & (f != 0.0f);
        const uint64_t bm = __builtin_amdgcn_ballot_w64(ok);
        const unsigned p = __builtin_amdgcn_mbcnt_hi((unsigned)(bm >> 32), __builtin_amdgcn_mbcnt_lo((unsigned)bm, nact - CAP * r2));
        nact += __builtin_popcountll(bm);
        exy[i] = xy; ezd[i] = zd; ef[i] = f;
        if (ok & (p < (unsigned)CAP)) {
          ep[i] = p;
          *LDSP(unsigned, LIST + swap23(p) * 4) = rec[i] & 0xffffffu;
          *LDSP(u16, LIST + O_D + swap23(p) * 2) = (u16)(zd >> 16);
          *LDSP(u16, LIST + O_DL + p * 2) = (u16)(rec[i] >> 24);
        }
      }
    }
    const int cnt = min(nact - CAP * r2, CAP);
    const int ntile = (cnt + 31) >> 5;
    auto issue_h2 = [&](unsigned off) {
      asm volatile("s_nop 4\n\tbuffer_load_dwordx4 %0, %4, %5, %6 offen\n\tbuffer_load_dwordx4 %1, %4, %5, %6 offen offset:16\n\t"
                   "buffer_load_dwordx4 %2, %4, %5, %6 offen offset:32\n\tbuffer_load_dwordx4 %3, %4, %5, %6 offen offset:48"
                   : "=&v"(hN[0]), "=&v"(hN[1]), "=&v"(hN[2]), "=&v"(hN[3]) : "v"(off), "s"(rs_ws), "s"((unsigned)WS_H) : "memory");
    };
    auto issue_h = [&](int t) { FRESH_LANE; issue_h2(*LDSP(unsigned, LIST + (32 * t + l31) * 4) * 16u + 64u * h); };
    auto wait_h = [&]() {
      asm volatile("s_waitcnt vmcnt(12)" : "+v"(hN[0]), "+v"(hN[1]), "+v"(hN[2]), "+v"(hN[3])::"memory");
    };
    unsigned vo[4];
    auto prep_v = [&](int t) {
      FRESH_LANE;
      const int drow = lane >> 3;
      const unsigned dchunk = 16u * (((lane & 7) ^ (drow & 2)) + 8);
#pragma unroll
      for (int i = 0; i < 4; ++i) vo[i] = *LDSP(unsigned, LIST + (32 * t + 8 * i + drow) * 4) * 64u + dchunk;
    };
    auto issue_v = [&](int k) {
      asm volatile("s_waitcnt lgkmcnt(0)" ::: "memory");
      dma16x4(rs_sv, vo, 128 * k, TB + 4096 * k);
    };
    u32x4 rbf;
    auto calc_rbf = [&](int t) {
      FRESH_LANE;
      const float d = lo16(*LDSP(u16, LIST + O_D + 64 * t + 2 * l31));
      const f32x4 f0 = *LDSP(f32x4, L_FR + 32 * h), f1 = *LDSP(f32x4, L_FR + 32 * h + 16);
      const float inv = __builtin_amdgcn_rcpf(d);
#pragma unroll
      for (int i = 0; i < 4; ++i) {
        const float fa = i < 2 ? f0[2 * i] : f1[2 * i - 4], fb = i < 2 ? f0[2 * i + 1] : f1[2 * i - 3];
        const unsigned x = pk2(d * fa, d * fb);
        float ra = lo16(x) * 0.15915494309189535f, rc = hi16(x) * 0.15915494309189535f;
        ra -= __builtin_rintf(ra); rc -= __builtin_rintf(rc);
        rbf[i] = pk2(__builtin_amdgcn_sinf(ra) * inv, __builtin_amdgcn_sinf(rc) * inv);
      }
      asm volatile("" : "+v"(rbf));
    };
    setprio(3);
    issue_h(0);
    prep_v(0);
    STAMP(5);
    if (r2 == 0) __builtin_amdgcn_s_barrier();
    const bool late = STAG >= 1 && r2 == 0 && lighter;
    if (!late) {
      issue_v(0); issue_v(1); issue_v(2);
      if (STAG >= 1 && r2 == 0) __builtin_amdgcn_s_barrier();
    }
    setprio(P_START);
    SB();
#pragma unroll
    for (int i = 0; i < 4; ++i) {
      if (i >= nstep) break;
      const float inv = __builtin_amdgcn_rcpf(hi16(ezd[i])), f = ef[i];
      const unsigned hxy = pk2(lo16(exy[i]) * inv, hi16(exy[i]) * inv);
      const float hz = rb(lo16(ezd[i]) * inv);
      const unsigned fxy = pk2(f * lo16(hxy), f * hi16(hxy));
      const unsigned fzf = pk2(f * hz, f);
      if (ep[i] != SENT) {
        const unsigned a2 = LIST + ep[i] * 2;
        *LDSP(u16, a2 + O_F) = (u16)(fzf >> 16);
        *LDSP(u16, a2 + O_FX) = (u16)fxy;
        *LDSP(u16, a2 + O_FX + 256) = (u16)(fxy >> 16);
        *LDSP(u16, a2 + O_FX + 512) = (u16)fzf;
      }
    }
    if (first) {
#pragma unroll
      for (int i = 0; i < 16; ++i)
#pragma unroll
        for (int k = 0; k < 4; ++k) {
          acc[i >> 2][(i >> 1) & 1][8 * (i & 1) + 2 * k] = lo16(own[i][k]);
          acc[i >> 2][(i >> 1) & 1][8 * (i & 1) + 2 * k + 1] = hi16(own[i][k]);
        }
      first = false;
    }
    calc_rbf(0);
    SB();
    if (late) {
      __builtin_amdgcn_s_barrier();
      issue_v(0); issue_v(1); issue_v(2);
    }
    wait_h();
    STAMP(6);
    auto tile = [&](int t, auto nsc) {
      constexpr int NS = decltype(nsc)::value;
      FRESH_LANE;
      const unsigned WL = L0 + L_W + lane * 16, la = LIST + 64 * t + 16 * h;
      const unsigned mypair = (unsigned)(grp * 32 + l31 + 1) * 0x10001u;
      u32x4 Wb[5];
      auto ldW = [&](int ct) {
        Wb[4] = *LDSP(u32x4, WL + (24 + ct) * 1024);
#pragma unroll
        for (int ks = 0; ks < 4; ++ks) Wb[ks] = *LDSP(u32x4, WL + (4 * ct + ks) * 1024);
      };
      ldW(4);
      u32x4 msk[2], S1[2], sr[3][2];
#pragma unroll
      for (int s = 0; s < NS; ++s) msk[s] = *LDSP(u32x4, la + O_DL + 32 * s);
#pragma unroll
      for (int s = 0; s < NS; ++s)
#pragma unroll
        for (int k = 0; k < 3; ++k) sr[k][s] = *LDSP(u32x4, la + O_FX + 256 * k + 32 * s);
      SB();
      f32x16 ph, w;
      auto gemm = [&]() {
        w = mfma(rbf, Wb[4], zero16());
        ph = mfma(hN[0], Wb[0], zero16());
#pragma unroll
        for (int ks = 1; ks < 4; ++ks) ph = mfma(hN[ks], Wb[ks], ph);
      };
#define PIN1(a) asm volatile("" : "+v"(a))
#define PIN2(a, b) do { PIN1(a); PIN1(b); } while (0)
#define PIN3(a, b, c) asm volatile("" : "+v"(a), "+v"(b), "+v"(c))
      auto pack = [&](u32x4 (&a)[2]) {
        const f32x16 m = ph * w;
#pragma unroll
        for (int s = 0; s < NS; ++s) { a[s] = pack8(m, s); PIN1(a[s]); }
      };
#define MIX(id, lead, n, v) do { __builtin_amdgcn_sched_group_barrier(0x8, lead, id); _Pragma("unroll") for (int i_ = 0; i_ < n; ++i_) { \
        __builtin_amdgcn_sched_group_barrier(0x402, v, id); __builtin_amdgcn_sched_group_barrier(0x8, 1, id); } } while (0)
      u32x4 aa[2], ab[2];
      gemm();
#pragma unroll
      for (int s = 0; s < NS; ++s) {
        msk[s] = eqmask4(msk[s], mypair, 0x00010001u);
#pragma unroll
        for (int k = 0; k < 3; ++k) sr[k][s] &= msk[s];
      }
      MIX(0, 1, 4, 4 * NS);
      PIN2(ph, w);
      SB();
      ldW(5);
      SB();
      pack(aa);
      SB();
      gemm();
      PIN2(ph, w);
      SB();
      ldW(0);
      PIN2(aa[0], aa[NS - 1]);
      SB();
#pragma unroll
      for (int s = 0; s < NS; ++s)
#pragma unroll
        for (int k = 0; k < 3; ++k) acc[1 + k][0] = mfma(aa[s], sr[k][s], acc[1 + k][0]);
      pack(ab);
      MIX(1, 2, 3 * NS - 2, 6);
      PIN3(acc[1][0], acc[2][0], acc[3][0]);
      SB();
      gemm();
      PIN2(ph, w);
      SB();
      ldW(1);
      PIN2(ab[0], ab[NS - 1]);
      SB();
#pragma unroll
      for (int s = 0; s < NS; ++s)
#pragma unroll
        for (int k = 0; k < 3; ++k) acc[1 + k][1] = mfma(ab[s], sr[k][s], acc[1 + k][1]);
      pack(aa);
      MIX(2, 2, 3 * NS - 2, 6);
      PIN3(acc[1][1], acc[2][1], acc[3][1]);
      SB();
      unsigned tra, se, dn;
      f32x4 f0, f1;
      u32x4 rbn;
      {
        FRESH_LANE;
        const unsigned la2 = LIST + 64 * t + 16 * h, mp2 = (unsigned)(grp * 32 + l31 + 1) * 0x10001u;
#pragma unroll
        for (int s = 0; s < NS; ++s) { msk[s] = *LDSP(u32x4, la2 + O_DL + 32 * s); S1[s] = *LDSP(u32x4, la2 + O_F + 32 * s); }
        dn = *LDSP(u16, LIST + O_D + 64 * (t + 1) + 2 * l31);
        f0 = *LDSP(f32x4, L_FR + 32 * h); f1 = *LDSP(f32x4, L_FR + 32 * h + 16);
        se = *LDSP(unsigned, LIST + (32 * (t + 1) + l31) * 4);
#pragma unroll
        for (int i = 0; i < 4; ++i) vo[i] = *LDSP(unsigned, LIST + (32 * (t + 1) + 8 * i + (lane >> 3)) * 4);
        SB();
        gemm();
#pragma unroll
        for (int s = 0; s < NS; ++s) { S1[s] &= eqmask4(msk[s], mp2, 0x00010001u); PIN1(S1[s]); }
        const unsigned dchunk = 16u * (((lane & 7) ^ ((lane >> 3) & 2)) + 8);
#pragma unroll
        for (int i = 0; i < 4; ++i) { vo[i] = vo[i] * 64u + dchunk; PIN1(vo[i]); }
        se = se * 16u + 64u * h; PIN1(se);
        const int q4 = (lane >> 2) & 3, pp = lane & 3;
        tra = (q4 & 1) ? TB + (4 * h + (q4 >> 1)) * 128 + 64 * (pp & 1) + (lane & 16) + 8 * (pp >> 1) : 0x100000u;
        PIN1(tra);
        MIX(3, 2, 3, 8);
        PIN2(ph, w);
        SB();
      }
      ldW(2);
      PIN2(aa[0], aa[NS - 1]);
      SB();
#pragma unroll
      for (int s = 0; s < NS; ++s) acc[0][0] = mfma(aa[s], S1[s], acc[0][0]);
      PIN3(acc[0][0], ph, w);
      SB();
      pack(ab);
      SB();
      const float dnf = lo16(dn), inv = __builtin_amdgcn_rcpf(dnf);
      auto rbf2 = [&](float fa, float fb) {
        const unsigned x = pk2(dnf * fa, dnf * fb);
        float ra = lo16(x) * 0.15915494309189535f, rc = hi16(x) * 0.15915494309189535f;
        ra -= __builtin_rintf(ra); rc -= __builtin_rintf(rc);
        return pk2(__builtin_amdgcn_sinf(ra) * inv, __builtin_amdgcn_sinf(rc) * inv);
      };
      gemm();
      { unsigned r0 = rbf2(f0[0], f0[1]), r1_ = rbf2(f0[2], f0[3]); PIN2(r0, r1_); rbn[0] = r0; rbn[1] = r1_; }
      MIX(4, 1, 4, 6);
      PIN2(ph, w);
      SB();
      ldW(3);
      PIN2(ab[0], ab[NS - 1]);
      SB();
#pragma unroll
      for (int s = 0; s < NS; ++s) acc[0][1] = mfma(ab[s], S1[s], acc[0][1]);
      PIN3(acc[0][1], ph, w);
      SB();
      f32x16 mv[2];
      mv[0] = ph * w;
      PIN1(mv[0]);
      SB();
      gemm();
      { unsigned r0 = rbf2(f1[0], f1[1]), r1_ = rbf2(f1[2], f1[3]); PIN2(r0, r1_); rbn[2] = r0; rbn[3] = r1_; }
      MIX(5, 1, 4, 6);
      PIN2(ph, w);
      SB();
      issue_h2(se);
      rbf = rbn;
      mv[1] = ph * w;
      PIN1(mv[1]);
      SB();
      P_PH2;
      asm volatile("s_waitcnt vmcnt(4)" ::: "memory");
      if (t < 2) STAMP(7 + 2 * t);
      u32x2 va[8], vb[8];
      auto ldv = [&](u32x2 (&v)[8], int t2, int k) {
#pragma unroll
        for (int i = 0; i < 4 * NS; ++i)
          v[i] = trread(tra + k * 4096 + (8 * (i >> 1) + 2 * (i & 1)) * 128 + 32 * (t2 ^ (i & 1)));
      };
      auto vv = [&](const u32x2 (&v)[8], const f32x16& m, f32x16& ac) {
        u32x4 a[2];
#pragma unroll
        for (int i = 0; i < 4 * NS; ++i) {
          const unsigned v0 = v[i][0], v1 = v[i][1];
          a[i >> 2][i & 3] = pk2(m[2 * i] * __uint_as_float(v0), m[2 * i + 1] * __uint_as_float(v1));
        }
#pragma unroll
        for (int s = 0; s < NS; ++s) ac = mfma(a[s], S1[s], ac);
      };
      ldv(va, 0, 0); ldv(vb, 1, 0);
      SB();
      vv(va, mv[0], acc[1][0]);
      SB();
      issue_v(0);
      ldv(va, 0, 1);
      SB();
      vv(vb, mv[1], acc[1][1]);
      SB();
      ldv(vb, 1, 1);
      SB();
      vv(va, mv[0], acc[2][0]);
      SB();
      issue_v(1);
      ldv(va, 0, 2);
      SB();
      vv(vb, mv[1], acc[2][1]);
      SB();
      ldv(vb, 1, 2);
      SB();
      vv(va, mv[0], acc[3][0]);
      SB();
      issue_v(2);
      if (t < 2) STAMP(8 + 2 * t);
      vv(vb, mv[1], acc[3][1]);
      SB();
      wait_h();
    };
    const int nfull = (cnt + 15) >> 5;
    for (int t = 0; t < nfull; ++t) { tile(t, std::integral_constant<int, 2>{}); setprio(P_NEXT); }
    if (nfull < ntile) tile(nfull, std::integral_constant<int, 1>{});
    if (nact <= CAP * (r2 + 1)) break;
    asm volatile("s_waitcnt vmcnt(0)" ::: "memory");
#pragma unroll
    for (int i = 0; i < 13; ++i) asm volatile("" : "=v"(ol[i]));
    }
    if (more1) ++r1;
    else if (moreb) { ++batch; r1 = 0; }
    else break;
    asm volatile("s_waitcnt vmcnt(0)" ::: "memory");
    wg_barrier();
  }
  STAMP(13);
  {
    FRESH_LANE;
    u32x4 o[16];
#pragma unroll
    for (int i = 0; i < 16; ++i) o[i] = pack8(acc[i >> 2][(i >> 1) & 1], i & 1);
    asm volatile("s_waitcnt vmcnt(0)" ::: "memory");
    if (l31 < GN) {
      const unsigned wa = TB + l31 * 512 + 16 * ((4 * h) ^ (l31 & 7));
#pragma unroll
      for (int i = 0; i < 16; ++i) *LDSP(u32x4, (wa ^ (16 * (i & 3))) + 128 * (i >> 2)) = o[i];
    }
    const int gw = __builtin_amdgcn_readfirstlane(max(min(nb - grp * GN, GN), 0));
    const uint64_t op = (uint64_t)out + (size_t)(node0 + grp * GN) * 512;
    const __amdgpu_buffer_rsrc_t rb_o = __builtin_amdgcn_make_buffer_rsrc((void*)op, 0, NOST * gw * 512, 0x00020000);
    u32x4 v[(GN + 1) / 2];
#pragma unroll
    for (int i = 0; i < (GN + 1) / 2; ++i) v[i] = *LDSP(u32x4, TB + i * 1024 + lane * 16);
    const unsigned oa = h * 512 + 16 * (l31 ^ h);
#pragma unroll
    for (int i = 0; i < (GN + 1) / 2; ++i) __builtin_amdgcn_raw_buffer_store_b128(v[i], rb_o, oa ^ (32 * (i & 3)), i * 1024, K2AUX);
  }
  asm volatile("painn_code_hi:");
  STAMP(14);
  STAMP(15);
}

#if defined(__HIP_DEVICE_COMPILE__)
#pragma clang attribute pop
#endif
extern "C" void kernel_launch(void* const* d_in, const int* in_sizes, int n_in, void* d_out, int out_size, void* d_ws, size_t ws_size,
                              hipStream_t stream) {
  const u16* sv = (const u16*)d_in[0];
  const u16* pos = (const u16*)d_in[1];
  const int* src = (const int*)d_in[2];
  const int* dst = (const int*)d_in[3];
  const u16* fr = (const u16*)d_in[4];
  const u16* w1 = (const u16*)d_in[5];
  const u16* w2 = (const u16*)d_in[6];
  const u16* wr = (const u16*)d_in[7];
  const int N = in_sizes[1] / 3, E = in_sizes[2];
  const int BN = (N + 255) / 256 < 2 ? 2 : (N + 255) / 256;
  const int B = (N + BN - 1) / BN, bpx = (B + 7) / 8;
#define HWN 200
#define WTGT 196
  int chunk = (((E + WTGT - 1) / WTGT) + 3) & ~3;
  if (chunk < 1024) chunk = 1024;
  const int W = (E + chunk - 1) / chunk;
  const int NPX = bpx * BN, HW = HWN, WP = (W + 7) & ~7;
  const unsigned magicBN = (unsigned)(0x100000000ull / (unsigned)BN) + 1u;
  char* ws = (char*)d_ws;
  (void)hipFuncSetAttribute((const void*)k_agg, hipFuncAttributeMaxDynamicSharedMemorySize, L_END);
  k_bin<<<WP + HW + 8, 512, 0, stream>>>(src, dst, (unsigned*)(ws + WS_T), (u32x4*)(ws + WS_SEG), sv, pos, w1, w2, wr, fr, (u32x4*)ws,
                                        (u32x2*)(ws + WS_P), (u16*)(ws + WS_C), ws + WS_H, N, E, W, HW, magicBN, BN, WP, NPX, chunk);
  k_agg<<<8 * bpx, 512, L_END, stream>>>(sv, ws, (u16*)d_out, N, W, BN, B, bpx);
}
```

```cpp
#if defined(__HIP_DEVICE_COMPILE__)
#pragma clang attribute push(__attribute__((target("no-packed-fp32-ops"))), apply_to = function)
#endif
#include <hip/hip_runtime.h>
#include <stdint.h>
#include <type_traits>

#define TID __builtin_amdgcn_workitem_id_x()
#define BID __builtin_amdgcn_workgroup_id_x()
typedef unsigned short u16;
typedef __attribute__((ext_vector_type(8))) __bf16 bf16x8;
typedef __attribute__((ext_vector_type(2))) __bf16 bf16x2;
typedef __attribute__((ext_vector_type(4))) short s16x4;
typedef __attribute__((ext_vector_type(2))) float f32x2;
typedef __attribute__((ext_vector_type(4))) float f32x4;
typedef __attribute__((ext_vector_type(16))) float f32x16;
typedef __attribute__((ext_vector_type(4))) unsigned u32x4;
typedef __attribute__((ext_vector_type(3))) unsigned u32x3;
typedef __attribute__((ext_vector_type(2))) unsigned u32x2;
#define LDSP(T, off) ((__attribute__((address_space(3))) T*)(uintptr_t)(off))
#define DI __device__ __forceinline__
#define SB() __builtin_amdgcn_sched_barrier(0)

constexpr int WT = 17;
constexpr int CH = 4096;
constexpr int SEGQ = CH / 4 + 192;
constexpr int GN = 25;
constexpr int NFRAG = 30;
constexpr int SUBC = 64;
constexpr int CAP = 128;
constexpr int BQ = 256;
constexpr unsigned SENT = 0xFFFFFFFFu;
constexpr int L_DPOS = 0;
constexpr int L_CNT = 4096;
constexpr int L_NB = L_CNT + 256;
constexpr int L_RAW = L_NB + 64;
constexpr int L_LIST = L_RAW + 1024;
constexpr int O_D = 640, O_DL = O_D + 256, O_F = O_DL + 256, O_FX = O_F + 256;
constexpr int LISTS = 1024 + O_FX + 768;
constexpr int L_W = L_RAW + 8 * LISTS;
constexpr int L_FR = L_W + NFRAG * 1024;
constexpr int L_TB = (L_FR + 64 + 511) / 512 * 512;
constexpr int TBS = GN * 512;
constexpr int L_END = L_TB + 8 * TBS;
static_assert(L_END <= 163840 && L_TB < 65536 && L_W % 16 == 0 && LISTS % 16 == 0 && SUBC == 64, "LDS");
constexpr size_t WS_T = 65536;
constexpr size_t WS_SEG = WS_T + 256 * 256 * 4;
constexpr size_t WS_P = WS_SEG + (size_t)256 * SEGQ * 16;
constexpr size_t WS_C = WS_P + 51200 * 8;
constexpr size_t WS_H = WS_C + 51200 * 2;
constexpr size_t WS_END = WS_H + 51200 * 128;
constexpr size_t WS_F = NFRAG * 1024;
static_assert(WS_END < 0xffffff, "ws");

DI float lo16(unsigned x) { return __uint_as_float(x << 16); }
DI float hi16(unsigned x) { return __uint_as_float(x & 0xffff0000u); }
DI unsigned pk2(float a, float b) { f32x2 v = {a, b}; return __builtin_bit_cast(unsigned, __builtin_convertvector(v, bf16x2)); }
DI float rb(float a) { return hi16(pk2(a, a)); }
DI f32x16 mfma(u32x4 a, u32x4 b, f32x16 c) {
  return __builtin_amdgcn_mfma_f32_32x32x16_bf16(__builtin_bit_cast(bf16x8, a), __builtin_bit_cast(bf16x8, b), c, 0, 0, 0);
}
DI f32x16 zero16() { f32x16 z;
#pragma unroll
  for (int r = 0; r < 16; ++r) z[r] = 0.f;
  return z; }
DI int wave_incl_scan(int v) {
  v += __builtin_amdgcn_update_dpp(0, v, 0x111, 0xf, 0xf, false);
  v += __builtin_amdgcn_update_dpp(0, v, 0x112, 0xf, 0xf, false);
  v += __builtin_amdgcn_update_dpp(0, v, 0x114, 0xf, 0xf, false);
  v += __builtin_amdgcn_update_dpp(0, v, 0x118, 0xf, 0xf, false);
  v += __builtin_amdgcn_update_dpp(0, v, 0x142, 0xa, 0xf, false);
  v += __builtin_amdgcn_update_dpp(0, v, 0x143, 0xc, 0xf, false);
  return v;
}
DI int chan_of(int t2, int i32) { return 32 * ((i32 >> 2) & 1) + 16 * t2 + 4 * (i32 >> 3) + (i32 & 3); }
DI u32x4 pack8(const f32x16& x, int s) {
  return u32x4{pk2(x[8 * s], x[8 * s + 1]), pk2(x[8 * s + 2], x[8 * s + 3]), pk2(x[8 * s + 4], x[8 * s + 5]), pk2(x[8 * s + 6], x[8 * s + 7])};
}
DI void setprio(int p) {
  if (p >= 3) __builtin_amdgcn_s_setprio(3);
  else if (p == 2) __builtin_amdgcn_s_setprio(2);
  else if (p == 1) __builtin_amdgcn_s_setprio(1);
  else __builtin_amdgcn_s_setprio(0);
}
DI u32x2 trread(unsigned a) { return __builtin_bit_cast(u32x2, __builtin_amdgcn_ds_read_tr16_b64_v4i16(LDSP(s16x4, a))); }
DI void st16(void* base, unsigned off, u32x4 v) {
  const __amdgpu_buffer_rsrc_t r = __builtin_amdgcn_make_buffer_rsrc(base, 0, 0x7fffffff, 0x00020000);
  __builtin_amdgcn_raw_buffer_store_b128(v, r, off, 0, WT);
}
DI unsigned qmod(float x) { return min((unsigned)(__builtin_amdgcn_fractf(x * 0.0625f) * 65536.0f), 65535u); }
DI unsigned opq(unsigned c) { asm volatile("" : "+v"(c)); return c; }
DI unsigned swap23(unsigned p) { return (p & ~12u) | ((p & 4u) << 1) | ((p & 8u) >> 1); }
DI void wg_barrier() { asm volatile("s_waitcnt lgkmcnt(0)\n\ts_barrier" ::: "memory"); }

DI void dma16(u32x4 rsrc, unsigned voff, unsigned ldsaddr) {
  asm volatile("s_mov_b32 m0, %2\n\ts_nop 3\n\tbuffer_load_dwordx4 %0, %1, 0 offen lds" ::"v"(voff), "s"(rsrc), "s"(ldsaddr) : "memory");
}
DI void dma16x4(u32x4 rsrc, const unsigned (&voff)[4], unsigned soff, unsigned m0) {
  asm volatile("s_mov_b32 m0, %6\n\ts_nop 3\n\tbuffer_load_dwordx4 %0, %4, %5 offen lds\n\t"
               "s_add_u32 m0, m0, 0x400\n\ts_nop 0\n\tbuffer_load_dwordx4 %1, %4, %5 offen lds\n\t"
               "s_add_u32 m0, m0, 0x400\n\ts_nop 0\n\tbuffer_load_dwordx4 %2, %4, %5 offen lds\n\t"
               "s_add_u32 m0, m0, 0x400\n\ts_nop 0\n\tbuffer_load_dwordx4 %3, %4, %5 offen lds"
               ::"v"(voff[0]), "v"(voff[1]), "v"(voff[2]), "v"(voff[3]), "s"(rsrc), "s"(soff), "s"(m0) : "memory", "scc");
}
constexpr int K1_CNT = 0, K1_STAGE = 8 * 256 * 4, K1_TOT = K1_STAGE + SEGQ * 16, K1_W1 = 0, K1_HT = 8192, K1_END = K1_HT + 8 * 4096;
#if defined(__HIP_DEVICE_COMPILE__)
#pragma clang attribute pop
#endif
extern "C" __global__ __launch_bounds__(512, 4) void k_bin(const int* __restrict__ src, const int* __restrict__ dst, unsigned* __restrict__ T,
                                                u32x4* __restrict__ seg, const u16* __restrict__ sv, const u16* __restrict__ pos,
                                                const u16* __restrict__ w1, const u16* __restrict__ w2, const u16* __restrict__ wr, const u16* __restrict__ freqs,
                                                u32x4* __restrict__ wf, u32x2* __restrict__ ptab, u16* __restrict__ ctab, char* __restrict__ htab, int N, int E,
                                                int W, int HW, unsigned magicBN, int BN, int WP, int NPX, int chunk) {
  __shared__ __attribute__((aligned(16))) char lds_raw[K1_END + 256];
  const unsigned L0 = (unsigned)(uintptr_t)(__attribute__((address_space(3))) char*)lds_raw;
  const int tid = TID, lane = tid & 63;
  const int wave = __builtin_amdgcn_readfirstlane(tid >> 6);
  int bid = BID;
  if (bid < WP + HW) { if (bid < WP) { if (bid >= W) return; bid += HW; } else bid -= WP; } else bid += W - WP;
  if (wave == 0) {
    uint64_t pc; unsigned len;
    asm volatile("s_getpc_b64 %0\npainn_self_lo:\n\ts_mov_b32 %1, painn_code_hi-painn_self_lo" : "=s"(pc), "=s"(len));
    const u32x4 rc = {(unsigned)pc, (unsigned)(pc >> 32) & 0xffffu, len, 0x00020000u};
    asm volatile("s_mov_b32 m0, %2\n\ts_nop 3\n\tbuffer_load_dword %0, %1, 0 offen lds" ::"v"(lane * 128), "s"(rc), "s"(L0 + K1_END) : "memory");
  }
  if (bid >= W + HW) {
    {
      uint64_t pc; unsigned len;
      asm volatile("s_getpc_b64 %0\npainn_code_lo:\n\ts_mov_b32 %1, painn_code_hi-painn_code_lo" : "=s"(pc), "=s"(len));
      const __amdgpu_buffer_rsrc_t rc = __builtin_amdgcn_make_buffer_rsrc((void*)pc, 0, len, 0x00020000);
      const unsigned x = __builtin_amdgcn_raw_buffer_load_b32(rc, tid * 64, 0, 0);
      asm volatile("" ::"v"(x));
    }
    const int idx = (bid - W - HW) * 512 + tid;
    if (idx >= NFRAG * 64) {
      if (idx < NFRAG * 64 + 16) ((unsigned*)wf)[WS_F / 4 + idx - NFRAG * 64] = (unsigned)freqs[idx - NFRAG * 64] << 16;
      return;
    }
    const int f = idx >> 6, i32 = lane & 31, h = lane >> 5;
    const u16* p; int s2;
    if (f < 24) { const int ct = f >> 2; p = w2 + (16 * (f & 3) + 4 * h) * 192 + 64 * (ct >> 1) + chan_of(ct & 1, i32); s2 = 8 * 192; }
    else { const int ct = f - 24; p = wr + 8 * h * 192 + 64 * (ct >> 1) + chan_of(ct & 1, i32); s2 = 4 * 192; }
    unsigned v[8];
#pragma unroll
    for (int j = 0; j < 8; ++j) v[j] = p[(j & 3) * 192 + (j >> 2) * s2];
    wf[idx] = u32x4{v[0] | v[1] << 16, v[2] | v[3] << 16, v[4] | v[5] << 16, v[6] | v[7] << 16};
    return;
  }
  if (bid < HW) {
    const int l31 = lane & 31, h = lane >> 5;
    const int nl = (wave * (HW >> 3) + (bid >> 3)) * 32, nend = min(N, ((bid & 7) + 1) * NPX);
    const int n0 = nl < NPX ? (bid & 7) * NPX + nl : N;
    const int nn = min(n0 + l31, N - 1);
    const unsigned HT = L0 + K1_HT + wave * 4096;
    {
      const uint64_t sp = (uint64_t)sv, wp = (uint64_t)w1;
      const u32x4 rs_sv = {(unsigned)sp, (unsigned)(sp >> 32), (unsigned)N * 512u, 0x00020000u};
      const u32x4 rs_w1 = {(unsigned)wp, (unsigned)(wp >> 32), 8192u, 0x00020000u};
      const unsigned o = (unsigned)(n0 + (lane >> 3)) * 512u + 16u * ((lane & 7) ^ (lane >> 3));
      const unsigned v4[4] = {o, o + 4096u, o + 8192u, o + 12288u};
      dma16x4(rs_sv, v4, 0, HT);
      dma16(rs_w1, tid * 16u, L0 + K1_W1 + wave * 1024);
    }
    unsigned px = pos[nn * 3], py = pos[nn * 3 + 1], pz = pos[nn * 3 + 2];
    asm volatile("s_waitcnt vmcnt(0)\n\ts_barrier" : "+v"(px), "+v"(py), "+v"(pz)::"memory");
    if (n0 >= N) return;
    {
      const uint64_t sp = (uint64_t)sv;
      const u32x4 rs_sv = {(unsigned)sp, (unsigned)(sp >> 32), (unsigned)N * 512u, 0x00020000u};
      asm volatile("s_mov_b32 m0, %2\n\ts_nop 3\n\tbuffer_load_dword %0, %1, 0 offen lds"
                   ::"v"(h == 0 ? (unsigned)(n0 + l31) * 512u + 128u : 0x7fffffffu), "s"(rs_sv), "s"(L0 + K1_END) : "memory");
    }
    u32x4 sf[4];
#pragma unroll
    for (int ks = 0; ks < 4; ++ks) sf[ks] = *LDSP(u32x4, HT + l31 * 128 + 16 * ((4 * h + ks) ^ (l31 & 7)));
    const unsigned ta = L0 + K1_W1 + (32 * h + ((lane >> 2) & 3)) * 128 + (16 * ((lane >> 4) & 1) + 4 * (lane & 3)) * 2;
    f32x16 d1[2];
#pragma unroll
    for (int ht = 0; ht < 2; ++ht) {
      d1[ht] = zero16();
#pragma unroll
      for (int ks = 0; ks < 4; ++ks) {
        const u32x2 a0 = trread(ta + ks * 1024 + ht * 64), a1 = trread(ta + ks * 1024 + 512 + ht * 64);
        d1[ht] = mfma(u32x4{a0[0], a0[1], a1[0], a1[1]}, sf[ks], d1[ht]);
      }
    }
#pragma unroll
    for (int ht = 0; ht < 2; ++ht) {
      f32x16 e = d1[ht] * -1.4426950408889634f;
#pragma unroll
      for (int r = 0; r < 16; ++r) e[r] = __builtin_amdgcn_exp2f(e[r]);
      e += 1.0f;
#pragma unroll
      for (int r = 0; r < 16; ++r) e[r] = __builtin_amdgcn_rcpf(e[r]);
      e *= d1[ht];
#pragma unroll
      for (int s = 0; s < 2; ++s) *LDSP(u32x4, HT + l31 * 128 + 16 * ((4 * h + 2 * ht + s) ^ (l31 & 7))) = pack8(e, s);
    }
#pragma unroll
    for (int i = 0; i < 4; ++i) {
      const int n = 8 * i + (lane >> 3);
      const u32x4 v = *LDSP(u32x4, HT + i * 1024 + lane * 16);
      if (n0 + n < nend) st16(htab, (n0 + n) * 128 + 16 * ((lane & 7) ^ (n & 7)), v);
    }
    if (h == 0 && n0 + l31 < nend) {
      ptab[n0 + l31] = u32x2{px | py << 16, pz << 16};
      ctab[n0 + l31] = (u16)(qmod(lo16(px)) >> 11 | (qmod(lo16(py)) >> 11) << 5 | (qmod(lo16(pz)) & 0xfc00u));
    }
    return;
  }
  const int cw = bid - HW;
  const int eend = min(E, (cw + 1) * chunk);
  const __amdgpu_buffer_rsrc_t rb_s = __builtin_amdgcn_make_buffer_rsrc((void*)src, 0, eend * 4, 0x00020000);
  const __amdgpu_buffer_rsrc_t rb_d = __builtin_amdgcn_make_buffer_rsrc((void*)dst, 0, eend * 4, 0x00020000);
  const int e0 = cw * chunk + tid * 4;
  u32x4 s4[2], d4[2];
#pragma unroll
  for (int u = 0; u < 2; ++u) d4[u] = __builtin_amdgcn_raw_buffer_load_b128(rb_d, (e0 + u * 2048) * 4, 0, 0);
#pragma unroll
  for (int u = 0; u < 2; ++u) s4[u] = __builtin_amdgcn_raw_buffer_load_b128(rb_s, (e0 + u * 2048) * 4, 0, 0);
  *LDSP(u32x4, L0 + K1_CNT + wave * 1024 + lane * 16) = u32x4{0, 0, 0, 0};
#pragma unroll
  for (int i = 0; i < 3; ++i) if (i * 512 + tid < SEGQ) *LDSP(u32x4, L0 + K1_STAGE + (i * 512 + tid) * 16) = u32x4{0, 0, 0, 0};
  unsigned ca[8], hi8[8], rank[8];
#pragma unroll
  for (int k = 0; k < 8; ++k) {
    const unsigned d = d4[k >> 2][k & 3];
    const unsigned b = (unsigned)(((uint64_t)d * magicBN) >> 32);
    ca[k] = L0 + K1_CNT + wave * 1024 + b * 4;
    rank[k] = SENT;
    if (e0 + (k >> 2) * 2048 + (k & 3) < eend) rank[k] = __hip_atomic_fetch_add(LDSP(unsigned, ca[k]), 1u, __ATOMIC_RELAXED, __HIP_MEMORY_SCOPE_WORKGROUP);
    const unsigned dl = d - b * (unsigned)BN;
    hi8[k] = dl + ((dl * 41u) >> 10) * 7u + 1u;
  }
  wg_barrier();
  if (wave == 0) {
    u32x4 c[8], tot = {0, 0, 0, 0};
#pragma unroll
    for (int w = 0; w < 8; ++w) { c[w] = *LDSP(u32x4, L0 + K1_CNT + w * 1024 + lane * 16); tot += c[w]; }
    const u32x4 q = (tot + 3u) >> 2;
    const unsigned ls = q[0] + q[1] + q[2] + q[3];
    const unsigned incl = wave_incl_scan(ls);
    u32x4 st; st[0] = incl - ls; st[1] = st[0] + q[0]; st[2] = st[1] + q[1]; st[3] = st[2] + q[2];
    u32x4 run = st * 4u;
#pragma unroll
    for (int w = 0; w < 8; ++w) { *LDSP(u32x4, L0 + K1_CNT + w * 1024 + lane * 16) = run; run += c[w]; }
    if (lane == 63) *LDSP(unsigned, L0 + K1_TOT) = incl;
    st16(T, cw * 1024 + 16 * lane, st | (q << 16));
  }
  wg_barrier();
  unsigned base[8];
#pragma unroll
  for (int k = 0; k < 8; ++k) base[k] = *LDSP(unsigned, ca[k]);
#pragma unroll
  for (int k = 0; k < 8; ++k)
    if (rank[k] != SENT) *LDSP(unsigned, L0 + K1_STAGE + (base[k] + rank[k]) * 4) = s4[k >> 2][k & 3] << 3 | hi8[k] << 24;
  const int total = *LDSP(int, L0 + K1_TOT);
  wg_barrier();
#pragma unroll
  for (int i = 0; i < 3; ++i) {
    const int qd = i * 512 + tid;
    if (qd < total) st16(seg, (cw * SEGQ + qd) * 16, *LDSP(u32x4, L0 + K1_STAGE + qd * 16));
  }
}

#if defined(__HIP_DEVICE_COMPILE__)
#pragma clang attribute push(__attribute__((target("no-packed-fp32-ops"))), apply_to = function)
#endif
DI u32x4 eqmask4(u32x4 a, unsigned b, unsigned ones) {
  u32x4 x;
#pragma unroll
  for (int i = 0; i < 4; ++i) x[i] = a[i] ^ b;
  asm("v_pk_min_u16 %0, %0, %4\n\tv_pk_min_u16 %1, %1, %4\n\tv_pk_min_u16 %2, %2, %4\n\tv_pk_min_u16 %3, %3, %4\n\t"
      "v_pk_sub_u16 %0, %0, %4\n\tv_pk_sub_u16 %1, %1, %4\n\tv_pk_sub_u16 %2, %2, %4\n\tv_pk_sub_u16 %3, %3, %4"
      : "+v"(x[0]), "+v"(x[1]), "+v"(x[2]), "+v"(x[3]) : "s"(ones));
  return x;
}

typedef __attribute__((ext_vector_type(2))) short s16x2;
extern "C" __global__ __launch_bounds__(512, 2) void k_agg(const u16* __restrict__ sv, const char* __restrict__ wf, u16* __restrict__ out, int N,
                                                           int W, int BN, int B, int bpx) {
  const int wave = __builtin_amdgcn_readfirstlane(TID >> 6);
  const int b = ((int)BID & 7) * bpx + ((int)BID >> 3);
  if (b >= B) return;
  constexpr unsigned L0 = 0;
  const int node0 = b * BN;
  const int nb = min(BN, N - node0);
  const uint64_t svp = (uint64_t)sv, wfp = (uint64_t)wf;
  const u32x4 rs_sv = {(unsigned)svp, (unsigned)(svp >> 32), (unsigned)N * 512u, 0x00020000u};
  const u32x4 rs_ws = {(unsigned)wfp, (unsigned)(wfp >> 32), (unsigned)WS_END, 0x00020000u};
#define FRESH_LANE int lane; asm volatile("v_mbcnt_lo_u32_b32 %0, -1, 0\n\tv_mbcnt_hi_u32_b32 %0, -1, %0" : "=v"(lane)); const int l31 = lane & 31, h = lane >> 5; (void)l31; (void)h
  const unsigned TB = L0 + L_TB + wave * TBS;

  f32x16 acc[4][2];
  u32x4 hN[4];
  int batch = 0, r1 = 0, r2 = 0;
  bool first = true;
  const int grp = wave;
  while (true) {
    u32x4 ol[13];
    {
      FRESH_LANE;
      const unsigned lo = 16u * lane;
      const unsigned v4[4] = {lo, lo + 1024u, lo + 2048u, lo + 3072u};
      const unsigned oo = first ? (unsigned)(node0 + wave * GN + h) * 512u + 16u * (l31 ^ h) : 0x7fff0000u;
#define OWNA(i) ((oo ^ (32u * ((i) & 7))) + 1024u * (i))
#define OWN4(j) asm volatile("s_nop 4\n\tbuffer_load_dwordx4 %0, %4, %8, 0 offen\n\tbuffer_load_dwordx4 %1, %5, %8, 0 offen\n\t" \
                             "buffer_load_dwordx4 %2, %6, %8, 0 offen\n\tbuffer_load_dwordx4 %3, %7, %8, 0 offen" \
                             : "=&v"(ol[4 * j]), "=&v"(ol[4 * j + 1]), "=&v"(ol[4 * j + 2]), "=&v"(ol[4 * j + 3]) \
                             : "v"(OWNA(4 * j)), "v"(OWNA(4 * j + 1)), "v"(OWNA(4 * j + 2)), "v"(OWNA(4 * j + 3)), "s"(rs_sv) : "memory")
#define OWN1() asm volatile("s_nop 4\n\tbuffer_load_dwordx4 %0, %1, %2, 0 offen" : "=&v"(ol[12]) : "v"(h ? 0x7fff0000u : OWNA(12)), "s"(rs_sv) : "memory")
      if (wave >= 4) {
        const int rw = wave - 4;
        const unsigned MAP = L_LIST + wave * LISTS + 1024;
        const int cpw = (W + 3) >> 2;
        const int cell = rw * cpw + lane;
        unsigned t;
        u32x2 dp;
        asm volatile("s_nop 4\n\tbuffer_load_dword %0, %2, %4, 0 offen\n\tbuffer_load_dwordx2 %1, %3, %4, 0 offen\n\ts_barrier\n\ts_waitcnt vmcnt(0)"
                     : "=&v"(t), "=&v"(dp) : "v"((lane < cpw && cell < W) ? (int)WS_T + (cell * 256 + b) * 4 : 0x7fffffff),
                       "v"((int)WS_P + (node0 + (2 * rw + h) * GN + l31 - 1) * 8), "s"(rs_ws) : "memory");
        const int q = t >> 16;
        const int incl = wave_incl_scan(q), excl = incl - q;
        const int total = __builtin_amdgcn_readlane(incl, 63);
        const int win0 = batch * BQ;
        const int gq = (int)(WS_SEG / 16) + cell * SEGQ + (int)(t & 0xffffu) - excl;
        const int hi = min(incl, win0 + BQ);
#pragma nounroll
        for (int i = max(excl, win0); i < hi; ++i) *LDSP(int, MAP + (i - win0) * 4) = (gq + i) * 16;
        unsigned mq[4];
#pragma unroll
        for (int s = 0; s < 4; ++s) mq[s] = *LDSP(int, MAP + (64 * s + lane) * 4);
#pragma unroll
        for (int s = 0; s < 4; ++s) if (win0 + 64 * s + lane >= total) mq[s] = 0x7fffffffu;
        dma16x4(rs_ws, mq, 0, L_W + rw * 4096);
        if (lane == 0) *LDSP(int, L_NB + rw * 4) = (total + BQ - 1) / BQ;
        const unsigned qx = (qmod(lo16(dp[0])) - 1024u) & 0xffffu, qy = qmod(hi16(dp[0])) - 1024u, qz = (qmod(__uint_as_float(dp[1])) - 512u) & 0xffffu;
        *LDSP(u32x4, L_DPOS + rw * 1024 + lo) = (unsigned)(l31 - 1) < (unsigned)GN ? u32x4{dp[0], dp[1], qx | qy << 16, qz} : u32x4{0x7fc07fc0u, 0x7fc00000u, 0u, 0x7fff0000u};
      } else {
        __builtin_amdgcn_s_barrier();
#pragma unroll
        for (int i = 0; i < 6; ++i) dma16x4(rs_ws, v4, WS_C + (wave * 6 + i) * 4096, L_TB + (wave * 6 + i) * 4096);
        dma16(rs_ws, WS_C + (96 + wave) * 1024u + lo, L_TB + (96 + wave) * 1024);
      }
      if (lane < 8) *LDSP(unsigned, L_CNT + lane * 32 + wave * 4) = lane >> 3;
      { const unsigned z = opq(0u); *LDSP(u32x4, L_RAW + wave * LISTS + lo) = u32x4{z, z, z, z}; *LDSP(u32x4, L_LIST + wave * LISTS + lo) = u32x4{z, z, z, z}; }
      OWN4(0); OWN1();
      asm volatile("s_waitcnt vmcnt(5)" ::: "memory");
      wg_barrier();
      u32x4 r4[2];
#pragma unroll
      for (int s = 0; s < 2; ++s) r4[s] = *LDSP(u32x4, L_W + (2 * s + h) * 4096 + (l31 >> 2) * 512 + wave * 64 + (lane & 3) * 16);
      OWN4(1);
      const unsigned one = opq(1u);
#pragma unroll
      for (int s = 0; s < 2; ++s) {
        unsigned cd[4];
        u32x2 dq[4];
#pragma unroll
        for (int c = 0; c < 4; ++c) {
          cd[c] = *LDSP(u16, L_TB + ((r4[s][c] >> 2) & 0x3fffffu));
          dq[c] = *LDSP(u32x2, L_DPOS + 8 + ((r4[s][c] >> 20) & 0xff0u));
        }
        unsigned rank[4];
#pragma unroll
        for (int c = 0; c < 4; ++c) {
          const unsigned cxy = ((cd[c] & 0x3ffu) * 0x400800u) & 0xf800f800u, cz = cd[c] & 0xfc00u;
          const unsigned qxy = dq[c][0], qz = dq[c][1];
          const s16x2 dxy = __builtin_bit_cast(s16x2, qxy) - __builtin_bit_cast(s16x2, cxy);
          const s16x2 dz = __builtin_bit_cast(s16x2, qz) - __builtin_bit_cast(s16x2, cz);
          const unsigned d2 = __builtin_amdgcn_sdot2(dxy, dxy, __builtin_amdgcn_sdot2(dz, dz, 0, false), false);
          rank[c] = 0x7fffffffu;
          if (d2 < 488400000u)
            rank[c] = __hip_atomic_fetch_add(LDSP(unsigned, L_CNT + (((r4[s][c] >> 24) & 0xe0u) | wave * 4)), one, __ATOMIC_RELAXED, __HIP_MEMORY_SCOPE_WORKGROUP);
        }
        if (s == 0) OWN4(2);
#pragma unroll
        for (int c = 0; c < 4; ++c) {
          const unsigned p = rank[c] - SUBC * r1;
          if (p < (unsigned)SUBC) *LDSP(unsigned, L_RAW + (r4[s][c] >> 29) * LISTS + (p * 32 + wave * 4)) = r4[s][c];
        }
      }
    }
    wg_barrier();
    bool more1, moreb;
    FRESH_LANE;
    const unsigned LIST = L0 + L_LIST + grp * LISTS;
    int nact = 0, nstep;
    bool lighter;
    unsigned exy[4], ezd[4], ep[4];
    float ef[4];
    u32x4 own[16];
    auto weights = [&](unsigned xy, unsigned zd, float f, unsigned p) {
      const float inv = __builtin_amdgcn_rcpf(hi16(zd));
      const unsigned hxy = pk2(lo16(xy) * inv, hi16(xy) * inv);
      const float hz = rb(lo16(zd) * inv);
      const unsigned fxy = pk2(f * lo16(hxy), f * hi16(hxy));
      const unsigned fzf = pk2(f * hz, f);
      if (p != SENT) {
        const unsigned a2 = LIST + p * 2;
        *LDSP(u16, a2 + O_F) = (u16)(fzf >> 16);
        *LDSP(u16, a2 + O_FX) = (u16)fxy;
        *LDSP(u16, a2 + O_FX + 256) = (u16)(fxy >> 16);
        *LDSP(u16, a2 + O_FX + 512) = (u16)fzf;
      }
    };
    {
      unsigned rec[4], rex[4];
      u32x2 ps[4], pd[4];
#pragma unroll
      for (int i = 0; i < 4; ++i) rec[i] = *LDSP(unsigned, L_RAW + grp * LISTS + lane * 4 + i * 256);
#pragma unroll
      for (int i = 0; i < 4; ++i) rex[i] = *LDSP(unsigned, L_RAW + grp * LISTS + lane * 4 + 1024 + i * 256);
      {
        asm volatile("s_nop 4\n\tbuffer_load_dwordx2 %0, %4, %8, %9 offen\n\tbuffer_load_dwordx2 %1, %5, %8, %9 offen\n\t"
                     "buffer_load_dwordx2 %2, %6, %8, %9 offen\n\tbuffer_load_dwordx2 %3, %7, %8, %9 offen"
                     : "=&v"(ps[0]), "=&v"(ps[1]), "=&v"(ps[2]), "=&v"(ps[3])
                     : "v"(rec[0] & 0xffffffu), "v"(rec[1] & 0xffffffu), "v"(rec[2] & 0xffffffu), "v"(rec[3] & 0xffffffu), "s"(rs_ws), "s"((unsigned)WS_P) : "memory");
      }
#pragma unroll
      for (int i = 0; i < 4; ++i) pd[i] = *LDSP(u32x2, L0 + L_DPOS + ((rec[i] >> 20) & 0xff0u));
      nstep = __builtin_amdgcn_ballot_w64(rec[3] != 0) ? 4 : __builtin_amdgcn_ballot_w64(rec[2] != 0) ? 3 : 2;
      {
        const u32x4 c0 = *LDSP(u32x4, L_CNT + (lane & 7) * 32), c1 = *LDSP(u32x4, L_CNT + (lane & 7) * 32 + 16);
        const int nbv = *LDSP(int, L_NB + (lane & 3) * 4);
        const unsigned mx = max(max(max(c0[0], c0[1]), max(c0[2], c0[3])), max(max(c1[0], c1[1]), max(c1[2], c1[3])));
        more1 = __builtin_amdgcn_ballot_w64(mx > (unsigned)(SUBC * (r1 + 1))) != 0;
        moreb = __builtin_amdgcn_ballot_w64(nbv > batch + 1) != 0;
        const int key = (c0[0] + c0[1] + c0[2] + c0[3] + c1[0] + c1[1] + c1[2] + c1[3]) * 2 + (((lane & 7) >> 2) ^ 1);
        lighter = __builtin_amdgcn_readlane(key, grp) < __builtin_amdgcn_readlane(key, grp ^ 4);
      }
#pragma unroll
      for (int i = 0; i < 2; ++i) *LDSP(unsigned, LIST + (64 * i + lane) * 4) = opq(0xffffffu);
      if (lane < 32) *LDSP(unsigned, LIST + (128 + lane) * 4) = opq(0xffffffu);
      *LDSP(unsigned, LIST + O_D + lane * 4) = 0x3f803f80u;
      *LDSP(unsigned, LIST + O_DL + lane * 4) = 0xffffffffu;
      asm volatile("s_waitcnt vmcnt(4)" : "+v"(ol[0]), "+v"(ol[1]), "+v"(ol[2]), "+v"(ol[3]), "+v"(ol[4]),
                   "+v"(ol[5]), "+v"(ol[6]), "+v"(ol[7]), "+v"(ol[8]), "+v"(ol[9]), "+v"(ol[10]), "+v"(ol[11]), "+v"(ol[12])::"memory");
#pragma unroll
      for (int i = 0; i < 12; ++i) *LDSP(u32x4, TB + i * 1024 + lane * 16) = ol[i];
      if (h == 0) *LDSP(u32x4, TB + 12 * 1024 + lane * 16) = ol[12];
      {
        const unsigned ra = TB + l31 * 512 + 16 * ((4 * h) ^ (l31 & 15));
#pragma unroll
        for (int i = 0; i < 16; ++i) own[i] = *LDSP(u32x4, (ra ^ (16 * (8 * ((i >> 2) & 1) + (i & 3)))) + 256 * (i >> 3));
      }
      asm volatile("s_waitcnt vmcnt(0)" : "+v"(ps[0]), "+v"(ps[1]), "+v"(ps[2]), "+v"(ps[3])::"memory");
      auto exact = [&](unsigned rc, u32x2 a, u32x2 c, unsigned& oxy, unsigned& ozd, float& of, unsigned& op) {
        const unsigned xy = pk2(lo16(a[0]) - lo16(c[0]), hi16(a[0]) - hi16(c[0]));
        const float z = rb(__uint_as_float(a[1]) - __uint_as_float(c[1]));
        const float x = lo16(xy), y = hi16(xy);
        const unsigned sq = pk2(x * x, y * y);
        const float q2 = rb(rb(lo16(sq) + hi16(sq) + rb(z * z)) + 1e-8f);
        const unsigned zd = pk2(z, __builtin_amdgcn_sqrtf(q2));
        const float d = hi16(zd);
        const float f = 0.5f * rb(rb(__builtin_amdgcn_cosf(rb(rb(3.141592653589793f * d) * 0.2f) * 0.15915494309189535f)) + 1.0f);
        const bool ok = (d < 5.0f) & (f != 0.0f);
        const uint64_t bm = __builtin_amdgcn_ballot_w64(ok);
        const unsigned p = __builtin_amdgcn_mbcnt_hi((unsigned)(bm >> 32), __builtin_amdgcn_mbcnt_lo((unsigned)bm, nact - CAP * r2));
        nact += __builtin_popcountll(bm);
        oxy = xy; ozd = zd; of = f; op = SENT;
        if (ok & (p < (unsigned)CAP)) {
          op = p;
          *LDSP(unsigned, LIST + swap23(p) * 4) = rc & 0xffffffu;
          *LDSP(u16, LIST + O_D + swap23(p) * 2) = (u16)(zd >> 16);
          *LDSP(u16, LIST + O_DL + p * 2) = (u16)(rc >> 24);
        }
      };
#pragma unroll
      for (int i = 0; i < 4; ++i) {
        exy[i] = 0; ezd[i] = 0; ef[i] = 0.f; ep[i] = SENT;
        if (i < nstep) exact(rec[i], ps[i], pd[i], exy[i], ezd[i], ef[i], ep[i]);
      }
#pragma nounroll
      while (__builtin_amdgcn_ballot_w64(rex[0] != 0)) {
        u32x2 a;
        asm volatile("s_nop 4\n\tbuffer_load_dwordx2 %0, %1, %2, %3 offen\n\ts_waitcnt vmcnt(0)" : "=&v"(a) : "v"(rex[0] & 0xffffffu), "s"(rs_ws), "s"((unsigned)WS_P) : "memory");
        unsigned xy, zd, p; float f;
        exact(rex[0], a, *LDSP(u32x2, L_DPOS + ((rex[0] >> 20) & 0xff0u)), xy, zd, f, p);
        weights(xy, zd, f, p);
        rex[0] = rex[1]; rex[1] = rex[2]; rex[2] = rex[3]; rex[3] = 0;
      }
    }
    const int cnt = __builtin_amdgcn_readfirstlane(max(min(nact - CAP * r2, CAP), 0));
    if (lane == 0) *LDSP(int, L_NB + 32 + wave * 4) = nact > CAP * (r2 + 1);
    const int ntile = (cnt + 31) >> 5;
    auto issue_h2 = [&](unsigned off) {
      asm volatile("s_nop 4\n\tbuffer_load_dwordx4 %0, %4, %5, %6 offen\n\tbuffer_load_dwordx4 %1, %4, %5, %6 offen offset:16\n\t"
                   "buffer_load_dwordx4 %2, %4, %5, %6 offen offset:32\n\tbuffer_load_dwordx4 %3, %4, %5, %6 offen offset:48"
                   : "=&v"(hN[0]), "=&v"(hN[1]), "=&v"(hN[2]), "=&v"(hN[3]) : "v"(off), "s"(rs_ws), "s"((unsigned)WS_H) : "memory");
    };
    auto issue_h = [&](int t) { FRESH_LANE; issue_h2(*LDSP(unsigned, LIST + (32 * t + l31) * 4) * 16u + 64u * h); };
    auto wait_h = [&]() {
      asm volatile("s_waitcnt vmcnt(12)" : "+v"(hN[0]), "+v"(hN[1]), "+v"(hN[2]), "+v"(hN[3])::"memory");
    };
    unsigned vo[4];
    auto prep_v = [&](int t) {
      FRESH_LANE;
      const int drow = lane >> 3;
      const unsigned dchunk = 16u * (((lane & 7) ^ (drow & 2)) + 8);
#pragma unroll
      for (int i = 0; i < 4; ++i) vo[i] = *LDSP(unsigned, LIST + (32 * t + 8 * i + drow) * 4) * 64u + dchunk;
    };
    auto issue_v = [&](int k) {
      asm volatile("s_waitcnt lgkmcnt(0)" ::: "memory");
      dma16x4(rs_sv, vo, 128 * k, TB + 4096 * k);
    };
    u32x4 rbf;
    auto calc_rbf = [&](int t) {
      FRESH_LANE;
      const float d = lo16(*LDSP(u16, LIST + O_D + 64 * t + 2 * l31));
      const f32x4 f0 = *LDSP(f32x4, L_FR + 32 * h), f1 = *LDSP(f32x4, L_FR + 32 * h + 16);
      const float inv = __builtin_amdgcn_rcpf(d);
#pragma unroll
      for (int i = 0; i < 4; ++i) {
        const float fa = i < 2 ? f0[2 * i] : f1[2 * i - 4], fb = i < 2 ? f0[2 * i + 1] : f1[2 * i - 3];
        const unsigned x = pk2(d * fa, d * fb);
        float ra = lo16(x) * 0.15915494309189535f, rc = hi16(x) * 0.15915494309189535f;
        ra -= __builtin_rintf(ra); rc -= __builtin_rintf(rc);
        rbf[i] = pk2(__builtin_amdgcn_sinf(ra) * inv, __builtin_amdgcn_sinf(rc) * inv);
      }
      asm volatile("" : "+v"(rbf));
    };
    setprio(3);
    issue_h(0);
    prep_v(0);
    {
      const unsigned lo = 16u * lane;
      const unsigned v4[4] = {lo, lo + 1024u, lo + 2048u, lo + 3072u};
      const int wo = min(wave * 4096, NFRAG * 1024 + 64 - 4096);
      dma16x4(rs_ws, v4, wo, L_W + wo);
      wg_barrier();
    }
    const bool late = lighter;
    if (!late) {
      issue_v(0); issue_v(1); issue_v(2);
      asm volatile("s_waitcnt vmcnt(12)\n\ts_barrier" ::: "memory");
    }
    setprio(min(ntile - 1, 2));
    SB();
#pragma unroll
    for (int i = 0; i < 4; ++i) if (i < nstep) weights(exy[i], ezd[i], ef[i], ep[i]);
    if (first) {
#pragma unroll
      for (int i = 0; i < 16; ++i)
#pragma unroll
        for (int k = 0; k < 4; ++k) {
          acc[i >> 2][(i >> 1) & 1][8 * (i & 1) + 2 * k] = lo16(own[i][k]);
          acc[i >> 2][(i >> 1) & 1][8 * (i & 1) + 2 * k + 1] = hi16(own[i][k]);
        }
      first = false;
    }
    SB();
    if (late) {
      asm volatile("s_waitcnt vmcnt(0)\n\ts_barrier" ::: "memory");
      issue_v(0); issue_v(1); issue_v(2);
    }
    const bool more2 = __builtin_amdgcn_ballot_w64(*LDSP(int, L_NB + 32 + (lane & 7) * 4) != 0) != 0;
    calc_rbf(0);
    wait_h();
    auto tile = [&](int t, auto nsc) {
      constexpr int NS = decltype(nsc)::value;
      FRESH_LANE;
      const unsigned WL = L0 + L_W + lane * 16, la = LIST + 64 * t + 16 * h;
      const unsigned mypair = (unsigned)(grp * 32 + l31 + 1) * 0x10001u;
      u32x4 Wb[5];
      auto ldW = [&](int ct) {
        Wb[4] = *LDSP(u32x4, WL + (24 + ct) * 1024);
#pragma unroll
        for (int ks = 0; ks < 4; ++ks) Wb[ks] = *LDSP(u32x4, WL + (4 * ct + ks) * 1024);
      };
      ldW(4);
      u32x4 msk[2], S1[2], sr[3][2];
#pragma unroll
      for (int s = 0; s < NS; ++s) msk[s] = *LDSP(u32x4, la + O_DL + 32 * s);
#pragma unroll
      for (int s = 0; s < NS; ++s)
#pragma unroll
        for (int k = 0; k < 3; ++k) sr[k][s] = *LDSP(u32x4, la + O_FX + 256 * k + 32 * s);
      SB();
      f32x16 ph, w;
      auto gemm = [&]() {
        w = mfma(rbf, Wb[4], zero16());
        ph = mfma(hN[0], Wb[0], zero16());
#pragma unroll
        for (int ks = 1; ks < 4; ++ks) ph = mfma(hN[ks], Wb[ks], ph);
      };
#define PIN1(a) asm volatile("" : "+v"(a))
#define PIN2(a, b) do { PIN1(a); PIN1(b); } while (0)
#define PIN3(a, b, c) asm volatile("" : "+v"(a), "+v"(b), "+v"(c))
      auto pack = [&](u32x4 (&a)[2]) {
        const f32x16 m = ph * w;
#pragma unroll
        for (int s = 0; s < NS; ++s) { a[s] = pack8(m, s); PIN1(a[s]); }
      };
#define MIX(id, lead, n, v) do { __builtin_amdgcn_sched_group_barrier(0x8, lead, id); _Pragma("unroll") for (int i_ = 0; i_ < n; ++i_) { \
        __builtin_amdgcn_sched_group_barrier(0x402, v, id); __builtin_amdgcn_sched_group_barrier(0x8, 1, id); } } while (0)
      u32x4 aa[2], ab[2];
      gemm();
#pragma unroll
      for (int s = 0; s < NS; ++s) {
        msk[s] = eqmask4(msk[s], mypair, 0x00010001u);
#pragma unroll
        for (int k = 0; k < 3; ++k) sr[k][s] &= msk[s];
      }
      MIX(0, 1, 4, 4 * NS);
      PIN2(ph, w);
      SB();
      ldW(5);
      SB();
      pack(aa);
      SB();
      gemm();
      PIN2(ph, w);
      SB();
      ldW(0);
      PIN2(aa[0], aa[NS - 1]);
      SB();
#pragma unroll
      for (int s = 0; s < NS; ++s)
#pragma unroll
        for (int k = 0; k < 3; ++k) acc[1 + k][0] = mfma(aa[s], sr[k][s], acc[1 + k][0]);
      pack(ab);
      MIX(1, 2, 3 * NS - 2, 6);
      PIN3(acc[1][0], acc[2][0], acc[3][0]);
      SB();
      gemm();
      PIN2(ph, w);
      SB();
      ldW(1);
      PIN2(ab[0], ab[NS - 1]);
      SB();
#pragma unroll
      for (int s = 0; s < NS; ++s)
#pragma unroll
        for (int k = 0; k < 3; ++k) acc[1 + k][1] = mfma(ab[s], sr[k][s], acc[1 + k][1]);
      pack(aa);
      MIX(2, 2, 3 * NS - 2, 6);
      PIN3(acc[1][1], acc[2][1], acc[3][1]);
      SB();
      unsigned tra, se, dn;
      f32x4 f0, f1;
      u32x4 rbn;
      {
        FRESH_LANE;
        const unsigned la2 = LIST + 64 * t + 16 * h, mp2 = (unsigned)(grp * 32 + l31 + 1) * 0x10001u;
#pragma unroll
        for (int s = 0; s < NS; ++s) { msk[s] = *LDSP(u32x4, la2 + O_DL + 32 * s); S1[s] = *LDSP(u32x4, la2 + O_F + 32 * s); }
        dn = *LDSP(u16, LIST + O_D + 64 * (t + 1) + 2 * l31);
        f0 = *LDSP(f32x4, L_FR + 32 * h); f1 = *LDSP(f32x4, L_FR + 32 * h + 16);
        se = *LDSP(unsigned, LIST + (32 * (t + 1) + l31) * 4);
#pragma unroll
        for (int i = 0; i < 4; ++i) vo[i] = *LDSP(unsigned, LIST + (32 * (t + 1) + 8 * i + (lane >> 3)) * 4);
        SB();
        gemm();
#pragma unroll
        for (int s = 0; s < NS; ++s) { S1[s] &= eqmask4(msk[s], mp2, 0x00010001u); PIN1(S1[s]); }
        const unsigned dchunk = 16u * (((lane & 7) ^ ((lane >> 3) & 2)) + 8);
#pragma unroll
        for (int i = 0; i < 4; ++i) { vo[i] = vo[i] * 64u + dchunk; PIN1(vo[i]); }
        se = se * 16u + 64u * h; PIN1(se);
        const int q4 = (lane >> 2) & 3, pp = lane & 3;
        tra = (q4 & 1) ? TB + (4 * h + (q4 >> 1)) * 128 + 64 * (pp & 1) + (lane & 16) + 8 * (pp >> 1) : 0x100000u;
        PIN1(tra);
        MIX(3, 2, 3, 8);
        PIN2(ph, w);
        SB();
      }
      ldW(2);
      PIN2(aa[0], aa[NS - 1]);
      SB();
#pragma unroll
      for (int s = 0; s < NS; ++s) acc[0][0] = mfma(aa[s], S1[s], acc[0][0]);
      PIN3(acc[0][0], ph, w);
      SB();
      pack(ab);
      SB();
      const float dnf = lo16(dn), inv = __builtin_amdgcn_rcpf(dnf);
      auto rbf2 = [&](float fa, float fb) {
        const unsigned x = pk2(dnf * fa, dnf * fb);
        float ra = lo16(x) * 0.15915494309189535f, rc = hi16(x) * 0.15915494309189535f;
        ra -= __builtin_rintf(ra); rc -= __builtin_rintf(rc);
        return pk2(__builtin_amdgcn_sinf(ra) * inv, __builtin_amdgcn_sinf(rc) * inv);
      };
      gemm();
      { unsigned r0 = rbf2(f0[0], f0[1]), r1_ = rbf2(f0[2], f0[3]); PIN2(r0, r1_); rbn[0] = r0; rbn[1] = r1_; }
      MIX(4, 1, 4, 6);
      PIN2(ph, w);
      SB();
      ldW(3);
      PIN2(ab[0], ab[NS - 1]);
      SB();
#pragma unroll
      for (int s = 0; s < NS; ++s) acc[0][1] = mfma(ab[s], S1[s], acc[0][1]);
      PIN3(acc[0][1], ph, w);
      SB();
      f32x16 mv[2];
      mv[0] = ph * w;
      PIN1(mv[0]);
      SB();
      gemm();
      { unsigned r0 = rbf2(f1[0], f1[1]), r1_ = rbf2(f1[2], f1[3]); PIN2(r0, r1_); rbn[2] = r0; rbn[3] = r1_; }
      MIX(5, 1, 4, 6);
      PIN2(ph, w);
      SB();
      issue_h2(se);
      rbf = rbn;
      mv[1] = ph * w;
      PIN1(mv[1]);
      SB();
      __builtin_amdgcn_s_setprio(3);
      asm volatile("s_waitcnt vmcnt(4)" ::: "memory");
      u32x2 va[8], vb[8];
      auto ldv = [&](u32x2 (&v)[8], int t2, int k) {
#pragma unroll
        for (int i = 0; i < 4 * NS; ++i)
          v[i] = trread(tra + k * 4096 + (8 * (i >> 1) + 2 * (i & 1)) * 128 + 32 * (t2 ^ (i & 1)));
      };
      auto vv = [&](const u32x2 (&v)[8], const f32x16& m, f32x16& ac) {
        u32x4 a[2];
#pragma unroll
        for (int i = 0; i < 4 * NS; ++i) {
          const unsigned v0 = v[i][0], v1 = v[i][1];
          a[i >> 2][i & 3] = pk2(m[2 * i] * __uint_as_float(v0), m[2 * i + 1] * __uint_as_float(v1));
        }
#pragma unroll
        for (int s = 0; s < NS; ++s) ac = mfma(a[s], S1[s], ac);
      };
      ldv(va, 0, 0); ldv(vb, 1, 0);
      SB();
      vv(va, mv[0], acc[1][0]);
      SB();
      issue_v(0);
      ldv(va, 0, 1);
      SB();
      vv(vb, mv[1], acc[1][1]);
      SB();
      ldv(vb, 1, 1);
      SB();
      vv(va, mv[0], acc[2][0]);
      SB();
      issue_v(1);
      ldv(va, 0, 2);
      SB();
      vv(vb, mv[1], acc[2][1]);
      SB();
      ldv(vb, 1, 2);
      SB();
      vv(va, mv[0], acc[3][0]);
      SB();
      issue_v(2);
      vv(vb, mv[1], acc[3][1]);
      SB();
      wait_h();
    };
    const int nfull = (cnt + 15) >> 5;
    for (int t = 0; t < nfull; ++t) { tile(t, std::integral_constant<int, 2>{}); setprio(min(ntile - 2 - t, 2)); }
    if (nfull < ntile) tile(nfull, std::integral_constant<int, 1>{});
    if (more2) ++r2;
    else if (more1) { ++r1; r2 = 0; }
    else if (moreb) { ++batch; r1 = 0; r2 = 0; }
    else break;
    asm volatile("s_waitcnt vmcnt(0)" ::: "memory");
    wg_barrier();
  }
  {
    FRESH_LANE;
    u32x4 o[16];
#pragma unroll
    for (int i = 0; i < 16; ++i) o[i] = pack8(acc[i >> 2][(i >> 1) & 1], i & 1);
    asm volatile("s_waitcnt vmcnt(0)" ::: "memory");
    if (l31 < GN) {
      const unsigned wa = TB + l31 * 512 + 16 * ((4 * h) ^ (l31 & 7));
#pragma unroll
      for (int i = 0; i < 16; ++i) *LDSP(u32x4, (wa ^ (16 * (i & 3))) + 128 * (i >> 2)) = o[i];
    }
    const int gw = __builtin_amdgcn_readfirstlane(max(min(nb - grp * GN, GN), 0));
    const uint64_t op = (uint64_t)out + (size_t)(node0 + grp * GN) * 512;
    const __amdgpu_buffer_rsrc_t rb_o = __builtin_amdgcn_make_buffer_rsrc((void*)op, 0, gw * 512, 0x00020000);
    u32x4 v[(GN + 1) / 2];
#pragma unroll
    for (int i = 0; i < (GN + 1) / 2; ++i) v[i] = *LDSP(u32x4, TB + i * 1024 + lane * 16);
    const unsigned oa = h * 512 + 16 * (l31 ^ h);
#pragma unroll
    for (int i = 0; i < (GN + 1) / 2; ++i) __builtin_amdgcn_raw_buffer_store_b128(v[i], rb_o, oa ^ (32 * (i & 3)), i * 1024, WT);
  }
  asm volatile("painn_code_hi:");
}

extern "C" __global__ __launch_bounds__(192) void k_gen_phi(const u16* __restrict__ sv, const u16* __restrict__ w1, const u16* __restrict__ w2,
                                                            float* __restrict__ phi) {
  __shared__ float s[64], hd[64];
  const int t = TID; const size_t n = BID;
  if (t < 64) s[t] = lo16(sv[n * 256 + t]);
  wg_barrier();
  if (t < 64) {
    float a = 0.f;
    for (int k = 0; k < 64; ++k) a += s[k] * lo16(w1[k * 64 + t]);
    hd[t] = rb(a * __builtin_amdgcn_rcpf(1.0f + __builtin_amdgcn_exp2f(a * -1.4426950408889634f)));
  }
  wg_barrier();
  float a = 0.f;
  for (int k = 0; k < 64; ++k) a += hd[k] * lo16(w2[k * 192 + t]);
  phi[n * 192 + t] = a;
}
extern "C" __global__ __launch_bounds__(256) void k_gen_edge(const u16* __restrict__ sv, const u16* __restrict__ pos, const int* __restrict__ src,
                                                             const int* __restrict__ dst, const u16* __restrict__ freqs, const u16* __restrict__ wr,
                                                             const float* __restrict__ phi, float* __restrict__ acc, int E) {
  const size_t e = (size_t)BID * 4 + (TID >> 6);
  const int c = TID & 63;
  if (e >= (size_t)E) return;
  const size_t s = src[e], t = dst[e];
  const float x = rb(lo16(pos[s * 3]) - lo16(pos[t * 3])), y = rb(lo16(pos[s * 3 + 1]) - lo16(pos[t * 3 + 1])), z = rb(lo16(pos[s * 3 + 2]) - lo16(pos[t * 3 + 2]));
  const float d = rb(__builtin_amdgcn_sqrtf(rb(rb(rb(x * x) + rb(y * y) + rb(z * z)) + 1e-8f)));
  if (!(d < 5.0f)) return;
  const float inv = __builtin_amdgcn_rcpf(d);
  const float f = 0.5f * rb(rb(__builtin_amdgcn_cosf(rb(rb(3.141592653589793f * d) * 0.2f) * 0.15915494309189535f)) + 1.0f);
  float w0 = 0.f, w1 = 0.f, w2 = 0.f;
  for (int k = 0; k < 16; ++k) {
    float r = rb(d * lo16(freqs[k])) * 0.15915494309189535f;
    r -= __builtin_rintf(r);
    const float b = rb(__builtin_amdgcn_sinf(r) * inv);
    w0 += b * lo16(wr[k * 192 + c]); w1 += b * lo16(wr[k * 192 + 64 + c]); w2 += b * lo16(wr[k * 192 + 128 + c]);
  }
  const float ms = phi[s * 192 + c] * w0 * f, mv = phi[s * 192 + 64 + c] * w1 * f, mr = phi[s * 192 + 128 + c] * w2 * f;
  float* a = acc + t * 256 + c;
  __hip_atomic_fetch_add(a, ms, __ATOMIC_RELAXED, __HIP_MEMORY_SCOPE_AGENT);
  __hip_atomic_fetch_add(a + 64, mv * lo16(sv[s * 256 + 64 + c]) + mr * rb(x * inv), __ATOMIC_RELAXED, __HIP_MEMORY_SCOPE_AGENT);
  __hip_atomic_fetch_add(a + 128, mv * lo16(sv[s * 256 + 128 + c]) + mr * rb(y * inv), __ATOMIC_RELAXED, __HIP_MEMORY_SCOPE_AGENT);
  __hip_atomic_fetch_add(a + 192, mv * lo16(sv[s * 256 + 192 + c]) + mr * rb(z * inv), __ATOMIC_RELAXED, __HIP_MEMORY_SCOPE_AGENT);
}
extern "C" __global__ __launch_bounds__(256) void k_gen_out(const u16* __restrict__ sv, const float* __restrict__ acc, u16* __restrict__ out) {
  const size_t i = (size_t)BID * 256 + TID;
  out[i] = (u16)pk2(lo16(sv[i]) + acc[i], 0.f);
}

#if defined(__HIP_DEVICE_COMPILE__)
#pragma clang attribute pop
#endif
extern "C" void kernel_launch(void* const* d_in, const int* in_sizes, int n_in, void* d_out, int out_size, void* d_ws, size_t ws_size,
                              hipStream_t stream) {
  const u16* sv = (const u16*)d_in[0];
  const u16* pos = (const u16*)d_in[1];
  const int* src = (const int*)d_in[2];
  const int* dst = (const int*)d_in[3];
  const u16* fr = (const u16*)d_in[4];
  const u16* w1 = (const u16*)d_in[5];
  const u16* w2 = (const u16*)d_in[6];
  const u16* wr = (const u16*)d_in[7];
  const int N = in_sizes[1] / 3, E = in_sizes[2];
  char* ws = (char*)d_ws;
  if (N <= 0) return;
  if (N > 51200 || E > 256 * CH) {
    float* acc = (float*)ws;
    float* phi = acc + (size_t)N * 256;
    (void)hipMemsetAsync(acc, 0, (size_t)N * 1024, stream);
    k_gen_phi<<<N, 192, 0, stream>>>(sv, w1, w2, phi);
    if (E > 0) k_gen_edge<<<(E + 3) / 4, 256, 0, stream>>>(sv, pos, src, dst, fr, wr, phi, acc, E);
    k_gen_out<<<N, 256, 0, stream>>>(sv, acc, (u16*)d_out);
    return;
  }
  const int BN = (N + 255) / 256 < 2 ? 2 : (N + 255) / 256;
  const int B = (N + BN - 1) / BN, bpx = (B + 7) / 8;
  int chunk = (((E + 195) / 196) + 3) & ~3;
  chunk = chunk < 1024 ? 1024 : chunk > CH ? CH : chunk;
  const int W = (E + chunk - 1) / chunk;
  const int NPX = bpx * BN, HW = 200, WP = (W + 7) & ~7;
  const unsigned magicBN = (unsigned)(0x100000000ull / (unsigned)BN) + 1u;
  (void)hipFuncSetAttribute((const void*)k_agg, hipFuncAttributeMaxDynamicSharedMemorySize, L_END);
  k_bin<<<WP + HW + 8, 512, 0, stream>>>(src, dst, (unsigned*)(ws + WS_T), (u32x4*)(ws + WS_SEG), sv, pos, w1, w2, wr, fr, (u32x4*)ws,
                                        (u32x2*)(ws + WS_P), (u16*)(ws + WS_C), ws + WS_H, N, E, W, HW, magicBN, BN, WP, NPX, chunk);
  k_agg<<<8 * bpx, 512, L_END, stream>>>(sv, ws, (u16*)d_out, N, W, BN, B, bpx);
}
```

```cpp
#if defined(__HIP_DEVICE_COMPILE__)
#pragma clang attribute push(__attribute__((target("no-packed-fp32-ops"))), apply_to = function)
#endif
#include <hip/hip_runtime.h>
#include <stdint.h>
#include <type_traits>

#define TID __builtin_amdgcn_workitem_id_x()
#define BID __builtin_amdgcn_workgroup_id_x()
typedef unsigned short u16;
typedef __attribute__((ext_vector_type(8))) __bf16 bf16x8;
typedef __attribute__((ext_vector_type(2))) __bf16 bf16x2;
typedef __attribute__((ext_vector_type(4))) short s16x4;
typedef __attribute__((ext_vector_type(2))) float f32x2;
typedef __attribute__((ext_vector_type(4))) float f32x4;
typedef __attribute__((ext_vector_type(16))) float f32x16;
typedef __attribute__((ext_vector_type(4))) unsigned u32x4;
typedef __attribute__((ext_vector_type(3))) unsigned u32x3;
typedef __attribute__((ext_vector_type(2))) unsigned u32x2;
#define LDSP(T, off) ((__attribute__((address_space(3))) T*)(uintptr_t)(off))
#define DI __device__ __forceinline__
#define SB() __builtin_amdgcn_sched_barrier(0)

constexpr int WT = 17;
constexpr int CH = 4096;
constexpr int SEGQ = CH / 4 + 192;
constexpr int GN = 25;
constexpr int NFRAG = 30;
constexpr int SUBC = 32;
constexpr int CAP = 128;
constexpr int BQ = 256;
constexpr unsigned SENT = 0xFFFFFFFFu;
constexpr int L_DPOS = 0;
constexpr int L_RAW = 4096;
constexpr int L_CNT = L_RAW + 8192;
constexpr int L_NB = L_CNT + 256;
constexpr int L_LIST = L_NB + 64;
constexpr int O_D = 640, O_DL = O_D + 256, O_F = O_DL + 256, O_FX = O_F + 256;
constexpr int LISTS = O_FX + 768;
constexpr int L_W = L_LIST + 8 * LISTS;
constexpr int L_FR = L_W + NFRAG * 1024;
constexpr int L_TB = (L_FR + 64 + 511) / 512 * 512;
constexpr int TBS = GN * 512;
constexpr int L_END = L_TB + 8 * TBS;
static_assert(L_END <= 163840 && L_TB < 65536 && L_W % 16 == 0 && LISTS % 16 == 0 && SUBC == 32, "LDS");
constexpr size_t WS_T = 65536;
constexpr size_t WS_SEG = WS_T + 256 * 256 * 4;
constexpr size_t WS_P = WS_SEG + (size_t)256 * SEGQ * 16;
constexpr size_t WS_C = WS_P + 51200 * 8;
constexpr size_t WS_H = WS_C + 51200 * 2;
constexpr size_t WS_END = WS_H + 51200 * 128;
constexpr size_t WS_F = NFRAG * 1024;
static_assert(WS_END < 0xffffff, "ws");

DI float lo16(unsigned x) { return __uint_as_float(x << 16); }
DI float hi16(unsigned x) { return __uint_as_float(x & 0xffff0000u); }
DI unsigned pk2(float a, float b) { f32x2 v = {a, b}; return __builtin_bit_cast(unsigned, __builtin_convertvector(v, bf16x2)); }
DI float rb(float a) { return hi16(pk2(a, a)); }
DI f32x16 mfma(u32x4 a, u32x4 b, f32x16 c) {
  return __builtin_amdgcn_mfma_f32_32x32x16_bf16(__builtin_bit_cast(bf16x8, a), __builtin_bit_cast(bf16x8, b), c, 0, 0, 0);
}
DI f32x16 zero16() { f32x16 z;
#pragma unroll
  for (int r = 0; r < 16; ++r) z[r] = 0.f;
  return z; }
DI int wave_incl_scan(int v) {
  v += __builtin_amdgcn_update_dpp(0, v, 0x111, 0xf, 0xf, false);
  v += __builtin_amdgcn_update_dpp(0, v, 0x112, 0xf, 0xf, false);
  v += __builtin_amdgcn_update_dpp(0, v, 0x114, 0xf, 0xf, false);
  v += __builtin_amdgcn_update_dpp(0, v, 0x118, 0xf, 0xf, false);
  v += __builtin_amdgcn_update_dpp(0, v, 0x142, 0xa, 0xf, false);
  v += __builtin_amdgcn_update_dpp(0, v, 0x143, 0xc, 0xf, false);
  return v;
}
DI int chan_of(int t2, int i32) { return 32 * ((i32 >> 2) & 1) + 16 * t2 + 4 * (i32 >> 3) + (i32 & 3); }
DI u32x4 pack8(const f32x16& x, int s) {
  return u32x4{pk2(x[8 * s], x[8 * s + 1]), pk2(x[8 * s + 2], x[8 * s + 3]), pk2(x[8 * s + 4], x[8 * s + 5]), pk2(x[8 * s + 6], x[8 * s + 7])};
}
DI void setprio(int p) {
  if (p >= 3) __builtin_amdgcn_s_setprio(3);
  else if (p == 2) __builtin_amdgcn_s_setprio(2);
  else if (p == 1) __builtin_amdgcn_s_setprio(1);
  else __builtin_amdgcn_s_setprio(0);
}
DI u32x2 trread(unsigned a) { return __builtin_bit_cast(u32x2, __builtin_amdgcn_ds_read_tr16_b64_v4i16(LDSP(s16x4, a))); }
DI void st16(void* base, unsigned off, u32x4 v) {
  const __amdgpu_buffer_rsrc_t r = __builtin_amdgcn_make_buffer_rsrc(base, 0, 0x7fffffff, 0x00020000);
  __builtin_amdgcn_raw_buffer_store_b128(v, r, off, 0, WT);
}
DI unsigned qmod(float x) { return min((unsigned)(__builtin_amdgcn_fractf(x * 0.0625f) * 65536.0f), 65535u); }
DI unsigned opq(unsigned c) { asm volatile("" : "+v"(c)); return c; }
DI unsigned swap23(unsigned p) { return (p & ~12u) | ((p & 4u) << 1) | ((p & 8u) >> 1); }
DI void wg_barrier() { asm volatile("s_waitcnt lgkmcnt(0)\n\ts_barrier" ::: "memory"); }

DI void dma16(u32x4 rsrc, unsigned voff, unsigned ldsaddr) {
  asm volatile("s_mov_b32 m0, %2\n\ts_nop 3\n\tbuffer_load_dwordx4 %0, %1, 0 offen lds" ::"v"(voff), "s"(rsrc), "s"(ldsaddr) : "memory");
}
DI void dma16x4(u32x4 rsrc, const unsigned (&voff)[4], unsigned soff, unsigned m0) {
  asm volatile("s_mov_b32 m0, %6\n\ts_nop 3\n\tbuffer_load_dwordx4 %0, %4, %5 offen lds\n\t"
               "s_add_u32 m0, m0, 0x400\n\ts_nop 0\n\tbuffer_load_dwordx4 %1, %4, %5 offen lds\n\t"
               "s_add_u32 m0, m0, 0x400\n\ts_nop 0\n\tbuffer_load_dwordx4 %2, %4, %5 offen lds\n\t"
               "s_add_u32 m0, m0, 0x400\n\ts_nop 0\n\tbuffer_load_dwordx4 %3, %4, %5 offen lds"
               ::"v"(voff[0]), "v"(voff[1]), "v"(voff[2]), "v"(voff[3]), "s"(rsrc), "s"(soff), "s"(m0) : "memory", "scc");
}
constexpr int K1_CNT = 0, K1_STAGE = 8 * 256 * 4, K1_TOT = K1_STAGE + SEGQ * 16, K1_W1 = 0, K1_HT = 8192, K1_END = K1_HT + 8 * 4096;
#if defined(__HIP_DEVICE_COMPILE__)
#pragma clang attribute pop
#endif
extern "C" __global__ __launch_bounds__(512, 4) void k_bin(const int* __restrict__ src, const int* __restrict__ dst, unsigned* __restrict__ T,
                                                u32x4* __restrict__ seg, const u16* __restrict__ sv, const u16* __restrict__ pos,
                                                const u16* __restrict__ w1, const u16* __restrict__ w2, const u16* __restrict__ wr, const u16* __restrict__ freqs,
                                                u32x4* __restrict__ wf, u32x2* __restrict__ ptab, u16* __restrict__ ctab, char* __restrict__ htab, int N, int E,
                                                int W, int HW, unsigned magicBN, int BN, int WP, int NPX, int chunk) {
  __shared__ __attribute__((aligned(16))) char lds_raw[K1_END + 256];
  const unsigned L0 = (unsigned)(uintptr_t)(__attribute__((address_space(3))) char*)lds_raw;
  const int tid = TID, lane = tid & 63;
  const int wave = __builtin_amdgcn_readfirstlane(tid >> 6);
  int bid = BID;
  if (bid < WP + HW) { if (bid < WP) { if (bid >= W) return; bid += HW; } else bid -= WP; } else bid += W - WP;
  if (wave == 0) {
    uint64_t pc; unsigned len;
    asm volatile("s_getpc_b64 %0\npainn_self_lo:\n\ts_mov_b32 %1, painn_code_hi-painn_self_lo" : "=s"(pc), "=s"(len));
    const u32x4 rc = {(unsigned)pc, (unsigned)(pc >> 32) & 0xffffu, len, 0x00020000u};
    asm volatile("s_mov_b32 m0, %2\n\ts_nop 3\n\tbuffer_load_dword %0, %1, 0 offen lds" ::"v"(lane * 128), "s"(rc), "s"(L0 + K1_END) : "memory");
  }
  if (bid >= W + HW) {
    {
      uint64_t pc; unsigned len;
      asm volatile("s_getpc_b64 %0\npainn_code_lo:\n\ts_mov_b32 %1, painn_code_hi-painn_code_lo" : "=s"(pc), "=s"(len));
      const __amdgpu_buffer_rsrc_t rc = __builtin_amdgcn_make_buffer_rsrc((void*)pc, 0, len, 0x00020000);
      const unsigned x = __builtin_amdgcn_raw_buffer_load_b32(rc, tid * 64, 0, 0);
      asm volatile("" ::"v"(x));
    }
    const int idx = (bid - W - HW) * 512 + tid;
    if (idx >= NFRAG * 64) {
      if (idx < NFRAG * 64 + 16) ((unsigned*)wf)[WS_F / 4 + idx - NFRAG * 64] = (unsigned)freqs[idx - NFRAG * 64] << 16;
      return;
    }
    const int f = idx >> 6, i32 = lane & 31, h = lane >> 5;
    const u16* p; int s2;
    if (f < 24) { const int ct = f >> 2; p = w2 + (16 * (f & 3) + 4 * h) * 192 + 64 * (ct >> 1) + chan_of(ct & 1, i32); s2 = 8 * 192; }
    else { const int ct = f - 24; p = wr + 8 * h * 192 + 64 * (ct >> 1) + chan_of(ct & 1, i32); s2 = 4 * 192; }
    unsigned v[8];
#pragma unroll
    for (int j = 0; j < 8; ++j) v[j] = p[(j & 3) * 192 + (j >> 2) * s2];
    wf[idx] = u32x4{v[0] | v[1] << 16, v[2] | v[3] << 16, v[4] | v[5] << 16, v[6] | v[7] << 16};
    return;
  }
  if (bid < HW) {
    const int l31 = lane & 31, h = lane >> 5;
    const int nl = (wave * (HW >> 3) + (bid >> 3)) * 32, nend = min(N, ((bid & 7) + 1) * NPX);
    const int n0 = nl < NPX ? (bid & 7) * NPX + nl : N;
    const int nn = min(n0 + l31, N - 1);
    const unsigned HT = L0 + K1_HT + wave * 4096;
    {
      const uint64_t sp = (uint64_t)sv, wp = (uint64_t)w1;
      const u32x4 rs_sv = {(unsigned)sp, (unsigned)(sp >> 32), (unsigned)N * 512u, 0x00020000u};
      const u32x4 rs_w1 = {(unsigned)wp, (unsigned)(wp >> 32), 8192u, 0x00020000u};
      const unsigned o = (unsigned)(n0 + (lane >> 3)) * 512u + 16u * ((lane & 7) ^ (lane >> 3));
      const unsigned v4[4] = {o, o + 4096u, o + 8192u, o + 12288u};
      dma16x4(rs_sv, v4, 0, HT);
      dma16(rs_w1, tid * 16u, L0 + K1_W1 + wave * 1024);
    }
    unsigned px = pos[nn * 3], py = pos[nn * 3 + 1], pz = pos[nn * 3 + 2];
    asm volatile("s_waitcnt vmcnt(0)\n\ts_barrier" : "+v"(px), "+v"(py), "+v"(pz)::"memory");
    if (n0 >= N) return;
    {
      const uint64_t sp = (uint64_t)sv;
      const u32x4 rs_sv = {(unsigned)sp, (unsigned)(sp >> 32), (unsigned)N * 512u, 0x00020000u};
      asm volatile("s_mov_b32 m0, %2\n\ts_nop 3\n\tbuffer_load_dword %0, %1, 0 offen lds"
                   ::"v"(h == 0 ? (unsigned)(n0 + l31) * 512u + 128u : 0x7fffffffu), "s"(rs_sv), "s"(L0 + K1_END) : "memory");
    }
    u32x4 sf[4];
#pragma unroll
    for (int ks = 0; ks < 4; ++ks) sf[ks] = *LDSP(u32x4, HT + l31 * 128 + 16 * ((4 * h + ks) ^ (l31 & 7)));
    const unsigned ta = L0 + K1_W1 + (32 * h + ((lane >> 2) & 3)) * 128 + (16 * ((lane >> 4) & 1) + 4 * (lane & 3)) * 2;
    f32x16 d1[2];
#pragma unroll
    for (int ht = 0; ht < 2; ++ht) {
      d1[ht] = zero16();
#pragma unroll
      for (int ks = 0; ks < 4; ++ks) {
        const u32x2 a0 = trread(ta + ks * 1024 + ht * 64), a1 = trread(ta + ks * 1024 + 512 + ht * 64);
        d1[ht] = mfma(u32x4{a0[0], a0[1], a1[0], a1[1]}, sf[ks], d1[ht]);
      }
    }
#pragma unroll
    for (int ht = 0; ht < 2; ++ht) {
      f32x16 e = d1[ht] * -1.4426950408889634f;
#pragma unroll
      for (int r = 0; r < 16; ++r) e[r] = __builtin_amdgcn_exp2f(e[r]);
      e += 1.0f;
#pragma unroll
      for (int r = 0; r < 16; ++r) e[r] = __builtin_amdgcn_rcpf(e[r]);
      e *= d1[ht];
#pragma unroll
      for (int s = 0; s < 2; ++s) *LDSP(u32x4, HT + l31 * 128 + 16 * ((4 * h + 2 * ht + s) ^ (l31 & 7))) = pack8(e, s);
    }
#pragma unroll
    for (int i = 0; i < 4; ++i) {
      const int n = 8 * i + (lane >> 3);
      const u32x4 v = *LDSP(u32x4, HT + i * 1024 + lane * 16);
      if (n0 + n < nend) st16(htab, (n0 + n) * 128 + 16 * ((lane & 7) ^ (n & 7)), v);
    }
    if (h == 0 && n0 + l31 < nend) {
      ptab[n0 + l31] = u32x2{px | py << 16, pz << 16};
      ctab[n0 + l31] = (u16)(qmod(lo16(px)) >> 11 | (qmod(lo16(py)) >> 11) << 5 | (qmod(lo16(pz)) & 0xfc00u));
    }
    return;
  }
  const int cw = bid - HW;
  const int eend = min(E, (cw + 1) * chunk);
  const __amdgpu_buffer_rsrc_t rb_s = __builtin_amdgcn_make_buffer_rsrc((void*)src, 0, eend * 4, 0x00020000);
  const __amdgpu_buffer_rsrc_t rb_d = __builtin_amdgcn_make_buffer_rsrc((void*)dst, 0, eend * 4, 0x00020000);
  const int e0 = cw * chunk + tid * 4;
  u32x4 s4[2], d4[2];
#pragma unroll
  for (int u = 0; u < 2; ++u) d4[u] = __builtin_amdgcn_raw_buffer_load_b128(rb_d, (e0 + u * 2048) * 4, 0, 0);
#pragma unroll
  for (int u = 0; u < 2; ++u) s4[u] = __builtin_amdgcn_raw_buffer_load_b128(rb_s, (e0 + u * 2048) * 4, 0, 0);
  *LDSP(u32x4, L0 + K1_CNT + wave * 1024 + lane * 16) = u32x4{0, 0, 0, 0};
#pragma unroll
  for (int i = 0; i < 3; ++i) if (i * 512 + tid < SEGQ) *LDSP(u32x4, L0 + K1_STAGE + (i * 512 + tid) * 16) = u32x4{0, 0, 0, 0};
  unsigned ca[8], hi8[8], rank[8];
#pragma unroll
  for (int k = 0; k < 8; ++k) {
    const unsigned d = d4[k >> 2][k & 3];
    const unsigned b = (unsigned)(((uint64_t)d * magicBN) >> 32);
    ca[k] = L0 + K1_CNT + wave * 1024 + b * 4;
    rank[k] = SENT;
    if (e0 + (k >> 2) * 2048 + (k & 3) < eend) rank[k] = __hip_atomic_fetch_add(LDSP(unsigned, ca[k]), 1u, __ATOMIC_RELAXED, __HIP_MEMORY_SCOPE_WORKGROUP);
    const unsigned dl = d - b * (unsigned)BN;
    hi8[k] = dl + ((dl * 41u) >> 10) * 7u + 1u;
  }
  wg_barrier();
  if (wave == 0) {
    u32x4 c[8], tot = {0, 0, 0, 0};
#pragma unroll
    for (int w = 0; w < 8; ++w) { c[w] = *LDSP(u32x4, L0 + K1_CNT + w * 1024 + lane * 16); tot += c[w]; }
    const u32x4 q = (tot + 3u) >> 2;
    const unsigned ls = q[0] + q[1] + q[2] + q[3];
    const unsigned incl = wave_incl_scan(ls);
    u32x4 st; st[0] = incl - ls; st[1] = st[0] + q[0]; st[2] = st[1] + q[1]; st[3] = st[2] + q[2];
    u32x4 run = st * 4u;
#pragma unroll
    for (int w = 0; w < 8; ++w) { *LDSP(u32x4, L0 + K1_CNT + w * 1024 + lane * 16) = run; run += c[w]; }
    if (lane == 63) *LDSP(unsigned, L0 + K1_TOT) = incl;
    st16(T, cw * 1024 + 16 * lane, st | (q << 16));
  }
  wg_barrier();
  unsigned base[8];
#pragma unroll
  for (int k = 0; k < 8; ++k) base[k] = *LDSP(unsigned, ca[k]);
#pragma unroll
  for (int k = 0; k < 8; ++k)
    if (rank[k] != SENT) *LDSP(unsigned, L0 + K1_STAGE + (base[k] + rank[k]) * 4) = s4[k >> 2][k & 3] << 3 | hi8[k] << 24;
  const int total = *LDSP(int, L0 + K1_TOT);
  wg_barrier();
#pragma unroll
  for (int i = 0; i < 3; ++i) {
    const int qd = i * 512 + tid;
    if (qd < total) st16(seg, (cw * SEGQ + qd) * 16, *LDSP(u32x4, L0 + K1_STAGE + qd * 16));
  }
}

#if defined(__HIP_DEVICE_COMPILE__)
#pragma clang attribute push(__attribute__((target("no-packed-fp32-ops"))), apply_to = function)
#endif
DI u32x4 eqmask4(u32x4 a, unsigned b, unsigned ones) {
  u32x4 x;
#pragma unroll
  for (int i = 0; i < 4; ++i) x[i] = a[i] ^ b;
  asm("v_pk_min_u16 %0, %0, %4\n\tv_pk_min_u16 %1, %1, %4\n\tv_pk_min_u16 %2, %2, %4\n\tv_pk_min_u16 %3, %3, %4\n\t"
      "v_pk_sub_u16 %0, %0, %4\n\tv_pk_sub_u16 %1, %1, %4\n\tv_pk_sub_u16 %2, %2, %4\n\tv_pk_sub_u16 %3, %3, %4"
      : "+v"(x[0]), "+v"(x[1]), "+v"(x[2]), "+v"(x[3]) : "s"(ones));
  return x;
}

typedef __attribute__((ext_vector_type(2))) short s16x2;
extern "C" __global__ __launch_bounds__(512, 2) void k_agg(const u16* __restrict__ sv, const char* __restrict__ wf, u16* __restrict__ out, int N,
                                                           int W, int BN, int B, int bpx) {
  const int wave = __builtin_amdgcn_readfirstlane(TID >> 6);
  const int b = ((int)BID & 7) * bpx + ((int)BID >> 3);
  if (b >= B) return;
  constexpr unsigned L0 = 0;
  const int node0 = b * BN;
  const int nb = min(BN, N - node0);
  const uint64_t svp = (uint64_t)sv, wfp = (uint64_t)wf;
  const u32x4 rs_sv = {(unsigned)svp, (unsigned)(svp >> 32), (unsigned)N * 512u, 0x00020000u};
  const u32x4 rs_ws = {(unsigned)wfp, (unsigned)(wfp >> 32), (unsigned)WS_END, 0x00020000u};
#define FRESH_LANE int lane; asm volatile("v_mbcnt_lo_u32_b32 %0, -1, 0\n\tv_mbcnt_hi_u32_b32 %0, -1, %0" : "=v"(lane)); const int l31 = lane & 31, h = lane >> 5; (void)l31; (void)h
  const unsigned TB = L0 + L_TB + wave * TBS;

  f32x16 acc[4][2];
  u32x4 hN[4];
  int batch = 0, r1 = 0;
  bool first = true;
  const int grp = wave;
  while (true) {
    u32x4 ol[13];
    {
      FRESH_LANE;
      const unsigned lo = 16u * lane;
      const unsigned v4[4] = {lo, lo + 1024u, lo + 2048u, lo + 3072u};
      const unsigned oo = first ? (unsigned)(node0 + wave * GN + h) * 512u + 16u * (l31 ^ h) : 0x7fff0000u;
#define OWNA(i) ((oo ^ (32u * ((i) & 7))) + 1024u * (i))
#define OWN4(j) asm volatile("s_nop 4\n\tbuffer_load_dwordx4 %0, %4, %8, 0 offen\n\tbuffer_load_dwordx4 %1, %5, %8, 0 offen\n\t" \
                             "buffer_load_dwordx4 %2, %6, %8, 0 offen\n\tbuffer_load_dwordx4 %3, %7, %8, 0 offen" \
                             : "=&v"(ol[4 * j]), "=&v"(ol[4 * j + 1]), "=&v"(ol[4 * j + 2]), "=&v"(ol[4 * j + 3]) \
                             : "v"(OWNA(4 * j)), "v"(OWNA(4 * j + 1)), "v"(OWNA(4 * j + 2)), "v"(OWNA(4 * j + 3)), "s"(rs_sv) : "memory")
#define OWN1() asm volatile("s_nop 4\n\tbuffer_load_dwordx4 %0, %1, %2, 0 offen" : "=&v"(ol[12]) : "v"(h ? 0x7fff0000u : OWNA(12)), "s"(rs_sv) : "memory")
      if (wave >= 4) {
        const int rw = wave - 4;
        const unsigned MAP = L_LIST + wave * LISTS;
        const int cpw = (W + 3) >> 2;
        const int cell = rw * cpw + lane;
        unsigned t;
        u32x2 dp;
        asm volatile("s_nop 4\n\tbuffer_load_dword %0, %2, %4, 0 offen\n\tbuffer_load_dwordx2 %1, %3, %4, 0 offen\n\ts_barrier\n\ts_waitcnt vmcnt(0)"
                     : "=&v"(t), "=&v"(dp) : "v"((lane < cpw && cell < W) ? (int)WS_T + (cell * 256 + b) * 4 : 0x7fffffff),
                       "v"((int)WS_P + (node0 + (2 * rw + h) * GN + l31 - 1) * 8), "s"(rs_ws) : "memory");
        const int q = t >> 16;
        const int incl = wave_incl_scan(q), excl = incl - q;
        const int total = __builtin_amdgcn_readlane(incl, 63);
        const int win0 = batch * BQ;
        const int gq = (int)(WS_SEG / 16) + cell * SEGQ + (int)(t & 0xffffu) - excl;
        const int hi = min(incl, win0 + BQ);
#pragma nounroll
        for (int i = max(excl, win0); i < hi; ++i) *LDSP(int, MAP + (i - win0) * 4) = (gq + i) * 16;
        unsigned mq[4];
#pragma unroll
        for (int s = 0; s < 4; ++s) mq[s] = *LDSP(int, MAP + (64 * s + lane) * 4);
#pragma unroll
        for (int s = 0; s < 4; ++s) if (win0 + 64 * s + lane >= total) mq[s] = 0x7fffffffu;
        dma16x4(rs_ws, mq, 0, L_W + rw * 4096);
        if (lane == 0) *LDSP(int, L_NB + rw * 4) = (total + BQ - 1) / BQ;
        const unsigned qx = (qmod(lo16(dp[0])) - 1024u) & 0xffffu, qy = qmod(hi16(dp[0])) - 1024u, qz = (qmod(__uint_as_float(dp[1])) - 512u) & 0xffffu;
        *LDSP(u32x4, L_DPOS + rw * 1024 + lo) = (unsigned)(l31 - 1) < (unsigned)GN ? u32x4{dp[0], dp[1], qx | qy << 16, qz} : u32x4{0x7fc07fc0u, 0x7fc00000u, 0u, 0x7fff0000u};
      } else {
        __builtin_amdgcn_s_barrier();
#pragma unroll
        for (int i = 0; i < 6; ++i) dma16x4(rs_ws, v4, WS_C + (wave * 6 + i) * 4096, L_TB + (wave * 6 + i) * 4096);
        dma16(rs_ws, WS_C + (96 + wave) * 1024u + lo, L_TB + (96 + wave) * 1024);
      }
      if (lane < 8) *LDSP(unsigned, L_CNT + lane * 32 + wave * 4) = lane >> 3;
      { const unsigned z = opq(0u); *LDSP(u32x4, L_RAW + wave * 1024 + lo) = u32x4{z, z, z, z}; }
      OWN4(0); OWN1();
      asm volatile("s_waitcnt vmcnt(5)" ::: "memory");
      wg_barrier();
      u32x4 r4[2];
#pragma unroll
      for (int s = 0; s < 2; ++s) r4[s] = *LDSP(u32x4, L_W + (2 * s + h) * 4096 + ((wave + 2 * (2 * s + h)) & 7) * 512 + l31 * 16);
      OWN4(1);
      const unsigned one = opq(1u);
#pragma unroll
      for (int s = 0; s < 2; ++s) {
        unsigned cd[4];
        u32x2 dq[4];
#pragma unroll
        for (int c = 0; c < 4; ++c) {
          cd[c] = *LDSP(u16, L_TB + ((r4[s][c] >> 2) & 0x3fffffu));
          dq[c] = *LDSP(u32x2, L_DPOS + 8 + ((r4[s][c] >> 20) & 0xff0u));
        }
        unsigned rank[4];
#pragma unroll
        for (int c = 0; c < 4; ++c) {
          const unsigned cxy = ((cd[c] & 0x3ffu) * 0x400800u) & 0xf800f800u, cz = cd[c] & 0xfc00u;
          const unsigned qxy = dq[c][0], qz = dq[c][1];
          const s16x2 dxy = __builtin_bit_cast(s16x2, qxy) - __builtin_bit_cast(s16x2, cxy);
          const s16x2 dz = __builtin_bit_cast(s16x2, qz) - __builtin_bit_cast(s16x2, cz);
          const unsigned d2 = __builtin_amdgcn_sdot2(dxy, dxy, __builtin_amdgcn_sdot2(dz, dz, 0, false), false);
          rank[c] = 0x7fffffffu;
          if (d2 < 488400000u)
            rank[c] = __hip_atomic_fetch_add(LDSP(unsigned, L_CNT + (((r4[s][c] >> 24) & 0xe0u) | wave * 4)), one, __ATOMIC_RELAXED, __HIP_MEMORY_SCOPE_WORKGROUP);
        }
        if (s == 0) OWN4(2);
#pragma unroll
        for (int c = 0; c < 4; ++c) {
          const unsigned p = rank[c] - SUBC * r1;
          if (p < (unsigned)SUBC) *LDSP(unsigned, L_RAW + (((r4[s][c] >> 19) & 0x1c00u) | wave * 4) + p * 32) = r4[s][c];
        }
      }
    }
    wg_barrier();
    bool more1, moreb;
    for (int r2 = 0;; ++r2) {
    FRESH_LANE;
    const unsigned LIST = L0 + L_LIST + grp * LISTS;
    int nact = 0, nstep;
    bool lighter;
    unsigned exy[4], ezd[4], ep[4];
    float ef[4];
    u32x4 own[16];
    {
      unsigned rec[4];
      u32x2 ps[4], pd[4];
#pragma unroll
      for (int i = 0; i < 4; ++i) rec[i] = *LDSP(unsigned, L_RAW + grp * 1024 + lane * 4 + i * 256);
      {
        asm volatile("s_nop 4\n\tbuffer_load_dwordx2 %0, %4, %8, %9 offen\n\tbuffer_load_dwordx2 %1, %5, %8, %9 offen\n\t"
                     "buffer_load_dwordx2 %2, %6, %8, %9 offen\n\tbuffer_load_dwordx2 %3, %7, %8, %9 offen"
                     : "=&v"(ps[0]), "=&v"(ps[1]), "=&v"(ps[2]), "=&v"(ps[3])
                     : "v"(rec[0] & 0xffffffu), "v"(rec[1] & 0xffffffu), "v"(rec[2] & 0xffffffu), "v"(rec[3] & 0xffffffu), "s"(rs_ws), "s"((unsigned)WS_P) : "memory");
      }
#pragma unroll
      for (int i = 0; i < 4; ++i) pd[i] = *LDSP(u32x2, L0 + L_DPOS + ((rec[i] >> 20) & 0xff0u));
      nstep = __builtin_amdgcn_ballot_w64(rec[3] != 0) ? 4 : __builtin_amdgcn_ballot_w64(rec[2] != 0) ? 3 : 2;
      {
        const u32x4 c0 = *LDSP(u32x4, L_CNT + (lane & 7) * 32), c1 = *LDSP(u32x4, L_CNT + (lane & 7) * 32 + 16);
        const int nbv = *LDSP(int, L_NB + (lane & 3) * 4);
        const unsigned mx = max(max(max(c0[0], c0[1]), max(c0[2], c0[3])), max(max(c1[0], c1[1]), max(c1[2], c1[3])));
        more1 = __builtin_amdgcn_ballot_w64(mx > (unsigned)(SUBC * (r1 + 1))) != 0;
        moreb = __builtin_amdgcn_ballot_w64(nbv > batch + 1) != 0;
        const int key = (c0[0] + c0[1] + c0[2] + c0[3] + c1[0] + c1[1] + c1[2] + c1[3]) * 2 + (((lane & 7) >> 2) ^ 1);
        lighter = __builtin_amdgcn_readlane(key, grp) < __builtin_amdgcn_readlane(key, grp ^ 4);
      }
#pragma unroll
      for (int i = 0; i < 2; ++i) *LDSP(unsigned, LIST + (64 * i + lane) * 4) = opq(0xffffffu);
      if (lane < 32) *LDSP(unsigned, LIST + (128 + lane) * 4) = opq(0xffffffu);
      *LDSP(unsigned, LIST + O_D + lane * 4) = 0x3f803f80u;
      *LDSP(unsigned, LIST + O_DL + lane * 4) = 0xffffffffu;
      asm volatile("s_waitcnt vmcnt(4)" : "+v"(ol[0]), "+v"(ol[1]), "+v"(ol[2]), "+v"(ol[3]), "+v"(ol[4]),
                   "+v"(ol[5]), "+v"(ol[6]), "+v"(ol[7]), "+v"(ol[8]), "+v"(ol[9]), "+v"(ol[10]), "+v"(ol[11]), "+v"(ol[12])::"memory");
#pragma unroll
      for (int i = 0; i < 12; ++i) *LDSP(u32x4, TB + i * 1024 + lane * 16) = ol[i];
      if (h == 0) *LDSP(u32x4, TB + 12 * 1024 + lane * 16) = ol[12];
      {
        const unsigned ra = TB + l31 * 512 + 16 * ((4 * h) ^ (l31 & 15));
#pragma unroll
        for (int i = 0; i < 16; ++i) own[i] = *LDSP(u32x4, (ra ^ (16 * (8 * ((i >> 2) & 1) + (i & 3)))) + 256 * (i >> 3));
      }
      asm volatile("s_waitcnt vmcnt(0)" : "+v"(ps[0]), "+v"(ps[1]), "+v"(ps[2]), "+v"(ps[3])::"memory");
#pragma unroll
      for (int i = 0; i < 4; ++i) {
        exy[i] = 0; ezd[i] = 0; ef[i] = 0.f; ep[i] = SENT;
        if (i >= nstep) continue;
        const unsigned xy = pk2(lo16(ps[i][0]) - lo16(pd[i][0]), hi16(ps[i][0]) - hi16(pd[i][0]));
        const float z = rb(__uint_as_float(ps[i][1]) - __uint_as_float(pd[i][1]));
        const float x = lo16(xy), y = hi16(xy);
        const unsigned sq = pk2(x * x, y * y);
        const float q2 = rb(rb(lo16(sq) + hi16(sq) + rb(z * z)) + 1e-8f);
        const unsigned zd = pk2(z, __builtin_amdgcn_sqrtf(q2));
        const float d = hi16(zd);
        const float f = 0.5f * rb(rb(__builtin_amdgcn_cosf(rb(rb(3.141592653589793f * d) * 0.2f) * 0.15915494309189535f)) + 1.0f);
        const bool ok = (d < 5.0f) & (f != 0.0f);
        const uint64_t bm = __builtin_amdgcn_ballot_w64(ok);
        const unsigned p = __builtin_amdgcn_mbcnt_hi((unsigned)(bm >> 32), __builtin_amdgcn_mbcnt_lo((unsigned)bm, nact - CAP * r2));
        nact += __builtin_popcountll(bm);
        exy[i] = xy; ezd[i] = zd; ef[i] = f;
        if (ok & (p < (unsigned)CAP)) {
          ep[i] = p;
          *LDSP(unsigned, LIST + swap23(p) * 4) = rec[i] & 0xffffffu;
          *LDSP(u16, LIST + O_D + swap23(p) * 2) = (u16)(zd >> 16);
          *LDSP(u16, LIST + O_DL + p * 2) = (u16)(rec[i] >> 24);
        }
      }
    }
    const int cnt = min(nact - CAP * r2, CAP);
    const int ntile = (cnt + 31) >> 5;
    auto issue_h2 = [&](unsigned off) {
      asm volatile("s_nop 4\n\tbuffer_load_dwordx4 %0, %4, %5, %6 offen\n\tbuffer_load_dwordx4 %1, %4, %5, %6 offen offset:16\n\t"
                   "buffer_load_dwordx4 %2, %4, %5, %6 offen offset:32\n\tbuffer_load_dwordx4 %3, %4, %5, %6 offen offset:48"
                   : "=&v"(hN[0]), "=&v"(hN[1]), "=&v"(hN[2]), "=&v"(hN[3]) : "v"(off), "s"(rs_ws), "s"((unsigned)WS_H) : "memory");
    };
    auto issue_h = [&](int t) { FRESH_LANE; issue_h2(*LDSP(unsigned, LIST + (32 * t + l31) * 4) * 16u + 64u * h); };
    auto wait_h = [&]() {
      asm volatile("s_waitcnt vmcnt(12)" : "+v"(hN[0]), "+v"(hN[1]), "+v"(hN[2]), "+v"(hN[3])::"memory");
    };
    unsigned vo[4];
    auto prep_v = [&](int t) {
      FRESH_LANE;
      const int drow = lane >> 3;
      const unsigned dchunk = 16u * (((lane & 7) ^ (drow & 2)) + 8);
#pragma unroll
      for (int i = 0; i < 4; ++i) vo[i] = *LDSP(unsigned, LIST + (32 * t + 8 * i + drow) * 4) * 64u + dchunk;
    };
    auto issue_v = [&](int k) {
      asm volatile("s_waitcnt lgkmcnt(0)" ::: "memory");
      dma16x4(rs_sv, vo, 128 * k, TB + 4096 * k);
    };
    u32x4 rbf;
    auto calc_rbf = [&](int t) {
      FRESH_LANE;
      const float d = lo16(*LDSP(u16, LIST + O_D + 64 * t + 2 * l31));
      const f32x4 f0 = *LDSP(f32x4, L_FR + 32 * h), f1 = *LDSP(f32x4, L_FR + 32 * h + 16);
      const float inv = __builtin_amdgcn_rcpf(d);
#pragma unroll
      for (int i = 0; i < 4; ++i) {
        const float fa = i < 2 ? f0[2 * i] : f1[2 * i - 4], fb = i < 2 ? f0[2 * i + 1] : f1[2 * i - 3];
        const unsigned x = pk2(d * fa, d * fb);
        float ra = lo16(x) * 0.15915494309189535f, rc = hi16(x) * 0.15915494309189535f;
        ra -= __builtin_rintf(ra); rc -= __builtin_rintf(rc);
        rbf[i] = pk2(__builtin_amdgcn_sinf(ra) * inv, __builtin_amdgcn_sinf(rc) * inv);
      }
      asm volatile("" : "+v"(rbf));
    };
    setprio(3);
    issue_h(0);
    prep_v(0);
    if (r2 == 0) {
      const unsigned lo = 16u * lane;
      const unsigned v4[4] = {lo, lo + 1024u, lo + 2048u, lo + 3072u};
      const int wo = min(wave * 4096, NFRAG * 1024 + 64 - 4096);
      dma16x4(rs_ws, v4, wo, L_W + wo);
      __builtin_amdgcn_s_barrier();
    }
    const bool late = r2 == 0 && lighter;
    if (!late) {
      issue_v(0); issue_v(1); issue_v(2);
      if (r2 == 0) asm volatile("s_waitcnt vmcnt(12)\n\ts_barrier" ::: "memory");
    }
    setprio(min(ntile - 1, 2));
    SB();
#pragma unroll
    for (int i = 0; i < 4; ++i) {
      if (i >= nstep) break;
      const float inv = __builtin_amdgcn_rcpf(hi16(ezd[i])), f = ef[i];
      const unsigned hxy = pk2(lo16(exy[i]) * inv, hi16(exy[i]) * inv);
      const float hz = rb(lo16(ezd[i]) * inv);
      const unsigned fxy = pk2(f * lo16(hxy), f * hi16(hxy));
      const unsigned fzf = pk2(f * hz, f);
      if (ep[i] != SENT) {
        const unsigned a2 = LIST + ep[i] * 2;
        *LDSP(u16, a2 + O_F) = (u16)(fzf >> 16);
        *LDSP(u16, a2 + O_FX) = (u16)fxy;
        *LDSP(u16, a2 + O_FX + 256) = (u16)(fxy >> 16);
        *LDSP(u16, a2 + O_FX + 512) = (u16)fzf;
      }
    }
    if (first) {
#pragma unroll
      for (int i = 0; i < 16; ++i)
#pragma unroll
        for (int k = 0; k < 4; ++k) {
          acc[i >> 2][(i >> 1) & 1][8 * (i & 1) + 2 * k] = lo16(own[i][k]);
          acc[i >> 2][(i >> 1) & 1][8 * (i & 1) + 2 * k + 1] = hi16(own[i][k]);
        }
      first = false;
    }
    SB();
    if (late) {
      asm volatile("s_waitcnt vmcnt(0)\n\ts_barrier" ::: "memory");
      issue_v(0); issue_v(1); issue_v(2);
    }
    calc_rbf(0);
    wait_h();
    auto tile = [&](int t, auto nsc) {
      constexpr int NS = decltype(nsc)::value;
      FRESH_LANE;
      const unsigned WL = L0 + L_W + lane * 16, la = LIST + 64 * t + 16 * h;
      const unsigned mypair = (unsigned)(grp * 32 + l31 + 1) * 0x10001u;
      u32x4 Wb[5];
      auto ldW = [&](int ct) {
        Wb[4] = *LDSP(u32x4, WL + (24 + ct) * 1024);
#pragma unroll
        for (int ks = 0; ks < 4; ++ks) Wb[ks] = *LDSP(u32x4, WL + (4 * ct + ks) * 1024);
      };
      ldW(4);
      u32x4 msk[2], S1[2], sr[3][2];
#pragma unroll
      for (int s = 0; s < NS; ++s) msk[s] = *LDSP(u32x4, la + O_DL + 32 * s);
#pragma unroll
      for (int s = 0; s < NS; ++s)
#pragma unroll
        for (int k = 0; k < 3; ++k) sr[k][s] = *LDSP(u32x4, la + O_FX + 256 * k + 32 * s);
      SB();
      f32x16 ph, w;
      auto gemm = [&]() {
        w = mfma(rbf, Wb[4], zero16());
        ph = mfma(hN[0], Wb[0], zero16());
#pragma unroll
        for (int ks = 1; ks < 4; ++ks) ph = mfma(hN[ks], Wb[ks], ph);
      };
#define PIN1(a) asm volatile("" : "+v"(a))
#define PIN2(a, b) do { PIN1(a); PIN1(b); } while (0)
#define PIN3(a, b, c) asm volatile("" : "+v"(a), "+v"(b), "+v"(c))
      auto pack = [&](u32x4 (&a)[2]) {
        const f32x16 m = ph * w;
#pragma unroll
        for (int s = 0; s < NS; ++s) { a[s] = pack8(m, s); PIN1(a[s]); }
      };
#define MIX(id, lead, n, v) do { __builtin_amdgcn_sched_group_barrier(0x8, lead, id); _Pragma("unroll") for (int i_ = 0; i_ < n; ++i_) { \
        __builtin_amdgcn_sched_group_barrier(0x402, v, id); __builtin_amdgcn_sched_group_barrier(0x8, 1, id); } } while (0)
      u32x4 aa[2], ab[2];
      gemm();
#pragma unroll
      for (int s = 0; s < NS; ++s) {
        msk[s] = eqmask4(msk[s], mypair, 0x00010001u);
#pragma unroll
        for (int k = 0; k < 3; ++k) sr[k][s] &= msk[s];
      }
      MIX(0, 1, 4, 4 * NS);
      PIN2(ph, w);
      SB();
      ldW(5);
      SB();
      pack(aa);
      SB();
      gemm();
      PIN2(ph, w);
      SB();
      ldW(0);
      PIN2(aa[0], aa[NS - 1]);
      SB();
#pragma unroll
      for (int s = 0; s < NS; ++s)
#pragma unroll
        for (int k = 0; k < 3; ++k) acc[1 + k][0] = mfma(aa[s], sr[k][s], acc[1 + k][0]);
      pack(ab);
      MIX(1, 2, 3 * NS - 2, 6);
      PIN3(acc[1][0], acc[2][0], acc[3][0]);
      SB();
      gemm();
      PIN2(ph, w);
      SB();
      ldW(1);
      PIN2(ab[0], ab[NS - 1]);
      SB();
#pragma unroll
      for (int s = 0; s < NS; ++s)
#pragma unroll
        for (int k = 0; k < 3; ++k) acc[1 + k][1] = mfma(ab[s], sr[k][s], acc[1 + k][1]);
      pack(aa);
      MIX(2, 2, 3 * NS - 2, 6);
      PIN3(acc[1][1], acc[2][1], acc[3][1]);
      SB();
      unsigned tra, se, dn;
      f32x4 f0, f1;
      u32x4 rbn;
      {
        FRESH_LANE;
        const unsigned la2 = LIST + 64 * t + 16 * h, mp2 = (unsigned)(grp * 32 + l31 + 1) * 0x10001u;
#pragma unroll
        for (int s = 0; s < NS; ++s) { msk[s] = *LDSP(u32x4, la2 + O_DL + 32 * s); S1[s] = *LDSP(u32x4, la2 + O_F + 32 * s); }
        dn = *LDSP(u16, LIST + O_D + 64 * (t + 1) + 2 * l31);
        f0 = *LDSP(f32x4, L_FR + 32 * h); f1 = *LDSP(f32x4, L_FR + 32 * h + 16);
        se = *LDSP(unsigned, LIST + (32 * (t + 1) + l31) * 4);
#pragma unroll
        for (int i = 0; i < 4; ++i) vo[i] = *LDSP(unsigned, LIST + (32 * (t + 1) + 8 * i + (lane >> 3)) * 4);
        SB();
        gemm();
#pragma unroll
        for (int s = 0; s < NS; ++s) { S1[s] &= eqmask4(msk[s], mp2, 0x00010001u); PIN1(S1[s]); }
        const unsigned dchunk = 16u * (((lane & 7) ^ ((lane >> 3) & 2)) + 8);
#pragma unroll
        for (int i = 0; i < 4; ++i) { vo[i] = vo[i] * 64u + dchunk; PIN1(vo[i]); }
        se = se * 16u + 64u * h; PIN1(se);
        const int q4 = (lane >> 2) & 3, pp = lane & 3;
        tra = (q4 & 1) ? TB + (4 * h + (q4 >> 1)) * 128 + 64 * (pp & 1) + (lane & 16) + 8 * (pp >> 1) : 0x100000u;
        PIN1(tra);
        MIX(3, 2, 3, 8);
        PIN2(ph, w);
        SB();
      }
      ldW(2);
      PIN2(aa[0], aa[NS - 1]);
      SB();
#pragma unroll
      for (int s = 0; s < NS; ++s) acc[0][0] = mfma(aa[s], S1[s], acc[0][0]);
      PIN3(acc[0][0], ph, w);
      SB();
      pack(ab);
      SB();
      const float dnf = lo16(dn), inv = __builtin_amdgcn_rcpf(dnf);
      auto rbf2 = [&](float fa, float fb) {
        const unsigned x = pk2(dnf * fa, dnf * fb);
        float ra = lo16(x) * 0.15915494309189535f, rc = hi16(x) * 0.15915494309189535f;
        ra -= __builtin_rintf(ra); rc -= __builtin_rintf(rc);
        return pk2(__builtin_amdgcn_sinf(ra) * inv, __builtin_amdgcn_sinf(rc) * inv);
      };
      gemm();
      { unsigned r0 = rbf2(f0[0], f0[1]), r1_ = rbf2(f0[2], f0[3]); PIN2(r0, r1_); rbn[0] = r0; rbn[1] = r1_; }
      MIX(4, 1, 4, 6);
      PIN2(ph, w);
      SB();
      ldW(3);
      PIN2(ab[0], ab[NS - 1]);
      SB();
#pragma unroll
      for (int s = 0; s < NS; ++s) acc[0][1] = mfma(ab[s], S1[s], acc[0][1]);
      PIN3(acc[0][1], ph, w);
      SB();
      f32x16 mv[2];
      mv[0] = ph * w;
      PIN1(mv[0]);
      SB();
      gemm();
      { unsigned r0 = rbf2(f1[0], f1[1]), r1_ = rbf2(f1[2], f1[3]); PIN2(r0, r1_); rbn[2] = r0; rbn[3] = r1_; }
      MIX(5, 1, 4, 6);
      PIN2(ph, w);
      SB();
      issue_h2(se);
      rbf = rbn;
      mv[1] = ph * w;
      PIN1(mv[1]);
      SB();
      __builtin_amdgcn_s_setprio(3);
      asm volatile("s_waitcnt vmcnt(4)" ::: "memory");
      u32x2 va[8], vb[8];
      auto ldv = [&](u32x2 (&v)[8], int t2, int k) {
#pragma unroll
        for (int i = 0; i < 4 * NS; ++i)
          v[i] = trread(tra + k * 4096 + (8 * (i >> 1) + 2 * (i & 1)) * 128 + 32 * (t2 ^ (i & 1)));
      };
      auto vv = [&](const u32x2 (&v)[8], const f32x16& m, f32x16& ac) {
        u32x4 a[2];
#pragma unroll
        for (int i = 0; i < 4 * NS; ++i) {
          const unsigned v0 = v[i][0], v1 = v[i][1];
          a[i >> 2][i & 3] = pk2(m[2 * i] * __uint_as_float(v0), m[2 * i + 1] * __uint_as_float(v1));
        }
#pragma unroll
        for (int s = 0; s < NS; ++s) ac = mfma(a[s], S1[s], ac);
      };
      ldv(va, 0, 0); ldv(vb, 1, 0);
      SB();
      vv(va, mv[0], acc[1][0]);
      SB();
      issue_v(0);
      ldv(va, 0, 1);
      SB();
      vv(vb, mv[1], acc[1][1]);
      SB();
      ldv(vb, 1, 1);
      SB();
      vv(va, mv[0], acc[2][0]);
      SB();
      issue_v(1);
      ldv(va, 0, 2);
      SB();
      vv(vb, mv[1], acc[2][1]);
      SB();
      ldv(vb, 1, 2);
      SB();
      vv(va, mv[0], acc[3][0]);
      SB();
      issue_v(2);
      vv(vb, mv[1], acc[3][1]);
      SB();
      wait_h();
    };
    const int nfull = (cnt + 15) >> 5;
    for (int t = 0; t < nfull; ++t) { tile(t, std::integral_constant<int, 2>{}); setprio(min(ntile - 2 - t, 2)); }
    if (nfull < ntile) tile(nfull, std::integral_constant<int, 1>{});
    if (nact <= CAP * (r2 + 1)) break;
    asm volatile("s_waitcnt vmcnt(0)" ::: "memory");
#pragma unroll
    for (int i = 0; i < 13; ++i) asm volatile("" : "=v"(ol[i]));
    }
    if (more1) ++r1;
    else if (moreb) { ++batch; r1 = 0; }
    else break;
    asm volatile("s_waitcnt vmcnt(0)" ::: "memory");
    wg_barrier();
  }
  {
    FRESH_LANE;
    u32x4 o[16];
#pragma unroll
    for (int i = 0; i < 16; ++i) o[i] = pack8(acc[i >> 2][(i >> 1) & 1], i & 1);
    asm volatile("s_waitcnt vmcnt(0)" ::: "memory");
    if (l31 < GN) {
      const unsigned wa = TB + l31 * 512 + 16 * ((4 * h) ^ (l31 & 7));
#pragma unroll
      for (int i = 0; i < 16; ++i) *LDSP(u32x4, (wa ^ (16 * (i & 3))) + 128 * (i >> 2)) = o[i];
    }
    const int gw = __builtin_amdgcn_readfirstlane(max(min(nb - grp * GN, GN), 0));
    const uint64_t op = (uint64_t)out + (size_t)(node0 + grp * GN) * 512;
    const __amdgpu_buffer_rsrc_t rb_o = __builtin_amdgcn_make_buffer_rsrc((void*)op, 0, gw * 512, 0x00020000);
    u32x4 v[(GN + 1) / 2];
#pragma unroll
    for (int i = 0; i < (GN + 1) / 2; ++i) v[i] = *LDSP(u32x4, TB + i * 1024 + lane * 16);
    const unsigned oa = h * 512 + 16 * (l31 ^ h);
#pragma unroll
    for (int i = 0; i < (GN + 1) / 2; ++i) __builtin_amdgcn_raw_buffer_store_b128(v[i], rb_o, oa ^ (32 * (i & 3)), i * 1024, WT);
  }
  asm volatile("painn_code_hi:");
}

extern "C" __global__ __launch_bounds__(192) void k_gen_phi(const u16* __restrict__ sv, const u16* __restrict__ w1, const u16* __restrict__ w2,
                                                            float* __restrict__ phi) {
  __shared__ float s[64], hd[64];
  const int t = TID; const size_t n = BID;
  if (t < 64) s[t] = lo16(sv[n * 256 + t]);
  wg_barrier();
  if (t < 64) {
    float a = 0.f;
    for (int k = 0; k < 64; ++k) a += s[k] * lo16(w1[k * 64 + t]);
    hd[t] = rb(a * __builtin_amdgcn_rcpf(1.0f + __builtin_amdgcn_exp2f(a * -1.4426950408889634f)));
  }
  wg_barrier();
  float a = 0.f;
  for (int k = 0; k < 64; ++k) a += hd[k] * lo16(w2[k * 192 + t]);
  phi[n * 192 + t] = a;
}
extern "C" __global__ __launch_bounds__(256) void k_gen_edge(const u16* __restrict__ sv, const u16* __restrict__ pos, const int* __restrict__ src,
                                                             const int* __restrict__ dst, const u16* __restrict__ freqs, const u16* __restrict__ wr,
                                                             const float* __restrict__ phi, float* __restrict__ acc, int E) {
  const size_t e = (size_t)BID * 4 + (TID >> 6);
  const int c = TID & 63;
  if (e >= (size_t)E) return;
  const size_t s = src[e], t = dst[e];
  const float x = rb(lo16(pos[s * 3]) - lo16(pos[t * 3])), y = rb(lo16(pos[s * 3 + 1]) - lo16(pos[t * 3 + 1])), z = rb(lo16(pos[s * 3 + 2]) - lo16(pos[t * 3 + 2]));
  const float d = rb(__builtin_amdgcn_sqrtf(rb(rb(rb(x * x) + rb(y * y) + rb(z * z)) + 1e-8f)));
  if (!(d < 5.0f)) return;
  const float inv = __builtin_amdgcn_rcpf(d);
  const float f = 0.5f * rb(rb(__builtin_amdgcn_cosf(rb(rb(3.141592653589793f * d) * 0.2f) * 0.15915494309189535f)) + 1.0f);
  float w0 = 0.f, w1 = 0.f, w2 = 0.f;
  for (int k = 0; k < 16; ++k) {
    float r = rb(d * lo16(freqs[k])) * 0.15915494309189535f;
    r -= __builtin_rintf(r);
    const float b = rb(__builtin_amdgcn_sinf(r) * inv);
    w0 += b * lo16(wr[k * 192 + c]); w1 += b * lo16(wr[k * 192 + 64 + c]); w2 += b * lo16(wr[k * 192 + 128 + c]);
  }
  const float ms = phi[s * 192 + c] * w0 * f, mv = phi[s * 192 + 64 + c] * w1 * f, mr = phi[s * 192 + 128 + c] * w2 * f;
  float* a = acc + t * 256 + c;
  __hip_atomic_fetch_add(a, ms, __ATOMIC_RELAXED, __HIP_MEMORY_SCOPE_AGENT);
  __hip_atomic_fetch_add(a + 64, mv * lo16(sv[s * 256 + 64 + c]) + mr * rb(x * inv), __ATOMIC_RELAXED, __HIP_MEMORY_SCOPE_AGENT);
  __hip_atomic_fetch_add(a + 128, mv * lo16(sv[s * 256 + 128 + c]) + mr * rb(y * inv), __ATOMIC_RELAXED, __HIP_MEMORY_SCOPE_AGENT);
  __hip_atomic_fetch_add(a + 192, mv * lo16(sv[s * 256 + 192 + c]) + mr * rb(z * inv), __ATOMIC_RELAXED, __HIP_MEMORY_SCOPE_AGENT);
}
extern "C" __global__ __launch_bounds__(256) void k_gen_out(const u16* __restrict__ sv, const float* __restrict__ acc, u16* __restrict__ out) {
  const size_t i = (size_t)BID * 256 + TID;
  out[i] = (u16)pk2(lo16(sv[i]) + acc[i], 0.f);
}

#if defined(__HIP_DEVICE_COMPILE__)
#pragma clang attribute pop
#endif
extern "C" void kernel_launch(void* const* d_in, const int* in_sizes, int n_in, void* d_out, int out_size, void* d_ws, size_t ws_size,
                              hipStream_t stream) {
  const u16* sv = (const u16*)d_in[0];
  const u16* pos = (const u16*)d_in[1];
  const int* src = (const int*)d_in[2];
  const int* dst = (const int*)d_in[3];
  const u16* fr = (const u16*)d_in[4];
  const u16* w1 = (const u16*)d_in[5];
  const u16* w2 = (const u16*)d_in[6];
  const u16* wr = (const u16*)d_in[7];
  const int N = in_sizes[1] / 3, E = in_sizes[2];
  char* ws = (char*)d_ws;
  if (N <= 0) return;
  if (N > 51200 || E > 256 * CH) {
    float* acc = (float*)ws;
    float* phi = acc + (size_t)N * 256;
    (void)hipMemsetAsync(acc, 0, (size_t)N * 1024, stream);
    k_gen_phi<<<N, 192, 0, stream>>>(sv, w1, w2, phi);
    if (E > 0) k_gen_edge<<<(E + 3) / 4, 256, 0, stream>>>(sv, pos, src, dst, fr, wr, phi, acc, E);
    k_gen_out<<<N, 256, 0, stream>>>(sv, acc, (u16*)d_out);
    return;
  }
  const int BN = (N + 255) / 256 < 2 ? 2 : (N + 255) / 256;
  const int B = (N + BN - 1) / BN, bpx = (B + 7) / 8;
  int chunk = (((E + 195) / 196) + 3) & ~3;
  chunk = chunk < 1024 ? 1024 : chunk > CH ? CH : chunk;
  const int W = (E + chunk - 1) / chunk;
  const int NPX = bpx * BN, HW = 200, WP = (W + 7) & ~7;
  const unsigned magicBN = (unsigned)(0x100000000ull / (unsigned)BN) + 1u;
  (void)hipFuncSetAttribute((const void*)k_agg, hipFuncAttributeMaxDynamicSharedMemorySize, L_END);
  k_bin<<<WP + HW + 8, 512, 0, stream>>>(src, dst, (unsigned*)(ws + WS_T), (u32x4*)(ws + WS_SEG), sv, pos, w1, w2, wr, fr, (u32x4*)ws,
                                        (u32x2*)(ws + WS_P), (u16*)(ws + WS_C), ws + WS_H, N, E, W, HW, magicBN, BN, WP, NPX, chunk);
  k_agg<<<8 * bpx, 512, L_END, stream>>>(sv, ws, (u16*)d_out, N, W, BN, B, bpx);
}
```

```cpp
#if defined(__HIP_DEVICE_COMPILE__)
#pragma clang attribute push(__attribute__((target("no-packed-fp32-ops"))), apply_to = function)
#endif
#include <hip/hip_runtime.h>
#include <stdint.h>
#include <type_traits>

#define TID __builtin_amdgcn_workitem_id_x()
#define BID __builtin_amdgcn_workgroup_id_x()
typedef unsigned short u16;
typedef __attribute__((ext_vector_type(8))) __bf16 bf16x8;
typedef __attribute__((ext_vector_type(2))) __bf16 bf16x2;
typedef __attribute__((ext_vector_type(4))) short s16x4;
typedef __attribute__((ext_vector_type(2))) float f32x2;
typedef __attribute__((ext_vector_type(4))) float f32x4;
typedef __attribute__((ext_vector_type(16))) float f32x16;
typedef __attribute__((ext_vector_type(4))) unsigned u32x4;
typedef __attribute__((ext_vector_type(3))) unsigned u32x3;
typedef __attribute__((ext_vector_type(2))) unsigned u32x2;
#define LDSP(T, off) ((__attribute__((address_space(3))) T*)(uintptr_t)(off))
#define DI __device__ __forceinline__
#define SB() __builtin_amdgcn_sched_barrier(0)

constexpr int WT = 17;
constexpr int CH = 4096;
constexpr int SEGQ = CH / 4 + 192;
constexpr int GN = 25;
constexpr int NFRAG = 30;
constexpr int SUBC = 64;
constexpr int CAP = 128;
constexpr int BQ = 256;
constexpr unsigned SENT = 0xFFFFFFFFu;
constexpr int L_DPOS = 0;
constexpr int L_CNT = 4096;
constexpr int L_NB = L_CNT + 256;
constexpr int L_RAW = L_NB + 64;
constexpr int L_LIST = L_RAW + 1024;
constexpr int O_D = 640, O_DL = O_D + 256, O_F = O_DL + 256, O_FX = O_F + 256;
constexpr int LISTS = 1024 + O_FX + 768;
constexpr int L_W = L_RAW + 8 * LISTS;
constexpr int L_FR = L_W + NFRAG * 1024;
constexpr int L_TB = (L_FR + 64 + 511) / 512 * 512;
constexpr int TBS = GN * 512;
constexpr int L_END = L_TB + 8 * TBS;
static_assert(L_END <= 163840 && L_TB < 65536 && L_W % 16 == 0 && LISTS % 16 == 0 && SUBC == 64, "LDS");
constexpr size_t WS_T = 65536;
constexpr size_t WS_SEG = WS_T + 256 * 256 * 4;
constexpr size_t WS_P = WS_SEG + (size_t)256 * SEGQ * 16;
constexpr size_t WS_C = WS_P + 51200 * 8;
constexpr size_t WS_H = WS_C + 51200 * 2;
constexpr size_t WS_END = WS_H + 51200 * 128;
constexpr size_t WS_F = NFRAG * 1024;
static_assert(WS_END < 0xffffff, "ws");

DI float lo16(unsigned x) { return __uint_as_float(x << 16); }
DI float hi16(unsigned x) { return __uint_as_float(x & 0xffff0000u); }
DI unsigned pk2(float a, float b) { f32x2 v = {a, b}; return __builtin_bit_cast(unsigned, __builtin_convertvector(v, bf16x2)); }
DI float rb(float a) { return hi16(pk2(a, a)); }
DI f32x16 mfma(u32x4 a, u32x4 b, f32x16 c) {
  return __builtin_amdgcn_mfma_f32_32x32x16_bf16(__builtin_bit_cast(bf16x8, a), __builtin_bit_cast(bf16x8, b), c, 0, 0, 0);
}
DI f32x16 zero16() { f32x16 z;
#pragma unroll
  for (int r = 0; r < 16; ++r) z[r] = 0.f;
  return z; }
DI int wave_incl_scan(int v) {
  v += __builtin_amdgcn_update_dpp(0, v, 0x111, 0xf, 0xf, false);
  v += __builtin_amdgcn_update_dpp(0, v, 0x112, 0xf, 0xf, false);
  v += __builtin_amdgcn_update_dpp(0, v, 0x114, 0xf, 0xf, false);
  v += __builtin_amdgcn_update_dpp(0, v, 0x118, 0xf, 0xf, false);
  v += __builtin_amdgcn_update_dpp(0, v, 0x142, 0xa, 0xf, false);
  v += __builtin_amdgcn_update_dpp(0, v, 0x143, 0xc, 0xf, false);
  return v;
}
DI int chan_of(int t2, int i32) { return 32 * ((i32 >> 2) & 1) + 16 * t2 + 4 * (i32 >> 3) + (i32 & 3); }
DI u32x4 pack8(const f32x16& x, int s) {
  return u32x4{pk2(x[8 * s], x[8 * s + 1]), pk2(x[8 * s + 2], x[8 * s + 3]), pk2(x[8 * s + 4], x[8 * s + 5]), pk2(x[8 * s + 6], x[8 * s + 7])};
}
DI void setprio(int p) {
  if (p >= 3) __builtin_amdgcn_s_setprio(3);
  else if (p == 2) __builtin_amdgcn_s_setprio(2);
  else if (p == 1) __builtin_amdgcn_s_setprio(1);
  else __builtin_amdgcn_s_setprio(0);
}
DI u32x2 trread(unsigned a) { return __builtin_bit_cast(u32x2, __builtin_amdgcn_ds_read_tr16_b64_v4i16(LDSP(s16x4, a))); }
DI void st16(void* base, unsigned off, u32x4 v) {
  const __amdgpu_buffer_rsrc_t r = __builtin_amdgcn_make_buffer_rsrc(base, 0, 0x7fffffff, 0x00020000);
  __builtin_amdgcn_raw_buffer_store_b128(v, r, off, 0, WT);
}
DI unsigned qmod(float x) { return min((unsigned)(__builtin_amdgcn_fractf(x * 0.0625f) * 65536.0f), 65535u); }
DI unsigned opq(unsigned c) { asm volatile("" : "+v"(c)); return c; }
DI unsigned swap23(unsigned p) { return (p & ~12u) | ((p & 4u) << 1) | ((p & 8u) >> 1); }
DI void wg_barrier() { asm volatile("s_waitcnt lgkmcnt(0)\n\ts_barrier" ::: "memory"); }

DI void dma16(u32x4 rsrc, unsigned voff, unsigned ldsaddr) {
  asm volatile("s_mov_b32 m0, %2\n\ts_nop 3\n\tbuffer_load_dwordx4 %0, %1, 0 offen lds" ::"v"(voff), "s"(rsrc), "s"(ldsaddr) : "memory");
}
DI void dma16x4(u32x4 rsrc, const unsigned (&voff)[4], unsigned soff, unsigned m0) {
  asm volatile("s_mov_b32 m0, %6\n\ts_nop 3\n\tbuffer_load_dwordx4 %0, %4, %5 offen lds\n\t"
               "s_add_u32 m0, m0, 0x400\n\ts_nop 0\n\tbuffer_load_dwordx4 %1, %4, %5 offen lds\n\t"
               "s_add_u32 m0, m0, 0x400\n\ts_nop 0\n\tbuffer_load_dwordx4 %2, %4, %5 offen lds\n\t"
               "s_add_u32 m0, m0, 0x400\n\ts_nop 0\n\tbuffer_load_dwordx4 %3, %4, %5 offen lds"
               ::"v"(voff[0]), "v"(voff[1]), "v"(voff[2]), "v"(voff[3]), "s"(rsrc), "s"(soff), "s"(m0) : "memory", "scc");
}
constexpr int K1_CNT = 0, K1_STAGE = 8 * 256 * 4, K1_TOT = K1_STAGE + SEGQ * 16, K1_W1 = 0, K1_HT = 8192, K1_END = K1_HT + 8 * 4096;
#if defined(__HIP_DEVICE_COMPILE__)
#pragma clang attribute pop
#endif
extern "C" __global__ __launch_bounds__(512, 4) void k_bin(const int* __restrict__ src, const int* __restrict__ dst, unsigned* __restrict__ T,
                                                u32x4* __restrict__ seg, const u16* __restrict__ sv, const u16* __restrict__ pos,
                                                const u16* __restrict__ w1, const u16* __restrict__ w2, const u16* __restrict__ wr, const u16* __restrict__ freqs,
                                                u32x4* __restrict__ wf, u32x2* __restrict__ ptab, u16* __restrict__ ctab, char* __restrict__ htab, int N, int E,
                                                int W, int HW, unsigned magicBN, int BN, int WP, int NPX, int chunk) {
  __shared__ __attribute__((aligned(16))) char lds_raw[K1_END + 256];
  const unsigned L0 = (unsigned)(uintptr_t)(__attribute__((address_space(3))) char*)lds_raw;
  const int tid = TID, lane = tid & 63;
  const int wave = __builtin_amdgcn_readfirstlane(tid >> 6);
  int bid = BID;
  if (bid < WP + HW) { if (bid < WP) { if (bid >= W) return; bid += HW; } else bid -= WP; } else bid += W - WP;
  if (wave == 0) {
    uint64_t pc; unsigned len;
    asm volatile("s_getpc_b64 %0\npainn_self_lo:\n\ts_mov_b32 %1, painn_code_hi-painn_self_lo" : "=s"(pc), "=s"(len));
    const u32x4 rc = {(unsigned)pc, (unsigned)(pc >> 32) & 0xffffu, len, 0x00020000u};
    asm volatile("s_mov_b32 m0, %2\n\ts_nop 3\n\tbuffer_load_dword %0, %1, 0 offen lds" ::"v"(lane * 128), "s"(rc), "s"(L0 + K1_END) : "memory");
  }
  if (bid >= W + HW) {
    {
      uint64_t pc; unsigned len;
      asm volatile("s_getpc_b64 %0\npainn_code_lo:\n\ts_mov_b32 %1, painn_code_hi-painn_code_lo" : "=s"(pc), "=s"(len));
      const __amdgpu_buffer_rsrc_t rc = __builtin_amdgcn_make_buffer_rsrc((void*)pc, 0, len, 0x00020000);
      const unsigned x = __builtin_amdgcn_raw_buffer_load_b32(rc, tid * 64, 0, 0);
      asm volatile("" ::"v"(x));
    }
    const int idx = (bid - W - HW) * 512 + tid;
    if (idx >= NFRAG * 64) {
      if (idx < NFRAG * 64 + 16) ((unsigned*)wf)[WS_F / 4 + idx - NFRAG * 64] = (unsigned)freqs[idx - NFRAG * 64] << 16;
      return;
    }
    const int f = idx >> 6, i32 = lane & 31, h = lane >> 5;
    const u16* p; int s2;
    if (f < 24) { const int ct = f >> 2; p = w2 + (16 * (f & 3) + 4 * h) * 192 + 64 * (ct >> 1) + chan_of(ct & 1, i32); s2 = 8 * 192; }
    else { const int ct = f - 24; p = wr + 8 * h * 192 + 64 * (ct >> 1) + chan_of(ct & 1, i32); s2 = 4 * 192; }
    unsigned v[8];
#pragma unroll
    for (int j = 0; j < 8; ++j) v[j] = p[(j & 3) * 192 + (j >> 2) * s2];
    wf[idx] = u32x4{v[0] | v[1] << 16, v[2] | v[3] << 16, v[4] | v[5] << 16, v[6] | v[7] << 16};
    return;
  }
  if (bid < HW) {
    const int l31 = lane & 31, h = lane >> 5;
    const int nl = (wave * (HW >> 3) + (bid >> 3)) * 32, nend = min(N, ((bid & 7) + 1) * NPX);
    const int n0 = nl < NPX ? (bid & 7) * NPX + nl : N;
    const int nn = min(n0 + l31, N - 1);
    const unsigned HT = L0 + K1_HT + wave * 4096;
    {
      const uint64_t sp = (uint64_t)sv, wp = (uint64_t)w1;
      const u32x4 rs_sv = {(unsigned)sp, (unsigned)(sp >> 32), (unsigned)N * 512u, 0x00020000u};
      const u32x4 rs_w1 = {(unsigned)wp, (unsigned)(wp >> 32), 8192u, 0x00020000u};
      const unsigned o = (unsigned)(n0 + (lane >> 3)) * 512u + 16u * ((lane & 7) ^ (lane >> 3));
      const unsigned v4[4] = {o, o + 4096u, o + 8192u, o + 12288u};
      dma16x4(rs_sv, v4, 0, HT);
      dma16(rs_w1, tid * 16u, L0 + K1_W1 + wave * 1024);
    }
    unsigned px = pos[nn * 3], py = pos[nn * 3 + 1], pz = pos[nn * 3 + 2];
    asm volatile("s_waitcnt vmcnt(0)\n\ts_barrier" : "+v"(px), "+v"(py), "+v"(pz)::"memory");
    if (n0 >= N) return;
    {
      const uint64_t sp = (uint64_t)sv;
      const u32x4 rs_sv = {(unsigned)sp, (unsigned)(sp >> 32), (unsigned)N * 512u, 0x00020000u};
      asm volatile("s_mov_b32 m0, %2\n\ts_nop 3\n\tbuffer_load_dword %0, %1, 0 offen lds"
                   ::"v"(h == 0 ? (unsigned)(n0 + l31) * 512u + 128u : 0x7fffffffu), "s"(rs_sv), "s"(L0 + K1_END) : "memory");
    }
    u32x4 sf[4];
#pragma unroll
    for (int ks = 0; ks < 4; ++ks) sf[ks] = *LDSP(u32x4, HT + l31 * 128 + 16 * ((4 * h + ks) ^ (l31 & 7)));
    const unsigned ta = L0 + K1_W1 + (32 * h + ((lane >> 2) & 3)) * 128 + (16 * ((lane >> 4) & 1) + 4 * (lane & 3)) * 2;
    f32x16 d1[2];
#pragma unroll
    for (int ht = 0; ht < 2; ++ht) {
      d1[ht] = zero16();
#pragma unroll
      for (int ks = 0; ks < 4; ++ks) {
        const u32x2 a0 = trread(ta + ks * 1024 + ht * 64), a1 = trread(ta + ks * 1024 + 512 + ht * 64);
        d1[ht] = mfma(u32x4{a0[0], a0[1], a1[0], a1[1]}, sf[ks], d1[ht]);
      }
    }
#pragma unroll
    for (int ht = 0; ht < 2; ++ht) {
      f32x16 e = d1[ht] * -1.4426950408889634f;
#pragma unroll
      for (int r = 0; r < 16; ++r) e[r] = __builtin_amdgcn_exp2f(e[r]);
      e += 1.0f;
#pragma unroll
      for (int r = 0; r < 16; ++r) e[r] = __builtin_amdgcn_rcpf(e[r]);
      e *= d1[ht];
#pragma unroll
      for (int s = 0; s < 2; ++s) *LDSP(u32x4, HT + l31 * 128 + 16 * ((4 * h + 2 * ht + s) ^ (l31 & 7))) = pack8(e, s);
    }
#pragma unroll
    for (int i = 0; i < 4; ++i) {
      const int n = 8 * i + (lane >> 3);
      const u32x4 v = *LDSP(u32x4, HT + i * 1024 + lane * 16);
      if (n0 + n < nend) st16(htab, (n0 + n) * 128 + 16 * ((lane & 7) ^ (n & 7)), v);
    }
    if (h == 0 && n0 + l31 < nend) {
      ptab[n0 + l31] = u32x2{px | py << 16, pz << 16};
      ctab[n0 + l31] = (u16)(qmod(lo16(px)) >> 11 | (qmod(lo16(py)) >> 11) << 5 | (qmod(lo16(pz)) & 0xfc00u));
    }
    return;
  }
  const int cw = bid - HW;
  const int eend = min(E, (cw + 1) * chunk);
  const __amdgpu_buffer_rsrc_t rb_s = __builtin_amdgcn_make_buffer_rsrc((void*)src, 0, eend * 4, 0x00020000);
  const __amdgpu_buffer_rsrc_t rb_d = __builtin_amdgcn_make_buffer_rsrc((void*)dst, 0, eend * 4, 0x00020000);
  const int e0 = cw * chunk + tid * 4;
  u32x4 s4[2], d4[2];
#pragma unroll
  for (int u = 0; u < 2; ++u) d4[u] = __builtin_amdgcn_raw_buffer_load_b128(rb_d, (e0 + u * 2048) * 4, 0, 0);
#pragma unroll
  for (int u = 0; u < 2; ++u) s4[u] = __builtin_amdgcn_raw_buffer_load_b128(rb_s, (e0 + u * 2048) * 4, 0, 0);
  *LDSP(u32x4, L0 + K1_CNT + wave * 1024 + lane * 16) = u32x4{0, 0, 0, 0};
#pragma unroll
  for (int i = 0; i < 3; ++i) if (i * 512 + tid < SEGQ) *LDSP(u32x4, L0 + K1_STAGE + (i * 512 + tid) * 16) = u32x4{0, 0, 0, 0};
  unsigned ca[8], hi8[8], rank[8];
#pragma unroll
  for (int k = 0; k < 8; ++k) {
    const unsigned d = d4[k >> 2][k & 3];
    const unsigned b = (unsigned)(((uint64_t)d * magicBN) >> 32);
    ca[k] = L0 + K1_CNT + wave * 1024 + b * 4;
    rank[k] = SENT;
    if (e0 + (k >> 2) * 2048 + (k & 3) < eend) rank[k] = __hip_atomic_fetch_add(LDSP(unsigned, ca[k]), 1u, __ATOMIC_RELAXED, __HIP_MEMORY_SCOPE_WORKGROUP);
    const unsigned dl = d - b * (unsigned)BN;
    hi8[k] = dl + ((dl * 41u) >> 10) * 7u + 1u;
  }
  wg_barrier();
  if (wave == 0) {
    u32x4 c[8], tot = {0, 0, 0, 0};
#pragma unroll
    for (int w = 0; w < 8; ++w) { c[w] = *LDSP(u32x4, L0 + K1_CNT + w * 1024 + lane * 16); tot += c[w]; }
    const u32x4 q = (tot + 3u) >> 2;
    const unsigned ls = q[0] + q[1] + q[2] + q[3];
    const unsigned incl = wave_incl_scan(ls);
    u32x4 st; st[0] = incl - ls; st[1] = st[0] + q[0]; st[2] = st[1] + q[1]; st[3] = st[2] + q[2];
    u32x4 run = st * 4u;
#pragma unroll
    for (int w = 0; w < 8; ++w) { *LDSP(u32x4, L0 + K1_CNT + w * 1024 + lane * 16) = run; run += c[w]; }
    if (lane == 63) *LDSP(unsigned, L0 + K1_TOT) = incl;
    st16(T, cw * 1024 + 16 * lane, st | (q << 16));
  }
  wg_barrier();
  unsigned base[8];
#pragma unroll
  for (int k = 0; k < 8; ++k) base[k] = *LDSP(unsigned, ca[k]);
#pragma unroll
  for (int k = 0; k < 8; ++k)
    if (rank[k] != SENT) *LDSP(unsigned, L0 + K1_STAGE + (base[k] + rank[k]) * 4) = s4[k >> 2][k & 3] << 3 | hi8[k] << 24;
  const int total = *LDSP(int, L0 + K1_TOT);
  wg_barrier();
#pragma unroll
  for (int i = 0; i < 3; ++i) {
    const int qd = i * 512 + tid;
    if (qd < total) st16(seg, (cw * SEGQ + qd) * 16, *LDSP(u32x4, L0 + K1_STAGE + qd * 16));
  }
}

#if defined(__HIP_DEVICE_COMPILE__)
#pragma clang attribute push(__attribute__((target("no-packed-fp32-ops"))), apply_to = function)
#endif
DI u32x4 eqmask4(u32x4 a, unsigned b, unsigned ones) {
  u32x4 x;
#pragma unroll
  for (int i = 0; i < 4; ++i) x[i] = a[i] ^ b;
  asm("v_pk_min_u16 %0, %0, %4\n\tv_pk_min_u16 %1, %1, %4\n\tv_pk_min_u16 %2, %2, %4\n\tv_pk_min_u16 %3, %3, %4\n\t"
      "v_pk_sub_u16 %0, %0, %4\n\tv_pk_sub_u16 %1, %1, %4\n\tv_pk_sub_u16 %2, %2, %4\n\tv_pk_sub_u16 %3, %3, %4"
      : "+v"(x[0]), "+v"(x[1]), "+v"(x[2]), "+v"(x[3]) : "s"(ones));
  return x;
}

typedef __attribute__((ext_vector_type(2))) short s16x2;
extern "C" __global__ __launch_bounds__(512, 2) void k_agg(const u16* __restrict__ sv, const char* __restrict__ wf, u16* __restrict__ out, int N,
                                                           int W, int BN, int B, int bpx) {
  const int wave = __builtin_amdgcn_readfirstlane(TID >> 6);
  const int b = ((int)BID & 7) * bpx + ((int)BID >> 3);
  if (b >= B) return;
  constexpr unsigned L0 = 0;
  const int node0 = b * BN;
  const int nb = min(BN, N - node0);
  const uint64_t svp = (uint64_t)sv, wfp = (uint64_t)wf;
  const u32x4 rs_sv = {(unsigned)svp, (unsigned)(svp >> 32), (unsigned)N * 512u, 0x00020000u};
  const u32x4 rs_ws = {(unsigned)wfp, (unsigned)(wfp >> 32), (unsigned)WS_END, 0x00020000u};
#define FRESH_LANE int lane; asm volatile("v_mbcnt_lo_u32_b32 %0, -1, 0\n\tv_mbcnt_hi_u32_b32 %0, -1, %0" : "=v"(lane)); const int l31 = lane & 31, h = lane >> 5; (void)l31; (void)h
  const unsigned TB = L0 + L_TB + wave * TBS;

  f32x16 acc[4][2];
  u32x4 hN[4];
  int batch = 0, r1 = 0, r2 = 0;
  bool first = true;
  const int grp = wave;
  while (true) {
    u32x4 ol[13];
    {
      FRESH_LANE;
      const unsigned lo = 16u * lane;
      const unsigned v4[4] = {lo, lo + 1024u, lo + 2048u, lo + 3072u};
      const unsigned oo = first ? (unsigned)(node0 + wave * GN + h) * 512u + 16u * (l31 ^ h) : 0x7fff0000u;
#define OWNA(i) ((oo ^ (32u * ((i) & 7))) + 1024u * (i))
#define OWN4(j) asm volatile("s_nop 4\n\tbuffer_load_dwordx4 %0, %4, %8, 0 offen\n\tbuffer_load_dwordx4 %1, %5, %8, 0 offen\n\t" \
                             "buffer_load_dwordx4 %2, %6, %8, 0 offen\n\tbuffer_load_dwordx4 %3, %7, %8, 0 offen" \
                             : "=&v"(ol[4 * j]), "=&v"(ol[4 * j + 1]), "=&v"(ol[4 * j + 2]), "=&v"(ol[4 * j + 3]) \
                             : "v"(OWNA(4 * j)), "v"(OWNA(4 * j + 1)), "v"(OWNA(4 * j + 2)), "v"(OWNA(4 * j + 3)), "s"(rs_sv) : "memory")
#define OWN1() asm volatile("s_nop 4\n\tbuffer_load_dwordx4 %0, %1, %2, 0 offen" : "=&v"(ol[12]) : "v"(h ? 0x7fff0000u : OWNA(12)), "s"(rs_sv) : "memory")
      if (wave >= 4) {
        const int rw = wave - 4;
        const unsigned MAP = L_LIST + wave * LISTS + 1024;
        const int cpw = (W + 3) >> 2;
        const int cell = rw * cpw + lane;
        unsigned t;
        u32x2 dp;
        asm volatile("s_nop 4\n\tbuffer_load_dword %0, %2, %4, 0 offen\n\tbuffer_load_dwordx2 %1, %3, %4, 0 offen\n\ts_barrier\n\ts_waitcnt vmcnt(0)"
                     : "=&v"(t), "=&v"(dp) : "v"((lane < cpw && cell < W) ? (int)WS_T + (cell * 256 + b) * 4 : 0x7fffffff),
                       "v"((int)WS_P + (node0 + (2 * rw + h) * GN + l31 - 1) * 8), "s"(rs_ws) : "memory");
        const int q = t >> 16;
        const int incl = wave_incl_scan(q), excl = incl - q;
        const int total = __builtin_amdgcn_readlane(incl, 63);
        const int win0 = batch * BQ;
        const int gq = (int)(WS_SEG / 16) + cell * SEGQ + (int)(t & 0xffffu) - excl;
        const int hi = min(incl, win0 + BQ);
#pragma nounroll
        for (int i = max(excl, win0); i < hi; ++i) *LDSP(int, MAP + (i - win0) * 4) = (gq + i) * 16;
        unsigned mq[4];
#pragma unroll
        for (int s = 0; s < 4; ++s) mq[s] = *LDSP(int, MAP + (64 * s + lane) * 4);
#pragma unroll
        for (int s = 0; s < 4; ++s) if (win0 + 64 * s + lane >= total) mq[s] = 0x7fffffffu;
        dma16x4(rs_ws, mq, 0, L_W + rw * 4096);
        if (lane == 0) *LDSP(int, L_NB + rw * 4) = (total + BQ - 1) / BQ;
        const unsigned qx = (qmod(lo16(dp[0])) - 1024u) & 0xffffu, qy = qmod(hi16(dp[0])) - 1024u, qz = (qmod(__uint_as_float(dp[1])) - 512u) & 0xffffu;
        *LDSP(u32x4, L_DPOS + rw * 1024 + lo) = (unsigned)(l31 - 1) < (unsigned)GN ? u32x4{dp[0], dp[1], qx | qy << 16, qz} : u32x4{0x7fc07fc0u, 0x7fc00000u, 0u, 0x7fff0000u};
      } else {
        __builtin_amdgcn_s_barrier();
#pragma unroll
        for (int i = 0; i < 6; ++i) dma16x4(rs_ws, v4, WS_C + (wave * 6 + i) * 4096, L_TB + (wave * 6 + i) * 4096);
        dma16(rs_ws, WS_C + (96 + wave) * 1024u + lo, L_TB + (96 + wave) * 1024);
      }
      if (lane < 8) *LDSP(unsigned, L_CNT + lane * 32 + wave * 4) = lane >> 3;
      { const unsigned z = opq(0u); *LDSP(u32x4, L_RAW + wave * LISTS + lo) = u32x4{z, z, z, z}; *LDSP(u32x4, L_LIST + wave * LISTS + lo) = u32x4{z, z, z, z}; }
      OWN4(0); OWN1();
      asm volatile("s_waitcnt vmcnt(5)" ::: "memory");
      wg_barrier();
      u32x4 r4[2];
#pragma unroll
      for (int s = 0; s < 2; ++s) r4[s] = *LDSP(u32x4, L_W + (2 * s + h) * 4096 + (l31 >> 2) * 512 + wave * 64 + (lane & 3) * 16);
      OWN4(1);
      const unsigned one = opq(1u);
#pragma unroll
      for (int s = 0; s < 2; ++s) {
        unsigned cd[4];
        u32x2 dq[4];
#pragma unroll
        for (int c = 0; c < 4; ++c) {
          cd[c] = *LDSP(u16, L_TB + ((r4[s][c] >> 2) & 0x3fffffu));
          dq[c] = *LDSP(u32x2, L_DPOS + 8 + ((r4[s][c] >> 20) & 0xff0u));
        }
        unsigned rank[4];
#pragma unroll
        for (int c = 0; c < 4; ++c) {
          const unsigned cxy = ((cd[c] & 0x3ffu) * 0x400800u) & 0xf800f800u, cz = cd[c] & 0xfc00u;
          const unsigned qxy = dq[c][0], qz = dq[c][1];
          const s16x2 dxy = __builtin_bit_cast(s16x2, qxy) - __builtin_bit_cast(s16x2, cxy);
          const s16x2 dz = __builtin_bit_cast(s16x2, qz) - __builtin_bit_cast(s16x2, cz);
          const unsigned d2 = __builtin_amdgcn_sdot2(dxy, dxy, __builtin_amdgcn_sdot2(dz, dz, 0, false), false);
          rank[c] = 0x7fffffffu;
          if (d2 < 488400000u)
            rank[c] = __hip_atomic_fetch_add(LDSP(unsigned, L_CNT + (((r4[s][c] >> 24) & 0xe0u) | wave * 4)), one, __ATOMIC_RELAXED, __HIP_MEMORY_SCOPE_WORKGROUP);
        }
        if (s == 0) OWN4(2);
#pragma unroll
        for (int c = 0; c < 4; ++c) {
          const unsigned p = rank[c] - SUBC * r1;
          if (p < (unsigned)SUBC) *LDSP(unsigned, L_RAW + (r4[s][c] >> 29) * LISTS + (p * 32 + wave * 4)) = r4[s][c];
        }
      }
    }
    wg_barrier();
    bool more1, moreb;
    FRESH_LANE;
    const unsigned LIST = L0 + L_LIST + grp * LISTS;
    int nact = 0, nstep;
    bool lighter;
    unsigned exy[4], ezd[4], ep[4];
    float ef[4];
    u32x4 own[16];
    auto weights = [&](unsigned xy, unsigned zd, float f, unsigned p) {
      const float inv = __builtin_amdgcn_rcpf(hi16(zd));
      const unsigned hxy = pk2(lo16(xy) * inv, hi16(xy) * inv);
      const float hz = rb(lo16(zd) * inv);
      const unsigned fxy = pk2(f * lo16(hxy), f * hi16(hxy));
      const unsigned fzf = pk2(f * hz, f);
      if (p != SENT) {
        const unsigned a2 = LIST + p * 2;
        *LDSP(u16, a2 + O_F) = (u16)(fzf >> 16);
        *LDSP(u16, a2 + O_FX) = (u16)fxy;
        *LDSP(u16, a2 + O_FX + 256) = (u16)(fxy >> 16);
        *LDSP(u16, a2 + O_FX + 512) = (u16)fzf;
      }
    };
    {
      unsigned rec[4], rex[4];
      u32x2 ps[4], pd[4];
#pragma unroll
      for (int i = 0; i < 4; ++i) rec[i] = *LDSP(unsigned, L_RAW + grp * LISTS + lane * 4 + i * 256);
#pragma unroll
      for (int i = 0; i < 4; ++i) rex[i] = *LDSP(unsigned, L_RAW + grp * LISTS + lane * 4 + 1024 + i * 256);
      {
        asm volatile("s_nop 4\n\tbuffer_load_dwordx2 %0, %4, %8, %9 offen\n\tbuffer_load_dwordx2 %1, %5, %8, %9 offen\n\t"
                     "buffer_load_dwordx2 %2, %6, %8, %9 offen\n\tbuffer_load_dwordx2 %3, %7, %8, %9 offen"
                     : "=&v"(ps[0]), "=&v"(ps[1]), "=&v"(ps[2]), "=&v"(ps[3])
                     : "v"(rec[0] & 0xffffffu), "v"(rec[1] & 0xffffffu), "v"(rec[2] & 0xffffffu), "v"(rec[3] & 0xffffffu), "s"(rs_ws), "s"((unsigned)WS_P) : "memory");
      }
#pragma unroll
      for (int i = 0; i < 4; ++i) pd[i] = *LDSP(u32x2, L0 + L_DPOS + ((rec[i] >> 20) & 0xff0u));
      nstep = __builtin_amdgcn_ballot_w64(rec[3] != 0) ? 4 : __builtin_amdgcn_ballot_w64(rec[2] != 0) ? 3 : 2;
      {
        const u32x4 c0 = *LDSP(u32x4, L_CNT + (lane & 7) * 32), c1 = *LDSP(u32x4, L_CNT + (lane & 7) * 32 + 16);
        const int nbv = *LDSP(int, L_NB + (lane & 3) * 4);
        const unsigned mx = max(max(max(c0[0], c0[1]), max(c0[2], c0[3])), max(max(c1[0], c1[1]), max(c1[2], c1[3])));
        more1 = __builtin_amdgcn_ballot_w64(mx > (unsigned)(SUBC * (r1 + 1))) != 0;
        moreb = __builtin_amdgcn_ballot_w64(nbv > batch + 1) != 0;
        const int key = (c0[0] + c0[1] + c0[2] + c0[3] + c1[0] + c1[1] + c1[2] + c1[3]) * 2 + (((lane & 7) >> 2) ^ 1);
        lighter = __builtin_amdgcn_readlane(key, grp) < __builtin_amdgcn_readlane(key, grp ^ 4);
      }
#pragma unroll
      for (int i = 0; i < 2; ++i) *LDSP(unsigned, LIST + (64 * i + lane) * 4) = opq(0xffffffu);
      if (lane < 32) *LDSP(unsigned, LIST + (128 + lane) * 4) = opq(0xffffffu);
      *LDSP(unsigned, LIST + O_D + lane * 4) = 0x3f803f80u;
      *LDSP(unsigned, LIST + O_DL + lane * 4) = 0xffffffffu;
      asm volatile("s_waitcnt vmcnt(4)" : "+v"(ol[0]), "+v"(ol[1]), "+v"(ol[2]), "+v"(ol[3]), "+v"(ol[4]),
                   "+v"(ol[5]), "+v"(ol[6]), "+v"(ol[7]), "+v"(ol[8]), "+v"(ol[9]), "+v"(ol[10]), "+v"(ol[11]), "+v"(ol[12])::"memory");
#pragma unroll
      for (int i = 0; i < 12; ++i) *LDSP(u32x4, TB + i * 1024 + lane * 16) = ol[i];
      if (h == 0) *LDSP(u32x4, TB + 12 * 1024 + lane * 16) = ol[12];
      {
        const unsigned ra = TB + l31 * 512 + 16 * ((4 * h) ^ (l31 & 15));
#pragma unroll
        for (int i = 0; i < 16; ++i) own[i] = *LDSP(u32x4, (ra ^ (16 * (8 * ((i >> 2) & 1) + (i & 3)))) + 256 * (i >> 3));
      }
      asm volatile("s_waitcnt vmcnt(0)" : "+v"(ps[0]), "+v"(ps[1]), "+v"(ps[2]), "+v"(ps[3])::"memory");
      auto exact = [&](unsigned rc, u32x2 a, u32x2 c, unsigned& oxy, unsigned& ozd, float& of, unsigned& op) {
        const unsigned xy = pk2(lo16(a[0]) - lo16(c[0]), hi16(a[0]) - hi16(c[0]));
        const float z = rb(__uint_as_float(a[1]) - __uint_as_float(c[1]));
        const float x = lo16(xy), y = hi16(xy);
        const unsigned sq = pk2(x * x, y * y);
        const float q2 = rb(rb(lo16(sq) + hi16(sq) + rb(z * z)) + 1e-8f);
        const unsigned zd = pk2(z, __builtin_amdgcn_sqrtf(q2));
        const float d = hi16(zd);
        const float f = 0.5f * rb(rb(__builtin_amdgcn_cosf(rb(rb(3.141592653589793f * d) * 0.2f) * 0.15915494309189535f)) + 1.0f);
        const bool ok = (d < 5.0f) & (f != 0.0f);
        const uint64_t bm = __builtin_amdgcn_ballot_w64(ok);
        const unsigned p = __builtin_amdgcn_mbcnt_hi((unsigned)(bm >> 32), __builtin_amdgcn_mbcnt_lo((unsigned)bm, nact - CAP * r2));
        nact += __builtin_popcountll(bm);
        oxy = xy; ozd = zd; of = f; op = SENT;
        if (ok & (p < (unsigned)CAP)) {
          op = p;
          *LDSP(unsigned, LIST + swap23(p) * 4) = rc & 0xffffffu;
          *LDSP(u16, LIST + O_D + swap23(p) * 2) = (u16)(zd >> 16);
          *LDSP(u16, LIST + O_DL + p * 2) = (u16)(rc >> 24);
        }
      };
#pragma unroll
      for (int i = 0; i < 4; ++i) {
        exy[i] = 0; ezd[i] = 0; ef[i] = 0.f; ep[i] = SENT;
        if (i < nstep) exact(rec[i], ps[i], pd[i], exy[i], ezd[i], ef[i], ep[i]);
      }
#pragma nounroll
      while (__builtin_amdgcn_ballot_w64(rex[0] != 0)) {
        u32x2 a;
        asm volatile("s_nop 4\n\tbuffer_load_dwordx2 %0, %1, %2, %3 offen\n\ts_waitcnt vmcnt(0)" : "=&v"(a) : "v"(rex[0] & 0xffffffu), "s"(rs_ws), "s"((unsigned)WS_P) : "memory");
        unsigned xy, zd, p; float f;
        exact(rex[0], a, *LDSP(u32x2, L_DPOS + ((rex[0] >> 20) & 0xff0u)), xy, zd, f, p);
        weights(xy, zd, f, p);
        rex[0] = rex[1]; rex[1] = rex[2]; rex[2] = rex[3]; rex[3] = 0;
      }
    }
    const int cnt = __builtin_amdgcn_readfirstlane(max(min(nact - CAP * r2, CAP), 0));
    if (lane == 0) *LDSP(int, L_NB + 32 + wave * 4) = nact > CAP * (r2 + 1);
    const int ntile = (cnt + 31) >> 5;
    auto issue_h2 = [&](unsigned off) {
      asm volatile("s_nop 4\n\tbuffer_load_dwordx4 %0, %4, %5, %6 offen\n\tbuffer_load_dwordx4 %1, %4, %5, %6 offen offset:16\n\t"
                   "buffer_load_dwordx4 %2, %4, %5, %6 offen offset:32\n\tbuffer_load_dwordx4 %3, %4, %5, %6 offen offset:48"
                   : "=&v"(hN[0]), "=&v"(hN[1]), "=&v"(hN[2]), "=&v"(hN[3]) : "v"(off), "s"(rs_ws), "s"((unsigned)WS_H) : "memory");
    };
    auto issue_h = [&](int t) { FRESH_LANE; issue_h2(*LDSP(unsigned, LIST + (32 * t + l31) * 4) * 16u + 64u * h); };
    auto wait_h = [&]() {
      asm volatile("s_waitcnt vmcnt(12)" : "+v"(hN[0]), "+v"(hN[1]), "+v"(hN[2]), "+v"(hN[3])::"memory");
    };
    unsigned vo[4];
    auto prep_v = [&](int t) {
      FRESH_LANE;
      const int drow = lane >> 3;
      const unsigned dchunk = 16u * (((lane & 7) ^ (drow & 2)) + 8);
#pragma unroll
      for (int i = 0; i < 4; ++i) vo[i] = *LDSP(unsigned, LIST + (32 * t + 8 * i + drow) * 4) * 64u + dchunk;
    };
    auto issue_v = [&](int k) {
      asm volatile("s_waitcnt lgkmcnt(0)" ::: "memory");
      dma16x4(rs_sv, vo, 128 * k, TB + 4096 * k);
    };
    u32x4 rbf;
    auto calc_rbf = [&](int t) {
      FRESH_LANE;
      const float d = lo16(*LDSP(u16, LIST + O_D + 64 * t + 2 * l31));
      const f32x4 f0 = *LDSP(f32x4, L_FR + 32 * h), f1 = *LDSP(f32x4, L_FR + 32 * h + 16);
      const float inv = __builtin_amdgcn_rcpf(d);
#pragma unroll
      for (int i = 0; i < 4; ++i) {
        const float fa = i < 2 ? f0[2 * i] : f1[2 * i - 4], fb = i < 2 ? f0[2 * i + 1] : f1[2 * i - 3];
        const unsigned x = pk2(d * fa, d * fb);
        float ra = lo16(x) * 0.15915494309189535f, rc = hi16(x) * 0.15915494309189535f;
        ra -= __builtin_rintf(ra); rc -= __builtin_rintf(rc);
        rbf[i] = pk2(__builtin_amdgcn_sinf(ra) * inv, __builtin_amdgcn_sinf(rc) * inv);
      }
      asm volatile("" : "+v"(rbf));
    };
    setprio(3);
    issue_h(0);
    prep_v(0);
    {
      const unsigned lo = 16u * lane;
      const unsigned v4[4] = {lo, lo + 1024u, lo + 2048u, lo + 3072u};
      const int wo = min(wave * 4096, NFRAG * 1024 + 64 - 4096);
      dma16x4(rs_ws, v4, wo, L_W + wo);
      wg_barrier();
    }
    const bool late = lighter;
    if (!late) {
      issue_v(0); issue_v(1); issue_v(2);
      asm volatile("s_waitcnt vmcnt(12)\n\ts_barrier" ::: "memory");
    }
    setprio(min(ntile - 1, 2));
    SB();
#pragma unroll
    for (int i = 0; i < 4; ++i) if (i < nstep) weights(exy[i], ezd[i], ef[i], ep[i]);
    if (first) {
#pragma unroll
      for (int i = 0; i < 16; ++i)
#pragma unroll
        for (int k = 0; k < 4; ++k) {
          acc[i >> 2][(i >> 1) & 1][8 * (i & 1) + 2 * k] = lo16(own[i][k]);
          acc[i >> 2][(i >> 1) & 1][8 * (i & 1) + 2 * k + 1] = hi16(own[i][k]);
        }
      first = false;
    }
    SB();
    if (late) {
      asm volatile("s_waitcnt vmcnt(0)\n\ts_barrier" ::: "memory");
      issue_v(0); issue_v(1); issue_v(2);
    }
    const bool more2 = __builtin_amdgcn_ballot_w64(*LDSP(int, L_NB + 32 + (lane & 7) * 4) != 0) != 0;
    calc_rbf(0);
    wait_h();
    auto tile = [&](int t, auto nsc) {
      constexpr int NS = decltype(nsc)::value;
      FRESH_LANE;
      const unsigned WL = L0 + L_W + lane * 16, la = LIST + 64 * t + 16 * h;
      const unsigned mypair = (unsigned)(grp * 32 + l31 + 1) * 0x10001u;
      u32x4 Wb[5];
      auto ldW = [&](int ct) {
        Wb[4] = *LDSP(u32x4, WL + (24 + ct) * 1024);
#pragma unroll
        for (int ks = 0; ks < 4; ++ks) Wb[ks] = *LDSP(u32x4, WL + (4 * ct + ks) * 1024);
      };
      ldW(4);
      u32x4 msk[2], S1[2], sr[3][2];
#pragma unroll
      for (int s = 0; s < NS; ++s) msk[s] = *LDSP(u32x4, la + O_DL + 32 * s);
#pragma unroll
      for (int s = 0; s < NS; ++s)
#pragma unroll
        for (int k = 0; k < 3; ++k) sr[k][s] = *LDSP(u32x4, la + O_FX + 256 * k + 32 * s);
      SB();
      f32x16 ph, w;
      auto gemm = [&]() {
        w = mfma(rbf, Wb[4], zero16());
        ph = mfma(hN[0], Wb[0], zero16());
#pragma unroll
        for (int ks = 1; ks < 4; ++ks) ph = mfma(hN[ks], Wb[ks], ph);
      };
#define PIN1(a) asm volatile("" : "+v"(a))
#define PIN2(a, b) do { PIN1(a); PIN1(b); } while (0)
#define PIN3(a, b, c) asm volatile("" : "+v"(a), "+v"(b), "+v"(c))
      auto pack = [&](u32x4 (&a)[2]) {
        const f32x16 m = ph * w;
#pragma unroll
        for (int s = 0; s < NS; ++s) { a[s] = pack8(m, s); PIN1(a[s]); }
      };
#define MIX(id, lead, n, v) do { __builtin_amdgcn_sched_group_barrier(0x8, lead, id); _Pragma("unroll") for (int i_ = 0; i_ < n; ++i_) { \
        __builtin_amdgcn_sched_group_barrier(0x402, v, id); __builtin_amdgcn_sched_group_barrier(0x8, 1, id); } } while (0)
      u32x4 aa[2], ab[2];
      gemm();
#pragma unroll
      for (int s = 0; s < NS; ++s) {
        msk[s] = eqmask4(msk[s], mypair, 0x00010001u);
#pragma unroll
        for (int k = 0; k < 3; ++k) sr[k][s] &= msk[s];
      }
      MIX(0, 1, 4, 4 * NS);
      PIN2(ph, w);
      SB();
      ldW(5);
      SB();
      pack(aa);
      SB();
      gemm();
      PIN2(ph, w);
      SB();
      ldW(0);
      PIN2(aa[0], aa[NS - 1]);
      SB();
#pragma unroll
      for (int s = 0; s < NS; ++s)
#pragma unroll
        for (int k = 0; k < 3; ++k) acc[1 + k][0] = mfma(aa[s], sr[k][s], acc[1 + k][0]);
      pack(ab);
      MIX(1, 2, 3 * NS - 2, 6);
      PIN3(acc[1][0], acc[2][0], acc[3][0]);
      SB();
      gemm();
      PIN2(ph, w);
      SB();
      ldW(1);
      PIN2(ab[0], ab[NS - 1]);
      SB();
#pragma unroll
      for (int s = 0; s < NS; ++s)
#pragma unroll
        for (int k = 0; k < 3; ++k) acc[1 + k][1] = mfma(ab[s], sr[k][s], acc[1 + k][1]);
      pack(aa);
      MIX(2, 2, 3 * NS - 2, 6);
      PIN3(acc[1][1], acc[2][1], acc[3][1]);
      SB();
      unsigned tra, se, dn;
      f32x4 f0, f1;
      u32x4 rbn;
      {
        FRESH_LANE;
        const unsigned la2 = LIST + 64 * t + 16 * h, mp2 = (unsigned)(grp * 32 + l31 + 1) * 0x10001u;
#pragma unroll
        for (int s = 0; s < NS; ++s) { msk[s] = *LDSP(u32x4, la2 + O_DL + 32 * s); S1[s] = *LDSP(u32x4, la2 + O_F + 32 * s); }
        dn = *LDSP(u16, LIST + O_D + 64 * (t + 1) + 2 * l31);
        f0 = *LDSP(f32x4, L_FR + 32 * h); f1 = *LDSP(f32x4, L_FR + 32 * h + 16);
        se = *LDSP(unsigned, LIST + (32 * (t + 1) + l31) * 4);
#pragma unroll
        for (int i = 0; i < 4; ++i) vo[i] = *LDSP(unsigned, LIST + (32 * (t + 1) + 8 * i + (lane >> 3)) * 4);
        SB();
        gemm();
#pragma unroll
        for (int s = 0; s < NS; ++s) { S1[s] &= eqmask4(msk[s], mp2, 0x00010001u); PIN1(S1[s]); }
        const unsigned dchunk = 16u * (((lane & 7) ^ ((lane >> 3) & 2)) + 8);
#pragma unroll
        for (int i = 0; i < 4; ++i) { vo[i] = vo[i] * 64u + dchunk; PIN1(vo[i]); }
        se = se * 16u + 64u * h; PIN1(se);
        const int q4 = (lane >> 2) & 3, pp = lane & 3;
        tra = (q4 & 1) ? TB + (4 * h + (q4 >> 1)) * 128 + 64 * (pp & 1) + (lane & 16) + 8 * (pp >> 1) : 0x100000u;
        PIN1(tra);
        MIX(3, 2, 3, 8);
        PIN2(ph, w);
        SB();
      }
      ldW(2);
      PIN2(aa[0], aa[NS - 1]);
      SB();
#pragma unroll
      for (int s = 0; s < NS; ++s) acc[0][0] = mfma(aa[s], S1[s], acc[0][0]);
      PIN3(acc[0][0], ph, w);
      SB();
      pack(ab);
      SB();
      const float dnf = lo16(dn), inv = __builtin_amdgcn_rcpf(dnf);
      auto rbf2 = [&](float fa, float fb) {
        const unsigned x = pk2(dnf * fa, dnf * fb);
        float ra = lo16(x) * 0.15915494309189535f, rc = hi16(x) * 0.15915494309189535f;
        ra -= __builtin_rintf(ra); rc -= __builtin_rintf(rc);
        return pk2(__builtin_amdgcn_sinf(ra) * inv, __builtin_amdgcn_sinf(rc) * inv);
      };
      gemm();
      { unsigned r0 = rbf2(f0[0], f0[1]), r1_ = rbf2(f0[2], f0[3]); PIN2(r0, r1_); rbn[0] = r0; rbn[1] = r1_; }
      MIX(4, 1, 4, 6);
      PIN2(ph, w);
      SB();
      ldW(3);
      PIN2(ab[0], ab[NS - 1]);
      SB();
#pragma unroll
      for (int s = 0; s < NS; ++s) acc[0][1] = mfma(ab[s], S1[s], acc[0][1]);
      PIN3(acc[0][1], ph, w);
      SB();
      f32x16 mv[2];
      mv[0] = ph * w;
      PIN1(mv[0]);
      SB();
      gemm();
      { unsigned r0 = rbf2(f1[0], f1[1]), r1_ = rbf2(f1[2], f1[3]); PIN2(r0, r1_); rbn[2] = r0; rbn[3] = r1_; }
      MIX(5, 1, 4, 6);
      PIN2(ph, w);
      SB();
      issue_h2(se);
      rbf = rbn;
      mv[1] = ph * w;
      PIN1(mv[1]);
      SB();
      __builtin_amdgcn_s_setprio(3);
      asm volatile("s_waitcnt vmcnt(4)" ::: "memory");
      u32x2 va[8], vb[8];
      auto ldv = [&](u32x2 (&v)[8], int t2, int k) {
#pragma unroll
        for (int i = 0; i < 4 * NS; ++i)
          v[i] = trread(tra + k * 4096 + (8 * (i >> 1) + 2 * (i & 1)) * 128 + 32 * (t2 ^ (i & 1)));
      };
      auto vv = [&](const u32x2 (&v)[8], const f32x16& m, f32x16& ac) {
        u32x4 a[2];
#pragma unroll
        for (int i = 0; i < 4 * NS; ++i) {
          const unsigned v0 = v[i][0], v1 = v[i][1];
          a[i >> 2][i & 3] = pk2(m[2 * i] * __uint_as_float(v0), m[2 * i + 1] * __uint_as_float(v1));
        }
#pragma unroll
        for (int s = 0; s < NS; ++s) ac = mfma(a[s], S1[s], ac);
      };
      ldv(va, 0, 0); ldv(vb, 1, 0);
      SB();
      vv(va, mv[0], acc[1][0]);
      SB();
      issue_v(0);
      ldv(va, 0, 1);
      SB();
      vv(vb, mv[1], acc[1][1]);
      SB();
      ldv(vb, 1, 1);
      SB();
      vv(va, mv[0], acc[2][0]);
      SB();
      issue_v(1);
      ldv(va, 0, 2);
      SB();
      vv(vb, mv[1], acc[2][1]);
      SB();
      ldv(vb, 1, 2);
      SB();
      vv(va, mv[0], acc[3][0]);
      SB();
      issue_v(2);
      vv(vb, mv[1], acc[3][1]);
      SB();
      wait_h();
    };
    const int nfull = (cnt + 15) >> 5;
    for (int t = 0; t < nfull; ++t) { tile(t, std::integral_constant<int, 2>{}); setprio(min(ntile - 2 - t, 2)); }
    if (nfull < ntile) tile(nfull, std::integral_constant<int, 1>{});
    if (more2) ++r2;
    else if (more1) { ++r1; r2 = 0; }
    else if (moreb) { ++batch; r1 = 0; r2 = 0; }
    else break;
    asm volatile("s_waitcnt vmcnt(0)" ::: "memory");
    wg_barrier();
  }
  {
    FRESH_LANE;
    u32x4 o[16];
#pragma unroll
    for (int i = 0; i < 16; ++i) o[i] = pack8(acc[i >> 2][(i >> 1) & 1], i & 1);
    asm volatile("s_waitcnt vmcnt(0)" ::: "memory");
    if (l31 < GN) {
      const unsigned wa = TB + l31 * 512 + 16 * ((4 * h) ^ (l31 & 7));
#pragma unroll
      for (int i = 0; i < 16; ++i) *LDSP(u32x4, (wa ^ (16 * (i & 3))) + 128 * (i >> 2)) = o[i];
    }
    const int gw = __builtin_amdgcn_readfirstlane(max(min(nb - grp * GN, GN), 0));
    const uint64_t op = (uint64_t)out + (size_t)(node0 + grp * GN) * 512;
    const __amdgpu_buffer_rsrc_t rb_o = __builtin_amdgcn_make_buffer_rsrc((void*)op, 0, gw * 512, 0x00020000);
    u32x4 v[(GN + 1) / 2];
#pragma unroll
    for (int i = 0; i < (GN + 1) / 2; ++i) v[i] = *LDSP(u32x4, TB + i * 1024 + lane * 16);
    const unsigned oa = h * 512 + 16 * (l31 ^ h);
#pragma unroll
    for (int i = 0; i < (GN + 1) / 2; ++i) __builtin_amdgcn_raw_buffer_store_b128(v[i], rb_o, oa ^ (32 * (i & 3)), i * 1024, WT);
  }
  asm volatile("painn_code_hi:");
}

extern "C" __global__ __launch_bounds__(192) void k_gen_phi(const u16* __restrict__ sv, const u16* __restrict__ w1, const u16* __restrict__ w2,
                                                            float* __restrict__ phi) {
  __shared__ float s[64], hd[64];
  const int t = TID; const size_t n = BID;
  if (t < 64) s[t] = lo16(sv[n * 256 + t]);
  wg_barrier();
  if (t < 64) {
    float a = 0.f;
    for (int k = 0; k < 64; ++k) a += s[k] * lo16(w1[k * 64 + t]);
    hd[t] = rb(a * __builtin_amdgcn_rcpf(1.0f + __builtin_amdgcn_exp2f(a * -1.4426950408889634f)));
  }
  wg_barrier();
  float a = 0.f;
  for (int k = 0; k < 64; ++k) a += hd[k] * lo16(w2[k * 192 + t]);
  phi[n * 192 + t] = a;
}
extern "C" __global__ __launch_bounds__(256) void k_gen_edge(const u16* __restrict__ sv, const u16* __restrict__ pos, const int* __restrict__ src,
                                                             const int* __restrict__ dst, const u16* __restrict__ freqs, const u16* __restrict__ wr,
                                                             const float* __restrict__ phi, float* __restrict__ acc, int E) {
  const size_t e = (size_t)BID * 4 + (TID >> 6);
  const int c = TID & 63;
  if (e >= (size_t)E) return;
  const size_t s = src[e], t = dst[e];
  const float x = rb(lo16(pos[s * 3]) - lo16(pos[t * 3])), y = rb(lo16(pos[s * 3 + 1]) - lo16(pos[t * 3 + 1])), z = rb(lo16(pos[s * 3 + 2]) - lo16(pos[t * 3 + 2]));
  const float d = rb(__builtin_amdgcn_sqrtf(rb(rb(rb(x * x) + rb(y * y) + rb(z * z)) + 1e-8f)));
  if (!(d < 5.0f)) return;
  const float inv = __builtin_amdgcn_rcpf(d);
  const float f = 0.5f * rb(rb(__builtin_amdgcn_cosf(rb(rb(3.141592653589793f * d) * 0.2f) * 0.15915494309189535f)) + 1.0f);
  float w0 = 0.f, w1 = 0.f, w2 = 0.f;
  for (int k = 0; k < 16; ++k) {
    float r = rb(d * lo16(freqs[k])) * 0.15915494309189535f;
    r -= __builtin_rintf(r);
    const float b = rb(__builtin_amdgcn_sinf(r) * inv);
    w0 += b * lo16(wr[k * 192 + c]); w1 += b * lo16(wr[k * 192 + 64 + c]); w2 += b * lo16(wr[k * 192 + 128 + c]);
  }
  const float ms = phi[s * 192 + c] * w0 * f, mv = phi[s * 192 + 64 + c] * w1 * f, mr = phi[s * 192 + 128 + c] * w2 * f;
  float* a = acc + t * 256 + c;
  __hip_atomic_fetch_add(a, ms, __ATOMIC_RELAXED, __HIP_MEMORY_SCOPE_AGENT);
  __hip_atomic_fetch_add(a + 64, mv * lo16(sv[s * 256 + 64 + c]) + mr * rb(x * inv), __ATOMIC_RELAXED, __HIP_MEMORY_SCOPE_AGENT);
  __hip_atomic_fetch_add(a + 128, mv * lo16(sv[s * 256 + 128 + c]) + mr * rb(y * inv), __ATOMIC_RELAXED, __HIP_MEMORY_SCOPE_AGENT);
  __hip_atomic_fetch_add(a + 192, mv * lo16(sv[s * 256 + 192 + c]) + mr * rb(z * inv), __ATOMIC_RELAXED, __HIP_MEMORY_SCOPE_AGENT);
}
extern "C" __global__ __launch_bounds__(256) void k_gen_out(const u16* __restrict__ sv, const float* __restrict__ acc, u16* __restrict__ out) {
  const size_t i = (size_t)BID * 256 + TID;
  out[i] = (u16)pk2(lo16(sv[i]) + acc[i], 0.f);
}

extern "C" __global__ __launch_bounds__(512) void k_cal(u32x4* outp, u32x4* wsp, unsigned* sink, int nout, int nws, int mode) {
  const unsigned long long t0 = __builtin_amdgcn_s_memrealtime();
  const int g = BID * 512 + TID, st = 256 * 512;
  unsigned x = 0;
  if (mode & 1) {
    const int gi = (BID >> 3) * 512 + TID;
    for (int i = gi * 256; i < nout; i += 32 * 512 * 256) x ^= outp[i][0];
    for (int i = gi * 256; i < nws; i += 32 * 512 * 256) x ^= wsp[i][0];
  }
  if (mode & 2) { for (int i = g; i < nout; i += st) st16(outp, i * 16, u32x4{1, 2, 3, 4}); for (int i = g; i < nws; i += st) st16(wsp, i * 16, u32x4{1, 2, 3, 4}); }
  float a = TID, b = 1.0001f;
  f32x16 c = zero16();
  while (__builtin_amdgcn_s_memrealtime() - t0 < 3000ull) {
    if (mode & 4) {
#pragma unroll
      for (int i = 0; i < 16; ++i) { a = a * b + 0.5f; }
      c = mfma(u32x4{1, 2, 3, 4}, u32x4{5, 6, 7, 8}, c);
    } else __builtin_amdgcn_s_sleep(2);
  }
  if (x == 0x12345 || a + c[0] == 1.2345f) *sink = x;
}
#if defined(__HIP_DEVICE_COMPILE__)
#pragma clang attribute pop
#endif
extern "C" void kernel_launch(void* const* d_in, const int* in_sizes, int n_in, void* d_out, int out_size, void* d_ws, size_t ws_size,
                              hipStream_t stream) {
  const u16* sv = (const u16*)d_in[0];
  const u16* pos = (const u16*)d_in[1];
  const int* src = (const int*)d_in[2];
  const int* dst = (const int*)d_in[3];
  const u16* fr = (const u16*)d_in[4];
  const u16* w1 = (const u16*)d_in[5];
  const u16* w2 = (const u16*)d_in[6];
  const u16* wr = (const u16*)d_in[7];
  const int N = in_sizes[1] / 3, E = in_sizes[2];
  char* ws = (char*)d_ws;
  if (N <= 0) return;
  if (N > 51200 || E > 256 * CH) {
    float* acc = (float*)ws;
    float* phi = acc + (size_t)N * 256;
    (void)hipMemsetAsync(acc, 0, (size_t)N * 1024, stream);
    k_gen_phi<<<N, 192, 0, stream>>>(sv, w1, w2, phi);
    if (E > 0) k_gen_edge<<<(E + 3) / 4, 256, 0, stream>>>(sv, pos, src, dst, fr, wr, phi, acc, E);
    k_gen_out<<<N, 256, 0, stream>>>(sv, acc, (u16*)d_out);
    return;
  }
  const int BN = (N + 255) / 256 < 2 ? 2 : (N + 255) / 256;
  const int B = (N + BN - 1) / BN, bpx = (B + 7) / 8;
  int chunk = (((E + 195) / 196) + 3) & ~3;
  chunk = chunk < 1024 ? 1024 : chunk > CH ? CH : chunk;
  const int W = (E + chunk - 1) / chunk;
  const int NPX = bpx * BN, HW = 200, WP = (W + 7) & ~7;
  const unsigned magicBN = (unsigned)(0x100000000ull / (unsigned)BN) + 1u;
  (void)hipFuncSetAttribute((const void*)k_agg, hipFuncAttributeMaxDynamicSharedMemorySize, L_END);
  k_bin<<<WP + HW + 8, 512, 0, stream>>>(src, dst, (unsigned*)(ws + WS_T), (u32x4*)(ws + WS_SEG), sv, pos, w1, w2, wr, fr, (u32x4*)ws,
                                        (u32x2*)(ws + WS_P), (u16*)(ws + WS_C), ws + WS_H, N, E, W, HW, magicBN, BN, WP, NPX, chunk);
  k_agg<<<8 * bpx, 512, L_END, stream>>>(sv, ws, (u16*)d_out, N, W, BN, B, bpx);
  k_bin<<<WP + HW + 8, 512, 0, stream>>>(src, dst, (unsigned*)(ws + WS_T), (u32x4*)(ws + WS_SEG), sv, pos, w1, w2, wr, fr, (u32x4*)ws,
                                        (u32x2*)(ws + WS_P), (u16*)(ws + WS_C), ws + WS_H, N, E, W, HW, magicBN, BN, WP, NPX, chunk);
  k_agg<<<8 * bpx, 512, L_END, stream>>>(sv, ws, (u16*)d_out, N, W, BN, B, bpx);
}
```

```cpp
#if defined(__HIP_DEVICE_COMPILE__)
#pragma clang attribute push(__attribute__((target("no-packed-fp32-ops"))), apply_to = function)
#endif
#include <hip/hip_runtime.h>
#include <stdint.h>
#include <type_traits>

#define TID __builtin_amdgcn_workitem_id_x()
#define BID __builtin_amdgcn_workgroup_id_x()
typedef unsigned short u16;
typedef __attribute__((ext_vector_type(8))) __bf16 bf16x8;
typedef __attribute__((ext_vector_type(2))) __bf16 bf16x2;
typedef __attribute__((ext_vector_type(4))) short s16x4;
typedef __attribute__((ext_vector_type(2))) float f32x2;
typedef __attribute__((ext_vector_type(4))) float f32x4;
typedef __attribute__((ext_vector_type(16))) float f32x16;
typedef __attribute__((ext_vector_type(4))) unsigned u32x4;
typedef __attribute__((ext_vector_type(3))) unsigned u32x3;
typedef __attribute__((ext_vector_type(2))) unsigned u32x2;
#define LDSP(T, off) ((__attribute__((address_space(3))) T*)(uintptr_t)(off))
#define DI __device__ __forceinline__
#define SB() __builtin_amdgcn_sched_barrier(0)

constexpr int WT = 17;
constexpr int CH = 4096;
constexpr int SEGQ = CH / 4 + 192;
constexpr int GN = 25;
constexpr int NFRAG = 30;
constexpr int SUBC = 64;
constexpr int CAP = 128;
constexpr int BQ = 256;
constexpr unsigned SENT = 0xFFFFFFFFu;
constexpr int L_DPOS = 0;
constexpr int L_CNT = 4096;
constexpr int L_NB = L_CNT + 256;
constexpr int L_RAW = L_NB + 64;
constexpr int L_LIST = L_RAW + 1024;
constexpr int O_D = 640, O_DL = O_D + 256, O_F = O_DL + 256, O_FX = O_F + 256;
constexpr int LISTS = 1024 + O_FX + 768;
constexpr int L_W = L_RAW + 8 * LISTS;
constexpr int L_FR = L_W + NFRAG * 1024;
constexpr int L_TB = (L_FR + 64 + 511) / 512 * 512;
constexpr int TBS = GN * 512;
constexpr int L_END = L_TB + 8 * TBS;
static_assert(L_END <= 163840 && L_TB < 65536 && L_W % 16 == 0 && LISTS % 16 == 0 && SUBC == 64, "LDS");
constexpr size_t WS_T = 65536;
constexpr size_t WS_SEG = WS_T + 256 * 256 * 4;
constexpr size_t WS_P = WS_SEG + (size_t)256 * SEGQ * 16;
constexpr size_t WS_C = WS_P + 51200 * 8;
constexpr size_t WS_H = WS_C + 51200 * 2;
constexpr size_t WS_END = WS_H + 51200 * 128;
constexpr size_t WS_F = NFRAG * 1024;
static_assert(WS_END < 0xffffff, "ws");

DI float lo16(unsigned x) { return __uint_as_float(x << 16); }
DI float hi16(unsigned x) { return __uint_as_float(x & 0xffff0000u); }
DI unsigned pk2(float a, float b) { f32x2 v = {a, b}; return __builtin_bit_cast(unsigned, __builtin_convertvector(v, bf16x2)); }
DI float rb(float a) { return hi16(pk2(a, a)); }
DI f32x16 mfma(u32x4 a, u32x4 b, f32x16 c) {
  return __builtin_amdgcn_mfma_f32_32x32x16_bf16(__builtin_bit_cast(bf16x8, a), __builtin_bit_cast(bf16x8, b), c, 0, 0, 0);
}
DI f32x16 zero16() { f32x16 z;
#pragma unroll
  for (int r = 0; r < 16; ++r) z[r] = 0.f;
  return z; }
DI int wave_incl_scan(int v) {
  v += __builtin_amdgcn_update_dpp(0, v, 0x111, 0xf, 0xf, false);
  v += __builtin_amdgcn_update_dpp(0, v, 0x112, 0xf, 0xf, false);
  v += __builtin_amdgcn_update_dpp(0, v, 0x114, 0xf, 0xf, false);
  v += __builtin_amdgcn_update_dpp(0, v, 0x118, 0xf, 0xf, false);
  v += __builtin_amdgcn_update_dpp(0, v, 0x142, 0xa, 0xf, false);
  v += __builtin_amdgcn_update_dpp(0, v, 0x143, 0xc, 0xf, false);
  return v;
}
DI int chan_of(int t2, int i32) { return 32 * ((i32 >> 2) & 1) + 16 * t2 + 4 * (i32 >> 3) + (i32 & 3); }
DI u32x4 pack8(const f32x16& x, int s) {
  return u32x4{pk2(x[8 * s], x[8 * s + 1]), pk2(x[8 * s + 2], x[8 * s + 3]), pk2(x[8 * s + 4], x[8 * s + 5]), pk2(x[8 * s + 6], x[8 * s + 7])};
}
DI void setprio(int p) {
  if (p >= 3) __builtin_amdgcn_s_setprio(3);
  else if (p == 2) __builtin_amdgcn_s_setprio(2);
  else if (p == 1) __builtin_amdgcn_s_setprio(1);
  else __builtin_amdgcn_s_setprio(0);
}
DI u32x2 trread(unsigned a) { return __builtin_bit_cast(u32x2, __builtin_amdgcn_ds_read_tr16_b64_v4i16(LDSP(s16x4, a))); }
DI void st16(void* base, unsigned off, u32x4 v) {
  const __amdgpu_buffer_rsrc_t r = __builtin_amdgcn_make_buffer_rsrc(base, 0, 0x7fffffff, 0x00020000);
  __builtin_amdgcn_raw_buffer_store_b128(v, r, off, 0, WT);
}
DI unsigned qmod(float x) { return min((unsigned)(__builtin_amdgcn_fractf(x * 0.0625f) * 65536.0f), 65535u); }
DI unsigned opq(unsigned c) { asm volatile("" : "+v"(c)); return c; }
DI unsigned swap23(unsigned p) { return (p & ~12u) | ((p & 4u) << 1) | ((p & 8u) >> 1); }
DI void wg_barrier() { asm volatile("s_waitcnt lgkmcnt(0)\n\ts_barrier" ::: "memory"); }

DI void dma16(u32x4 rsrc, unsigned voff, unsigned ldsaddr) {
  asm volatile("s_mov_b32 m0, %2\n\ts_nop 3\n\tbuffer_load_dwordx4 %0, %1, 0 offen lds" ::"v"(voff), "s"(rsrc), "s"(ldsaddr) : "memory");
}
DI void dma16x4(u32x4 rsrc, const unsigned (&voff)[4], unsigned soff, unsigned m0) {
  asm volatile("s_mov_b32 m0, %6\n\ts_nop 3\n\tbuffer_load_dwordx4 %0, %4, %5 offen lds\n\t"
               "s_add_u32 m0, m0, 0x400\n\ts_nop 0\n\tbuffer_load_dwordx4 %1, %4, %5 offen lds\n\t"
               "s_add_u32 m0, m0, 0x400\n\ts_nop 0\n\tbuffer_load_dwordx4 %2, %4, %5 offen lds\n\t"
               "s_add_u32 m0, m0, 0x400\n\ts_nop 0\n\tbuffer_load_dwordx4 %3, %4, %5 offen lds"
               ::"v"(voff[0]), "v"(voff[1]), "v"(voff[2]), "v"(voff[3]), "s"(rsrc), "s"(soff), "s"(m0) : "memory", "scc");
}
constexpr int K1_CNT = 0, K1_STAGE = 8 * 256 * 4, K1_TOT = K1_STAGE + SEGQ * 16, K1_W1 = 0, K1_HT = 8192, K1_END = K1_HT + 8 * 4096;
#if defined(__HIP_DEVICE_COMPILE__)
#pragma clang attribute pop
#endif
extern "C" __global__ __launch_bounds__(512, 4) void k_bin(const int* __restrict__ src, const int* __restrict__ dst, const u16* __restrict__ sv,
                                                           const u16* __restrict__ pos, const u16* __restrict__ w1, char* __restrict__ ws,
                                                           unsigned shapeN, int E, unsigned magicBN, unsigned shapeE, const u16* __restrict__ w2,
                                                           const u16* __restrict__ wr, const u16* __restrict__ freqs) {
  asm volatile("" ::"s"(src), "s"(dst), "s"(sv), "s"(pos), "s"(w1), "s"(ws), "s"(shapeN), "s"(E), "s"(magicBN), "s"(shapeE));
  const int N = shapeN & 0xffff, BN = (shapeN >> 16) & 0xff, NPX = (shapeN >> 24) * BN, chunk = shapeE & 0xffff, W = shapeE >> 16, WP = (W + 7) & ~7;
  constexpr int HW = 200;
  unsigned* T = (unsigned*)(ws + WS_T);
  u32x4* seg = (u32x4*)(ws + WS_SEG);
  u32x4* wf = (u32x4*)ws;
  u32x2* ptab = (u32x2*)(ws + WS_P);
  u16* ctab = (u16*)(ws + WS_C);
  char* htab = ws + WS_H;
  __shared__ __attribute__((aligned(16))) char lds_raw[K1_END + 256];
  const unsigned L0 = (unsigned)(uintptr_t)(__attribute__((address_space(3))) char*)lds_raw;
  const int tid = TID, lane = tid & 63;
  const int wave = __builtin_amdgcn_readfirstlane(tid >> 6);
  int bid = BID;
  if (bid < WP + HW) { if (bid < WP) { if (bid >= W) return; bid += HW; } else bid -= WP; } else bid += W - WP;
  if (wave == 0) {
    uint64_t pc; unsigned len;
    asm volatile("s_getpc_b64 %0\npainn_self_lo:\n\ts_mov_b32 %1, painn_code_hi-painn_self_lo" : "=s"(pc), "=s"(len));
    const u32x4 rc = {(unsigned)pc, (unsigned)(pc >> 32) & 0xffffu, len, 0x00020000u};
    asm volatile("s_mov_b32 m0, %2\n\ts_nop 3\n\tbuffer_load_dword %0, %1, 0 offen lds" ::"v"(lane * 128), "s"(rc), "s"(L0 + K1_END) : "memory");
  }
  if (bid >= W + HW) {
    {
      uint64_t pc; unsigned len;
      asm volatile("s_getpc_b64 %0\npainn_code_lo:\n\ts_mov_b32 %1, painn_code_hi-painn_code_lo" : "=s"(pc), "=s"(len));
      const __amdgpu_buffer_rsrc_t rc = __builtin_amdgcn_make_buffer_rsrc((void*)pc, 0, len, 0x00020000);
      const unsigned x = __builtin_amdgcn_raw_buffer_load_b32(rc, tid * 64, 0, 0);
      asm volatile("" ::"v"(x));
    }
    const int idx = (bid - W - HW) * 512 + tid;
    if (idx >= NFRAG * 64) {
      if (idx < NFRAG * 64 + 16) ((unsigned*)wf)[WS_F / 4 + idx - NFRAG * 64] = (unsigned)freqs[idx - NFRAG * 64] << 16;
      return;
    }
    const int f = idx >> 6, i32 = lane & 31, h = lane >> 5;
    const u16* p; int s2;
    if (f < 24) { const int ct = f >> 2; p = w2 + (16 * (f & 3) + 4 * h) * 192 + 64 * (ct >> 1) + chan_of(ct & 1, i32); s2 = 8 * 192; }
    else { const int ct = f - 24; p = wr + 8 * h * 192 + 64 * (ct >> 1) + chan_of(ct & 1, i32); s2 = 4 * 192; }
    unsigned v[8];
#pragma unroll
    for (int j = 0; j < 8; ++j) v[j] = p[(j & 3) * 192 + (j >> 2) * s2];
    wf[idx] = u32x4{v[0] | v[1] << 16, v[2] | v[3] << 16, v[4] | v[5] << 16, v[6] | v[7] << 16};
    return;
  }
  if (bid < HW) {
    const int l31 = lane & 31, h = lane >> 5;
    const int nl = (wave * (HW >> 3) + (bid >> 3)) * 32, nend = min(N, ((bid & 7) + 1) * NPX);
    const int n0 = nl < NPX ? (bid & 7) * NPX + nl : N;
    const int nn = min(n0 + l31, N - 1);
    const unsigned HT = L0 + K1_HT + wave * 4096;
    {
      const uint64_t sp = (uint64_t)sv, wp = (uint64_t)w1;
      const u32x4 rs_sv = {(unsigned)sp, (unsigned)(sp >> 32), (unsigned)N * 512u, 0x00020000u};
      const u32x4 rs_w1 = {(unsigned)wp, (unsigned)(wp >> 32), 8192u, 0x00020000u};
      const unsigned o = (unsigned)(n0 + (lane >> 3)) * 512u + 16u * ((lane & 7) ^ (lane >> 3));
      const unsigned v4[4] = {o, o + 4096u, o + 8192u, o + 12288u};
      dma16x4(rs_sv, v4, 0, HT);
      dma16(rs_w1, tid * 16u, L0 + K1_W1 + wave * 1024);
    }
    unsigned px = pos[nn * 3], py = pos[nn * 3 + 1], pz = pos[nn * 3 + 2];
    asm volatile("s_waitcnt vmcnt(0)\n\ts_barrier" : "+v"(px), "+v"(py), "+v"(pz)::"memory");
    if (n0 >= N) return;
    {
      const uint64_t sp = (uint64_t)sv;
      const u32x4 rs_sv = {(unsigned)sp, (unsigned)(sp >> 32), (unsigned)N * 512u, 0x00020000u};
      asm volatile("s_mov_b32 m0, %2\n\ts_nop 3\n\tbuffer_load_dword %0, %1, 0 offen lds"
                   ::"v"(h == 0 ? (unsigned)(n0 + l31) * 512u + 128u : 0x7fffffffu), "s"(rs_sv), "s"(L0 + K1_END) : "memory");
    }
    u32x4 sf[4];
#pragma unroll
    for (int ks = 0; ks < 4; ++ks) sf[ks] = *LDSP(u32x4, HT + l31 * 128 + 16 * ((4 * h + ks) ^ (l31 & 7)));
    const unsigned ta = L0 + K1_W1 + (32 * h + ((lane >> 2) & 3)) * 128 + (16 * ((lane >> 4) & 1) + 4 * (lane & 3)) * 2;
    f32x16 d1[2];
#pragma unroll
    for (int ht = 0; ht < 2; ++ht) {
      d1[ht] = zero16();
#pragma unroll
      for (int ks = 0; ks < 4; ++ks) {
        const u32x2 a0 = trread(ta + ks * 1024 + ht * 64), a1 = trread(ta + ks * 1024 + 512 + ht * 64);
        d1[ht] = mfma(u32x4{a0[0], a0[1], a1[0], a1[1]}, sf[ks], d1[ht]);
      }
    }
#pragma unroll
    for (int ht = 0; ht < 2; ++ht) {
      f32x16 e = d1[ht] * -1.4426950408889634f;
#pragma unroll
      for (int r = 0; r < 16; ++r) e[r] = __builtin_amdgcn_exp2f(e[r]);
      e += 1.0f;
#pragma unroll
      for (int r = 0; r < 16; ++r) e[r] = __builtin_amdgcn_rcpf(e[r]);
      e *= d1[ht];
#pragma unroll
      for (int s = 0; s < 2; ++s) *LDSP(u32x4, HT + l31 * 128 + 16 * ((4 * h + 2 * ht + s) ^ (l31 & 7))) = pack8(e, s);
    }
#pragma unroll
    for (int i = 0; i < 4; ++i) {
      const int n = 8 * i + (lane >> 3);
      const u32x4 v = *LDSP(u32x4, HT + i * 1024 + lane * 16);
      if (n0 + n < nend) st16(htab, (n0 + n) * 128 + 16 * ((lane & 7) ^ (n & 7)), v);
    }
    if (h == 0 && n0 + l31 < nend) {
      ptab[n0 + l31] = u32x2{px | py << 16, pz << 16};
      ctab[n0 + l31] = (u16)(qmod(lo16(px)) >> 11 | (qmod(lo16(py)) >> 11) << 5 | (qmod(lo16(pz)) & 0xfc00u));
    }
    return;
  }
  const int cw = bid - HW;
  const int eend = min(E, (cw + 1) * chunk);
  const __amdgpu_buffer_rsrc_t rb_s = __builtin_amdgcn_make_buffer_rsrc((void*)src, 0, eend * 4, 0x00020000);
  const __amdgpu_buffer_rsrc_t rb_d = __builtin_amdgcn_make_buffer_rsrc((void*)dst, 0, eend * 4, 0x00020000);
  const int e0 = cw * chunk + tid * 4;
  u32x4 s4[2], d4[2];
#pragma unroll
  for (int u = 0; u < 2; ++u) d4[u] = __builtin_amdgcn_raw_buffer_load_b128(rb_d, (e0 + u * 2048) * 4, 0, 0);
#pragma unroll
  for (int u = 0; u < 2; ++u) s4[u] = __builtin_amdgcn_raw_buffer_load_b128(rb_s, (e0 + u * 2048) * 4, 0, 0);
  *LDSP(u32x4, L0 + K1_CNT + wave * 1024 + lane * 16) = u32x4{0, 0, 0, 0};
#pragma unroll
  for (int i = 0; i < 3; ++i) if (i * 512 + tid < SEGQ) *LDSP(u32x4, L0 + K1_STAGE + (i * 512 + tid) * 16) = u32x4{0, 0, 0, 0};
  unsigned ca[8], hi8[8], rank[8];
#pragma unroll
  for (int k = 0; k < 8; ++k) {
    const unsigned d = d4[k >> 2][k & 3];
    const unsigned b = (unsigned)(((uint64_t)d * magicBN) >> 32);
    ca[k] = L0 + K1_CNT + wave * 1024 + b * 4;
    rank[k] = SENT;
    if (e0 + (k >> 2) * 2048 + (k & 3) < eend) rank[k] = __hip_atomic_fetch_add(LDSP(unsigned, ca[k]), 1u, __ATOMIC_RELAXED, __HIP_MEMORY_SCOPE_WORKGROUP);
    const unsigned dl = d - b * (unsigned)BN;
    hi8[k] = dl + ((dl * 41u) >> 10) * 7u + 1u;
  }
  wg_barrier();
  if (wave == 0) {
    u32x4 c[8], tot = {0, 0, 0, 0};
#pragma unroll
    for (int w = 0; w < 8; ++w) { c[w] = *LDSP(u32x4, L0 + K1_CNT + w * 1024 + lane * 16); tot += c[w]; }
    const u32x4 q = (tot + 3u) >> 2;
    const unsigned ls = q[0] + q[1] + q[2] + q[3];
    const unsigned incl = wave_incl_scan(ls);
    u32x4 st; st[0] = incl - ls; st[1] = st[0] + q[0]; st[2] = st[1] + q[1]; st[3] = st[2] + q[2];
    u32x4 run = st * 4u;
#pragma unroll
    for (int w = 0; w < 8; ++w) { *LDSP(u32x4, L0 + K1_CNT + w * 1024 + lane * 16) = run; run += c[w]; }
    if (lane == 63) *LDSP(unsigned, L0 + K1_TOT) = incl;
    st16(T, cw * 1024 + 16 * lane, st | (q << 16));
  }
  wg_barrier();
  unsigned base[8];
#pragma unroll
  for (int k = 0; k < 8; ++k) base[k] = *LDSP(unsigned, ca[k]);
#pragma unroll
  for (int k = 0; k < 8; ++k)
    if (rank[k] != SENT) *LDSP(unsigned, L0 + K1_STAGE + (base[k] + rank[k]) * 4) = s4[k >> 2][k & 3] << 3 | hi8[k] << 24;
  const int total = *LDSP(int, L0 + K1_TOT);
  wg_barrier();
#pragma unroll
  for (int i = 0; i < 3; ++i) {
    const int qd = i * 512 + tid;
    if (qd < total) st16(seg, (cw * SEGQ + qd) * 16, *LDSP(u32x4, L0 + K1_STAGE + qd * 16));
  }
}

#if defined(__HIP_DEVICE_COMPILE__)
#pragma clang attribute push(__attribute__((target("no-packed-fp32-ops"))), apply_to = function)
#endif
DI u32x4 eqmask4(u32x4 a, unsigned b, unsigned ones) {
  u32x4 x;
#pragma unroll
  for (int i = 0; i < 4; ++i) x[i] = a[i] ^ b;
  asm("v_pk_min_u16 %0, %0, %4\n\tv_pk_min_u16 %1, %1, %4\n\tv_pk_min_u16 %2, %2, %4\n\tv_pk_min_u16 %3, %3, %4\n\t"
      "v_pk_sub_u16 %0, %0, %4\n\tv_pk_sub_u16 %1, %1, %4\n\tv_pk_sub_u16 %2, %2, %4\n\tv_pk_sub_u16 %3, %3, %4"
      : "+v"(x[0]), "+v"(x[1]), "+v"(x[2]), "+v"(x[3]) : "s"(ones));
  return x;
}

typedef __attribute__((ext_vector_type(2))) short s16x2;
extern "C" __global__ __launch_bounds__(512, 2) void k_agg(const u16* __restrict__ sv, const char* __restrict__ wf, u16* __restrict__ out, int N,
                                                           int W, int BN, int B, int bpx) {
  asm volatile("" ::"s"(sv), "s"(wf), "s"(out), "s"(N), "s"(W), "s"(BN), "s"(B), "s"(bpx));
  const int wave = __builtin_amdgcn_readfirstlane(TID >> 6);
  const int b = ((int)BID & 7) * bpx + ((int)BID >> 3);
  if (b >= B) return;
  constexpr unsigned L0 = 0;
  const int node0 = b * BN;
  const int nb = min(BN, N - node0);
  const uint64_t svp = (uint64_t)sv, wfp = (uint64_t)wf;
  const u32x4 rs_sv = {(unsigned)svp, (unsigned)(svp >> 32), (unsigned)N * 512u, 0x00020000u};
  const u32x4 rs_ws = {(unsigned)wfp, (unsigned)(wfp >> 32), (unsigned)WS_END, 0x00020000u};
#define FRESH_LANE int lane; asm volatile("v_mbcnt_lo_u32_b32 %0, -1, 0\n\tv_mbcnt_hi_u32_b32 %0, -1, %0" : "=v"(lane)); const int l31 = lane & 31, h = lane >> 5; (void)l31; (void)h
  const unsigned TB = L0 + L_TB + wave * TBS;

  f32x16 acc[4][2];
  u32x4 hN[4];
  int batch = 0, r1 = 0, r2 = 0;
  bool first = true;
  const int grp = wave;
  while (true) {
    u32x4 ol[13];
    {
      FRESH_LANE;
      const unsigned lo = 16u * lane;
      const unsigned v4[4] = {lo, lo + 1024u, lo + 2048u, lo + 3072u};
      const unsigned oo = first ? (unsigned)(node0 + wave * GN + h) * 512u + 16u * (l31 ^ h) : 0x7fff0000u;
#define OWNA(i) ((oo ^ (32u * ((i) & 7))) + 1024u * (i))
#define OWN4(j) asm volatile("s_nop 4\n\tbuffer_load_dwordx4 %0, %4, %8, 0 offen\n\tbuffer_load_dwordx4 %1, %5, %8, 0 offen\n\t" \
                             "buffer_load_dwordx4 %2, %6, %8, 0 offen\n\tbuffer_load_dwordx4 %3, %7, %8, 0 offen" \
                             : "=&v"(ol[4 * j]), "=&v"(ol[4 * j + 1]), "=&v"(ol[4 * j + 2]), "=&v"(ol[4 * j + 3]) \
                             : "v"(OWNA(4 * j)), "v"(OWNA(4 * j + 1)), "v"(OWNA(4 * j + 2)), "v"(OWNA(4 * j + 3)), "s"(rs_sv) : "memory")
#define OWN1() asm volatile("s_nop 4\n\tbuffer_load_dwordx4 %0, %1, %2, 0 offen" : "=&v"(ol[12]) : "v"(h ? 0x7fff0000u : OWNA(12)), "s"(rs_sv) : "memory")
      if (wave >= 4) {
        const int rw = wave - 4;
        const unsigned MAP = L_LIST + wave * LISTS + 1024;
        const int cpw = (W + 3) >> 2;
        const int cell = rw * cpw + lane;
        unsigned t;
        u32x2 dp;
        asm volatile("s_nop 4\n\tbuffer_load_dword %0, %2, %4, 0 offen\n\tbuffer_load_dwordx2 %1, %3, %4, 0 offen\n\ts_barrier\n\ts_waitcnt vmcnt(0)"
                     : "=&v"(t), "=&v"(dp) : "v"((lane < cpw && cell < W) ? (int)WS_T + (cell * 256 + b) * 4 : 0x7fffffff),
                       "v"((int)WS_P + (node0 + (2 * rw + h) * GN + l31 - 1) * 8), "s"(rs_ws) : "memory");
        const int q = t >> 16;
        const int incl = wave_incl_scan(q), excl = incl - q;
        const int total = __builtin_amdgcn_readlane(incl, 63);
        const int win0 = batch * BQ;
        const int gq = (int)(WS_SEG / 16) + cell * SEGQ + (int)(t & 0xffffu) - excl;
        const int hi = min(incl, win0 + BQ);
#pragma nounroll
        for (int i = max(excl, win0); i < hi; ++i) *LDSP(int, MAP + (i - win0) * 4) = (gq + i) * 16;
        unsigned mq[4];
#pragma unroll
        for (int s = 0; s < 4; ++s) mq[s] = *LDSP(int, MAP + (64 * s + lane) * 4);
#pragma unroll
        for (int s = 0; s < 4; ++s) if (win0 + 64 * s + lane >= total) mq[s] = 0x7fffffffu;
        dma16x4(rs_ws, mq, 0, L_W + rw * 4096);
        if (lane == 0) *LDSP(int, L_NB + rw * 4) = (total + BQ - 1) / BQ;
        const unsigned qx = (qmod(lo16(dp[0])) - 1024u) & 0xffffu, qy = qmod(hi16(dp[0])) - 1024u, qz = (qmod(__uint_as_float(dp[1])) - 512u) & 0xffffu;
        *LDSP(u32x4, L_DPOS + rw * 1024 + lo) = (unsigned)(l31 - 1) < (unsigned)GN ? u32x4{dp[0], dp[1], qx | qy << 16, qz} : u32x4{0x7fc07fc0u, 0x7fc00000u, 0u, 0x7fff0000u};
      } else {
        __builtin_amdgcn_s_barrier();
#pragma unroll
        for (int i = 0; i < 6; ++i) dma16x4(rs_ws, v4, WS_C + (wave * 6 + i) * 4096, L_TB + (wave * 6 + i) * 4096);
        dma16(rs_ws, WS_C + (96 + wave) * 1024u + lo, L_TB + (96 + wave) * 1024);
      }
      if (lane < 8) *LDSP(unsigned, L_CNT + lane * 32 + wave * 4) = lane >> 3;
      { const unsigned z = opq(0u); *LDSP(u32x4, L_RAW + wave * LISTS + lo) = u32x4{z, z, z, z}; *LDSP(u32x4, L_LIST + wave * LISTS + lo) = u32x4{z, z, z, z}; }
      OWN4(0); OWN1();
      asm volatile("s_waitcnt vmcnt(5)" ::: "memory");
      wg_barrier();
      u32x4 r4[2];
#pragma unroll
      for (int s = 0; s < 2; ++s) r4[s] = *LDSP(u32x4, L_W + (2 * s + h) * 4096 + (l31 >> 2) * 512 + wave * 64 + (lane & 3) * 16);
      OWN4(1);
      const unsigned one = opq(1u);
#pragma unroll
      for (int s = 0; s < 2; ++s) {
        unsigned cd[4];
        u32x2 dq[4];
#pragma unroll
        for (int c = 0; c < 4; ++c) {
          cd[c] = *LDSP(u16, L_TB + ((r4[s][c] >> 2) & 0x3fffffu));
          dq[c] = *LDSP(u32x2, L_DPOS + 8 + ((r4[s][c] >> 20) & 0xff0u));
        }
        unsigned rank[4];
#pragma unroll
        for (int c = 0; c < 4; ++c) {
          const unsigned cxy = ((cd[c] & 0x3ffu) * 0x400800u) & 0xf800f800u, cz = cd[c] & 0xfc00u;
          const unsigned qxy = dq[c][0], qz = dq[c][1];
          const s16x2 dxy = __builtin_bit_cast(s16x2, qxy) - __builtin_bit_cast(s16x2, cxy);
          const s16x2 dz = __builtin_bit_cast(s16x2, qz) - __builtin_bit_cast(s16x2, cz);
          const unsigned d2 = __builtin_amdgcn_sdot2(dxy, dxy, __builtin_amdgcn_sdot2(dz, dz, 0, false), false);
          rank[c] = 0x7fffffffu;
          if (d2 < 488400000u)
            rank[c] = __hip_atomic_fetch_add(LDSP(unsigned, L_CNT + (((r4[s][c] >> 24) & 0xe0u) | wave * 4)), one, __ATOMIC_RELAXED, __HIP_MEMORY_SCOPE_WORKGROUP);
        }
        if (s == 0) OWN4(2);
#pragma unroll
        for (int c = 0; c < 4; ++c) {
          const unsigned p = rank[c] - SUBC * r1;
          if (p < (unsigned)SUBC) *LDSP(unsigned, L_RAW + (r4[s][c] >> 29) * LISTS + (p * 32 + wave * 4)) = r4[s][c];
        }
      }
    }
    wg_barrier();
    bool more1, moreb;
    FRESH_LANE;
    const unsigned LIST = L0 + L_LIST + grp * LISTS;
    int nact = 0, nstep;
    bool lighter;
    unsigned exy[4], ezd[4], ep[4];
    float ef[4];
    u32x4 own[16];
    auto weights = [&](unsigned xy, unsigned zd, float f, unsigned p) {
      const float inv = __builtin_amdgcn_rcpf(hi16(zd));
      const unsigned hxy = pk2(lo16(xy) * inv, hi16(xy) * inv);
      const float hz = rb(lo16(zd) * inv);
      const unsigned fxy = pk2(f * lo16(hxy), f * hi16(hxy));
      const unsigned fzf = pk2(f * hz, f);
      if (p != SENT) {
        const unsigned a2 = LIST + p * 2;
        *LDSP(u16, a2 + O_F) = (u16)(fzf >> 16);
        *LDSP(u16, a2 + O_FX) = (u16)fxy;
        *LDSP(u16, a2 + O_FX + 256) = (u16)(fxy >> 16);
        *LDSP(u16, a2 + O_FX + 512) = (u16)fzf;
      }
    };
    {
      unsigned rec[4], rex[4];
      u32x2 ps[4], pd[4];
#pragma unroll
      for (int i = 0; i < 4; ++i) rec[i] = *LDSP(unsigned, L_RAW + grp * LISTS + lane * 4 + i * 256);
#pragma unroll
      for (int i = 0; i < 4; ++i) rex[i] = *LDSP(unsigned, L_RAW + grp * LISTS + lane * 4 + 1024 + i * 256);
      {
        asm volatile("s_nop 4\n\tbuffer_load_dwordx2 %0, %4, %8, %9 offen\n\tbuffer_load_dwordx2 %1, %5, %8, %9 offen\n\t"
                     "buffer_load_dwordx2 %2, %6, %8, %9 offen\n\tbuffer_load_dwordx2 %3, %7, %8, %9 offen"
                     : "=&v"(ps[0]), "=&v"(ps[1]), "=&v"(ps[2]), "=&v"(ps[3])
                     : "v"(rec[0] & 0xffffffu), "v"(rec[1] & 0xffffffu), "v"(rec[2] & 0xffffffu), "v"(rec[3] & 0xffffffu), "s"(rs_ws), "s"((unsigned)WS_P) : "memory");
      }
#pragma unroll
      for (int i = 0; i < 4; ++i) pd[i] = *LDSP(u32x2, L0 + L_DPOS + ((rec[i] >> 20) & 0xff0u));
      nstep = __builtin_amdgcn_ballot_w64(rec[3] != 0) ? 4 : __builtin_amdgcn_ballot_w64(rec[2] != 0) ? 3 : 2;
      {
        const u32x4 c0 = *LDSP(u32x4, L_CNT + (lane & 7) * 32), c1 = *LDSP(u32x4, L_CNT + (lane & 7) * 32 + 16);
        const int nbv = *LDSP(int, L_NB + (lane & 3) * 4);
        const unsigned mx = max(max(max(c0[0], c0[1]), max(c0[2], c0[3])), max(max(c1[0], c1[1]), max(c1[2], c1[3])));
        more1 = __builtin_amdgcn_ballot_w64(mx > (unsigned)(SUBC * (r1 + 1))) != 0;
        moreb = __builtin_amdgcn_ballot_w64(nbv > batch + 1) != 0;
        const int key = (c0[0] + c0[1] + c0[2] + c0[3] + c1[0] + c1[1] + c1[2] + c1[3]) * 2 + (((lane & 7) >> 2) ^ 1);
        lighter = __builtin_amdgcn_readlane(key, grp) < __builtin_amdgcn_readlane(key, grp ^ 4);
      }
#pragma unroll
      for (int i = 0; i < 2; ++i) *LDSP(unsigned, LIST + (64 * i + lane) * 4) = opq(0xffffffu);
      if (lane < 32) *LDSP(unsigned, LIST + (128 + lane) * 4) = opq(0xffffffu);
      *LDSP(unsigned, LIST + O_D + lane * 4) = 0x3f803f80u;
      *LDSP(unsigned, LIST + O_DL + lane * 4) = 0xffffffffu;
      asm volatile("s_waitcnt vmcnt(4)" : "+v"(ol[0]), "+v"(ol[1]), "+v"(ol[2]), "+v"(ol[3]), "+v"(ol[4]),
                   "+v"(ol[5]), "+v"(ol[6]), "+v"(ol[7]), "+v"(ol[8]), "+v"(ol[9]), "+v"(ol[10]), "+v"(ol[11]), "+v"(ol[12])::"memory");
#pragma unroll
      for (int i = 0; i < 12; ++i) *LDSP(u32x4, TB + i * 1024 + lane * 16) = ol[i];
      if (h == 0) *LDSP(u32x4, TB + 12 * 1024 + lane * 16) = ol[12];
      {
        const unsigned ra = TB + l31 * 512 + 16 * ((4 * h) ^ (l31 & 15));
#pragma unroll
        for (int i = 0; i < 16; ++i) own[i] = *LDSP(u32x4, (ra ^ (16 * (8 * ((i >> 2) & 1) + (i & 3)))) + 256 * (i >> 3));
      }
      asm volatile("s_waitcnt vmcnt(0)" : "+v"(ps[0]), "+v"(ps[1]), "+v"(ps[2]), "+v"(ps[3])::"memory");
      auto exact = [&](unsigned rc, u32x2 a, u32x2 c, unsigned& oxy, unsigned& ozd, float& of, unsigned& op) {
        const unsigned xy = pk2(lo16(a[0]) - lo16(c[0]), hi16(a[0]) - hi16(c[0]));
        const float z = rb(__uint_as_float(a[1]) - __uint_as_float(c[1]));
        const float x = lo16(xy), y = hi16(xy);
        const unsigned sq = pk2(x * x, y * y);
        const float q2 = rb(rb(lo16(sq) + hi16(sq) + rb(z * z)) + 1e-8f);
        const unsigned zd = pk2(z, __builtin_amdgcn_sqrtf(q2));
        const float d = hi16(zd);
        const float f = 0.5f * rb(rb(__builtin_amdgcn_cosf(rb(rb(3.141592653589793f * d) * 0.2f) * 0.15915494309189535f)) + 1.0f);
        const bool ok = (d < 5.0f) & (f != 0.0f);
        const uint64_t bm = __builtin_amdgcn_ballot_w64(ok);
        const unsigned p = __builtin_amdgcn_mbcnt_hi((unsigned)(bm >> 32), __builtin_amdgcn_mbcnt_lo((unsigned)bm, nact - CAP * r2));
        nact += __builtin_popcountll(bm);
        oxy = xy; ozd = zd; of = f; op = SENT;
        if (ok & (p < (unsigned)CAP)) {
          op = p;
          *LDSP(unsigned, LIST + swap23(p) * 4) = rc & 0xffffffu;
          *LDSP(u16, LIST + O_D + swap23(p) * 2) = (u16)(zd >> 16);
          *LDSP(u16, LIST + O_DL + p * 2) = (u16)(rc >> 24);
        }
      };
#pragma unroll
      for (int i = 0; i < 4; ++i) {
        exy[i] = 0; ezd[i] = 0; ef[i] = 0.f; ep[i] = SENT;
        if (i < nstep) exact(rec[i], ps[i], pd[i], exy[i], ezd[i], ef[i], ep[i]);
      }
#pragma nounroll
      while (__builtin_amdgcn_ballot_w64(rex[0] != 0)) {
        u32x2 a;
        asm volatile("s_nop 4\n\tbuffer_load_dwordx2 %0, %1, %2, %3 offen\n\ts_waitcnt vmcnt(0)" : "=&v"(a) : "v"(rex[0] & 0xffffffu), "s"(rs_ws), "s"((unsigned)WS_P) : "memory");
        unsigned xy, zd, p; float f;
        exact(rex[0], a, *LDSP(u32x2, L_DPOS + ((rex[0] >> 20) & 0xff0u)), xy, zd, f, p);
        weights(xy, zd, f, p);
        rex[0] = rex[1]; rex[1] = rex[2]; rex[2] = rex[3]; rex[3] = 0;
      }
    }
    const int cnt = __builtin_amdgcn_readfirstlane(max(min(nact - CAP * r2, CAP), 0));
    if (lane == 0) *LDSP(int, L_NB + 32 + wave * 4) = nact > CAP * (r2 + 1);
    const int ntile = (cnt + 31) >> 5;
    auto issue_h2 = [&](unsigned off) {
      asm volatile("s_nop 4\n\tbuffer_load_dwordx4 %0, %4, %5, %6 offen\n\tbuffer_load_dwordx4 %1, %4, %5, %6 offen offset:16\n\t"
                   "buffer_load_dwordx4 %2, %4, %5, %6 offen offset:32\n\tbuffer_load_dwordx4 %3, %4, %5, %6 offen offset:48"
                   : "=&v"(hN[0]), "=&v"(hN[1]), "=&v"(hN[2]), "=&v"(hN[3]) : "v"(off), "s"(rs_ws), "s"((unsigned)WS_H) : "memory");
    };
    auto issue_h = [&](int t) { FRESH_LANE; issue_h2(*LDSP(unsigned, LIST + (32 * t + l31) * 4) * 16u + 64u * h); };
    auto wait_h = [&]() {
      asm volatile("s_waitcnt vmcnt(12)" : "+v"(hN[0]), "+v"(hN[1]), "+v"(hN[2]), "+v"(hN[3])::"memory");
    };
    unsigned vo[4];
    auto prep_v = [&](int t) {
      FRESH_LANE;
      const int drow = lane >> 3;
      const unsigned dchunk = 16u * (((lane & 7) ^ (drow & 2)) + 8);
#pragma unroll
      for (int i = 0; i < 4; ++i) vo[i] = *LDSP(unsigned, LIST + (32 * t + 8 * i + drow) * 4) * 64u + dchunk;
    };
    auto issue_v = [&](int k) {
      asm volatile("s_waitcnt lgkmcnt(0)" ::: "memory");
      dma16x4(rs_sv, vo, 128 * k, TB + 4096 * k);
    };
    u32x4 rbf;
    auto calc_rbf = [&](int t) {
      FRESH_LANE;
      const float d = lo16(*LDSP(u16, LIST + O_D + 64 * t + 2 * l31));
      const f32x4 f0 = *LDSP(f32x4, L_FR + 32 * h), f1 = *LDSP(f32x4, L_FR + 32 * h + 16);
      const float inv = __builtin_amdgcn_rcpf(d);
#pragma unroll
      for (int i = 0; i < 4; ++i) {
        const float fa = i < 2 ? f0[2 * i] : f1[2 * i - 4], fb = i < 2 ? f0[2 * i + 1] : f1[2 * i - 3];
        const unsigned x = pk2(d * fa, d * fb);
        float ra = lo16(x) * 0.15915494309189535f, rc = hi16(x) * 0.15915494309189535f;
        ra -= __builtin_rintf(ra); rc -= __builtin_rintf(rc);
        rbf[i] = pk2(__builtin_amdgcn_sinf(ra) * inv, __builtin_amdgcn_sinf(rc) * inv);
      }
      asm volatile("" : "+v"(rbf));
    };
    setprio(3);
    issue_h(0);
    prep_v(0);
    {
      const unsigned lo = 16u * lane;
      const unsigned v4[4] = {lo, lo + 1024u, lo + 2048u, lo + 3072u};
      const int wo = min(wave * 4096, NFRAG * 1024 + 64 - 4096);
      dma16x4(rs_ws, v4, wo, L_W + wo);
      wg_barrier();
    }
    const bool late = lighter;
    if (!late) {
      issue_v(0); issue_v(1); issue_v(2);
      asm volatile("s_waitcnt vmcnt(12)\n\ts_barrier" ::: "memory");
    }
    setprio(min(ntile - 1, 2));
    SB();
#pragma unroll
    for (int i = 0; i < 4; ++i) if (i < nstep) weights(exy[i], ezd[i], ef[i], ep[i]);
    if (first) {
#pragma unroll
      for (int i = 0; i < 16; ++i)
#pragma unroll
        for (int k = 0; k < 4; ++k) {
          acc[i >> 2][(i >> 1) & 1][8 * (i & 1) + 2 * k] = lo16(own[i][k]);
          acc[i >> 2][(i >> 1) & 1][8 * (i & 1) + 2 * k + 1] = hi16(own[i][k]);
        }
      first = false;
    }
    SB();
    if (late) {
      asm volatile("s_waitcnt vmcnt(0)\n\ts_barrier" ::: "memory");
      issue_v(0); issue_v(1); issue_v(2);
    }
    const bool more2 = __builtin_amdgcn_ballot_w64(*LDSP(int, L_NB + 32 + (lane & 7) * 4) != 0) != 0;
    calc_rbf(0);
    wait_h();
    auto tile = [&](int t, auto nsc) {
      constexpr int NS = decltype(nsc)::value;
      FRESH_LANE;
      const unsigned WL = L0 + L_W + lane * 16, la = LIST + 64 * t + 16 * h;
      const unsigned mypair = (unsigned)(grp * 32 + l31 + 1) * 0x10001u;
      u32x4 Wb[5];
      auto ldW = [&](int ct) {
        Wb[4] = *LDSP(u32x4, WL + (24 + ct) * 1024);
#pragma unroll
        for (int ks = 0; ks < 4; ++ks) Wb[ks] = *LDSP(u32x4, WL + (4 * ct + ks) * 1024);
      };
      ldW(4);
      u32x4 msk[2], S1[2], sr[3][2];
#pragma unroll
      for (int s = 0; s < NS; ++s) msk[s] = *LDSP(u32x4, la + O_DL + 32 * s);
#pragma unroll
      for (int s = 0; s < NS; ++s)
#pragma unroll
        for (int k = 0; k < 3; ++k) sr[k][s] = *LDSP(u32x4, la + O_FX + 256 * k + 32 * s);
      SB();
      f32x16 ph, w;
      auto gemm = [&]() {
        w = mfma(rbf, Wb[4], zero16());
        ph = mfma(hN[0], Wb[0], zero16());
#pragma unroll
        for (int ks = 1; ks < 4; ++ks) ph = mfma(hN[ks], Wb[ks], ph);
      };
#define PIN1(a) asm volatile("" : "+v"(a))
#define PIN2(a, b) do { PIN1(a); PIN1(b); } while (0)
#define PIN3(a, b, c) asm volatile("" : "+v"(a), "+v"(b), "+v"(c))
      auto pack = [&](u32x4 (&a)[2]) {
        const f32x16 m = ph * w;
#pragma unroll
        for (int s = 0; s < NS; ++s) { a[s] = pack8(m, s); PIN1(a[s]); }
      };
#define MIX(id, lead, n, v) do { __builtin_amdgcn_sched_group_barrier(0x8, lead, id); _Pragma("unroll") for (int i_ = 0; i_ < n; ++i_) { \
        __builtin_amdgcn_sched_group_barrier(0x402, v, id); __builtin_amdgcn_sched_group_barrier(0x8, 1, id); } } while (0)
      u32x4 aa[2], ab[2];
      gemm();
#pragma unroll
      for (int s = 0; s < NS; ++s) {
        msk[s] = eqmask4(msk[s], mypair, 0x00010001u);
#pragma unroll
        for (int k = 0; k < 3; ++k) sr[k][s] &= msk[s];
      }
      MIX(0, 1, 4, 4 * NS);
      PIN2(ph, w);
      SB();
      ldW(5);
      SB();
      pack(aa);
      SB();
      gemm();
      PIN2(ph, w);
      SB();
      ldW(0);
      PIN2(aa[0], aa[NS - 1]);
      SB();
#pragma unroll
      for (int s = 0; s < NS; ++s)
#pragma unroll
        for (int k = 0; k < 3; ++k) acc[1 + k][0] = mfma(aa[s], sr[k][s], acc[1 + k][0]);
      pack(ab);
      MIX(1, 2, 3 * NS - 2, 6);
      PIN3(acc[1][0], acc[2][0], acc[3][0]);
      SB();
      gemm();
      PIN2(ph, w);
      SB();
      ldW(1);
      PIN2(ab[0], ab[NS - 1]);
      SB();
#pragma unroll
      for (int s = 0; s < NS; ++s)
#pragma unroll
        for (int k = 0; k < 3; ++k) acc[1 + k][1] = mfma(ab[s], sr[k][s], acc[1 + k][1]);
      pack(aa);
      MIX(2, 2, 3 * NS - 2, 6);
      PIN3(acc[1][1], acc[2][1], acc[3][1]);
      SB();
      unsigned tra, se, dn;
      f32x4 f0, f1;
      u32x4 rbn;
      {
        FRESH_LANE;
        const unsigned la2 = LIST + 64 * t + 16 * h, mp2 = (unsigned)(grp * 32 + l31 + 1) * 0x10001u;
#pragma unroll
        for (int s = 0; s < NS; ++s) { msk[s] = *LDSP(u32x4, la2 + O_DL + 32 * s); S1[s] = *LDSP(u32x4, la2 + O_F + 32 * s); }
        dn = *LDSP(u16, LIST + O_D + 64 * (t + 1) + 2 * l31);
        f0 = *LDSP(f32x4, L_FR + 32 * h); f1 = *LDSP(f32x4, L_FR + 32 * h + 16);
        se = *LDSP(unsigned, LIST + (32 * (t + 1) + l31) * 4);
#pragma unroll
        for (int i = 0; i < 4; ++i) vo[i] = *LDSP(unsigned, LIST + (32 * (t + 1) + 8 * i + (lane >> 3)) * 4);
        SB();
        gemm();
#pragma unroll
        for (int s = 0; s < NS; ++s) { S1[s] &= eqmask4(msk[s], mp2, 0x00010001u); PIN1(S1[s]); }
        const unsigned dchunk = 16u * (((lane & 7) ^ ((lane >> 3) & 2)) + 8);
#pragma unroll
        for (int i = 0; i < 4; ++i) { vo[i] = vo[i] * 64u + dchunk; PIN1(vo[i]); }
        se = se * 16u + 64u * h; PIN1(se);
        const int q4 = (lane >> 2) & 3, pp = lane & 3;
        tra = (q4 & 1) ? TB + (4 * h + (q4 >> 1)) * 128 + 64 * (pp & 1) + (lane & 16) + 8 * (pp >> 1) : 0x100000u;
        PIN1(tra);
        MIX(3, 2, 3, 8);
        PIN2(ph, w);
        SB();
      }
      ldW(2);
      PIN2(aa[0], aa[NS - 1]);
      SB();
#pragma unroll
      for (int s = 0; s < NS; ++s) acc[0][0] = mfma(aa[s], S1[s], acc[0][0]);
      PIN3(acc[0][0], ph, w);
      SB();
      pack(ab);
      SB();
      const float dnf = lo16(dn), inv = __builtin_amdgcn_rcpf(dnf);
      auto rbf2 = [&](float fa, float fb) {
        const unsigned x = pk2(dnf * fa, dnf * fb);
        float ra = lo16(x) * 0.15915494309189535f, rc = hi16(x) * 0.15915494309189535f;
        ra -= __builtin_rintf(ra); rc -= __builtin_rintf(rc);
        return pk2(__builtin_amdgcn_sinf(ra) * inv, __builtin_amdgcn_sinf(rc) * inv);
      };
      gemm();
      { unsigned r0 = rbf2(f0[0], f0[1]), r1_ = rbf2(f0[2], f0[3]); PIN2(r0, r1_); rbn[0] = r0; rbn[1] = r1_; }
      MIX(4, 1, 4, 6);
      PIN2(ph, w);
      SB();
      ldW(3);
      PIN2(ab[0], ab[NS - 1]);
      SB();
#pragma unroll
      for (int s = 0; s < NS; ++s) acc[0][1] = mfma(ab[s], S1[s], acc[0][1]);
      PIN3(acc[0][1], ph, w);
      SB();
      f32x16 mv[2];
      mv[0] = ph * w;
      PIN1(mv[0]);
      SB();
      gemm();
      { unsigned r0 = rbf2(f1[0], f1[1]), r1_ = rbf2(f1[2], f1[3]); PIN2(r0, r1_); rbn[2] = r0; rbn[3] = r1_; }
      MIX(5, 1, 4, 6);
      PIN2(ph, w);
      SB();
      issue_h2(se);
      rbf = rbn;
      mv[1] = ph * w;
      PIN1(mv[1]);
      SB();
      __builtin_amdgcn_s_setprio(3);
      asm volatile("s_waitcnt vmcnt(4)" ::: "memory");
      u32x2 va[8], vb[8];
      auto ldv = [&](u32x2 (&v)[8], int t2, int k) {
#pragma unroll
        for (int i = 0; i < 4 * NS; ++i)
          v[i] = trread(tra + k * 4096 + (8 * (i >> 1) + 2 * (i & 1)) * 128 + 32 * (t2 ^ (i & 1)));
      };
      auto vv = [&](const u32x2 (&v)[8], const f32x16& m, f32x16& ac) {
        u32x4 a[2];
#pragma unroll
        for (int i = 0; i < 4 * NS; ++i) {
          const unsigned v0 = v[i][0], v1 = v[i][1];
          a[i >> 2][i & 3] = pk2(m[2 * i] * __uint_as_float(v0), m[2 * i + 1] * __uint_as_float(v1));
        }
#pragma unroll
        for (int s = 0; s < NS; ++s) ac = mfma(a[s], S1[s], ac);
      };
      ldv(va, 0, 0); ldv(vb, 1, 0);
      SB();
      vv(va, mv[0], acc[1][0]);
      SB();
      issue_v(0);
      ldv(va, 0, 1);
      SB();
      vv(vb, mv[1], acc[1][1]);
      SB();
      ldv(vb, 1, 1);
      SB();
      vv(va, mv[0], acc[2][0]);
      SB();
      issue_v(1);
      ldv(va, 0, 2);
      SB();
      vv(vb, mv[1], acc[2][1]);
      SB();
      ldv(vb, 1, 2);
      SB();
      vv(va, mv[0], acc[3][0]);
      SB();
      issue_v(2);
      vv(vb, mv[1], acc[3][1]);
      SB();
      wait_h();
    };
    const int nfull = (cnt + 15) >> 5;
    for (int t = 0; t < nfull; ++t) { tile(t, std::integral_constant<int, 2>{}); setprio(min(ntile - 2 - t, 2)); }
    if (nfull < ntile) tile(nfull, std::integral_constant<int, 1>{});
    if (more2) ++r2;
    else if (more1) { ++r1; r2 = 0; }
    else if (moreb) { ++batch; r1 = 0; r2 = 0; }
    else break;
    asm volatile("s_waitcnt vmcnt(0)" ::: "memory");
    wg_barrier();
  }
  {
    FRESH_LANE;
    u32x4 o[16];
#pragma unroll
    for (int i = 0; i < 16; ++i) o[i] = pack8(acc[i >> 2][(i >> 1) & 1], i & 1);
    asm volatile("s_waitcnt vmcnt(0)" ::: "memory");
    if (l31 < GN) {
      const unsigned wa = TB + l31 * 512 + 16 * ((4 * h) ^ (l31 & 7));
#pragma unroll
      for (int i = 0; i < 16; ++i) *LDSP(u32x4, (wa ^ (16 * (i & 3))) + 128 * (i >> 2)) = o[i];
    }
    const int gw = __builtin_amdgcn_readfirstlane(max(min(nb - grp * GN, GN), 0));
    const uint64_t op = (uint64_t)out + (size_t)(node0 + grp * GN) * 512;
    const __amdgpu_buffer_rsrc_t rb_o = __builtin_amdgcn_make_buffer_rsrc((void*)op, 0, gw * 512, 0x00020000);
    u32x4 v[(GN + 1) / 2];
#pragma unroll
    for (int i = 0; i < (GN + 1) / 2; ++i) v[i] = *LDSP(u32x4, TB + i * 1024 + lane * 16);
    const unsigned oa = h * 512 + 16 * (l31 ^ h);
#pragma unroll
    for (int i = 0; i < (GN + 1) / 2; ++i) __builtin_amdgcn_raw_buffer_store_b128(v[i], rb_o, oa ^ (32 * (i & 3)), i * 1024, WT);
  }
  asm volatile("painn_code_hi:");
}

extern "C" __global__ __launch_bounds__(192) void k_gen_phi(const u16* __restrict__ sv, const u16* __restrict__ w1, const u16* __restrict__ w2,
                                                            float* __restrict__ phi) {
  __shared__ float s[64], hd[64];
  const int t = TID; const size_t n = BID;
  if (t < 64) s[t] = lo16(sv[n * 256 + t]);
  wg_barrier();
  if (t < 64) {
    float a = 0.f;
    for (int k = 0; k < 64; ++k) a += s[k] * lo16(w1[k * 64 + t]);
    hd[t] = rb(a * __builtin_amdgcn_rcpf(1.0f + __builtin_amdgcn_exp2f(a * -1.4426950408889634f)));
  }
  wg_barrier();
  float a = 0.f;
  for (int k = 0; k < 64; ++k) a += hd[k] * lo16(w2[k * 192 + t]);
  phi[n * 192 + t] = a;
}
extern "C" __global__ __launch_bounds__(256) void k_gen_edge(const u16* __restrict__ sv, const u16* __restrict__ pos, const int* __restrict__ src,
                                                             const int* __restrict__ dst, const u16* __restrict__ freqs, const u16* __restrict__ wr,
                                                             const float* __restrict__ phi, float* __restrict__ acc, int E) {
  const size_t e = (size_t)BID * 4 + (TID >> 6);
  const int c = TID & 63;
  if (e >= (size_t)E) return;
  const size_t s = src[e], t = dst[e];
  const float x = rb(lo16(pos[s * 3]) - lo16(pos[t * 3])), y = rb(lo16(pos[s * 3 + 1]) - lo16(pos[t * 3 + 1])), z = rb(lo16(pos[s * 3 + 2]) - lo16(pos[t * 3 + 2]));
  const float d = rb(__builtin_amdgcn_sqrtf(rb(rb(rb(x * x) + rb(y * y) + rb(z * z)) + 1e-8f)));
  if (!(d < 5.0f)) return;
  const float inv = __builtin_amdgcn_rcpf(d);
  const float f = 0.5f * rb(rb(__builtin_amdgcn_cosf(rb(rb(3.141592653589793f * d) * 0.2f) * 0.15915494309189535f)) + 1.0f);
  float w0 = 0.f, w1 = 0.f, w2 = 0.f;
  for (int k = 0; k < 16; ++k) {
    float r = rb(d * lo16(freqs[k])) * 0.15915494309189535f;
    r -= __builtin_rintf(r);
    const float b = rb(__builtin_amdgcn_sinf(r) * inv);
    w0 += b * lo16(wr[k * 192 + c]); w1 += b * lo16(wr[k * 192 + 64 + c]); w2 += b * lo16(wr[k * 192 + 128 + c]);
  }
  const float ms = phi[s * 192 + c] * w0 * f, mv = phi[s * 192 + 64 + c] * w1 * f, mr = phi[s * 192 + 128 + c] * w2 * f;
  float* a = acc + t * 256 + c;
  __hip_atomic_fetch_add(a, ms, __ATOMIC_RELAXED, __HIP_MEMORY_SCOPE_AGENT);
  __hip_atomic_fetch_add(a + 64, mv * lo16(sv[s * 256 + 64 + c]) + mr * rb(x * inv), __ATOMIC_RELAXED, __HIP_MEMORY_SCOPE_AGENT);
  __hip_atomic_fetch_add(a + 128, mv * lo16(sv[s * 256 + 128 + c]) + mr * rb(y * inv), __ATOMIC_RELAXED, __HIP_MEMORY_SCOPE_AGENT);
  __hip_atomic_fetch_add(a + 192, mv * lo16(sv[s * 256 + 192 + c]) + mr * rb(z * inv), __ATOMIC_RELAXED, __HIP_MEMORY_SCOPE_AGENT);
}
extern "C" __global__ __launch_bounds__(256) void k_gen_out(const u16* __restrict__ sv, const float* __restrict__ acc, u16* __restrict__ out) {
  const size_t i = (size_t)BID * 256 + TID;
  out[i] = (u16)pk2(lo16(sv[i]) + acc[i], 0.f);
}

#if defined(__HIP_DEVICE_COMPILE__)
#pragma clang attribute pop
#endif
extern "C" void kernel_launch(void* const* d_in, const int* in_sizes, int n_in, void* d_out, int out_size, void* d_ws, size_t ws_size,
                              hipStream_t stream) {
  const u16* sv = (const u16*)d_in[0];
  const u16* pos = (const u16*)d_in[1];
  const int* src = (const int*)d_in[2];
  const int* dst = (const int*)d_in[3];
  const u16* fr = (const u16*)d_in[4];
  const u16* w1 = (const u16*)d_in[5];
  const u16* w2 = (const u16*)d_in[6];
  const u16* wr = (const u16*)d_in[7];
  const int N = in_sizes[1] / 3, E = in_sizes[2];
  char* ws = (char*)d_ws;
  if (N <= 0) return;
  if (N > 51200 || E > 256 * CH) {
    float* acc = (float*)ws;
    float* phi = acc + (size_t)N * 256;
    (void)hipMemsetAsync(acc, 0, (size_t)N * 1024, stream);
    k_gen_phi<<<N, 192, 0, stream>>>(sv, w1, w2, phi);
    if (E > 0) k_gen_edge<<<(E + 3) / 4, 256, 0, stream>>>(sv, pos, src, dst, fr, wr, phi, acc, E);
    k_gen_out<<<N, 256, 0, stream>>>(sv, acc, (u16*)d_out);
    return;
  }
  const int BN = (N + 255) / 256 < 2 ? 2 : (N + 255) / 256;
  const int B = (N + BN - 1) / BN, bpx = (B + 7) / 8;
  int chunk = (((E + 195) / 196) + 3) & ~3;
  chunk = chunk < 1024 ? 1024 : chunk > CH ? CH : chunk;
  const int W = (E + chunk - 1) / chunk;
  const int HW = 200, WP = (W + 7) & ~7;
  const unsigned magicBN = (unsigned)(0x100000000ull / (unsigned)BN) + 1u;
  (void)hipFuncSetAttribute((const void*)k_agg, hipFuncAttributeMaxDynamicSharedMemorySize, L_END);
  k_bin<<<WP + HW + 8, 512, 0, stream>>>(src, dst, sv, pos, w1, ws, (unsigned)N | (unsigned)BN << 16 | (unsigned)bpx << 24, E, magicBN,
                                         (unsigned)chunk | (unsigned)W << 16, w2, wr, fr);
  k_agg<<<8 * bpx, 512, L_END, stream>>>(sv, ws, (u16*)d_out, N, W, BN, B, bpx);
}
```

```cpp
#if defined(__HIP_DEVICE_COMPILE__)
#pragma clang attribute push(__attribute__((target("no-packed-fp32-ops"))), apply_to = function)
#endif
#include <hip/hip_runtime.h>
#include <stdint.h>
#include <type_traits>

#define TID __builtin_amdgcn_workitem_id_x()
#define BID __builtin_amdgcn_workgroup_id_x()
typedef unsigned short u16;
typedef __attribute__((ext_vector_type(8))) __bf16 bf16x8;
typedef __attribute__((ext_vector_type(2))) __bf16 bf16x2;
typedef __attribute__((ext_vector_type(4))) short s16x4;
typedef __attribute__((ext_vector_type(2))) float f32x2;
typedef __attribute__((ext_vector_type(4))) float f32x4;
typedef __attribute__((ext_vector_type(16))) float f32x16;
typedef __attribute__((ext_vector_type(4))) unsigned u32x4;
typedef __attribute__((ext_vector_type(3))) unsigned u32x3;
typedef __attribute__((ext_vector_type(2))) unsigned u32x2;
#define LDSP(T, off) ((__attribute__((address_space(3))) T*)(uintptr_t)(off))
#define DI __device__ __forceinline__
#define SB() __builtin_amdgcn_sched_barrier(0)

constexpr int WT = 17;
constexpr int CH = 4096;
constexpr int SEGQ = CH / 4 + 192;
constexpr int GN = 25;
constexpr int NFRAG = 30;
constexpr int SUBC = 64;
constexpr int CAP = 128;
constexpr int BQ = 256;
constexpr unsigned SENT = 0xFFFFFFFFu;
constexpr int L_DPOS = 0;
constexpr int L_CNT = 4096;
constexpr int L_NB = L_CNT + 256;
constexpr int L_RAW = L_NB + 64;
constexpr int L_LIST = L_RAW + 1024;
constexpr int O_D = 640, O_DL = O_D + 256, O_F = O_DL + 256, O_FX = O_F + 256;
constexpr int LISTS = 1024 + O_FX + 768;
constexpr int L_W = L_RAW + 8 * LISTS;
constexpr int L_FR = L_W + NFRAG * 1024;
constexpr int L_TB = (L_FR + 64 + 511) / 512 * 512;
constexpr int TBS = GN * 512;
constexpr int L_END = L_TB + 8 * TBS;
static_assert(L_END <= 163840 && L_TB < 65536 && L_W % 16 == 0 && LISTS % 16 == 0 && SUBC == 64, "LDS");
constexpr size_t WS_T = 65536;
constexpr size_t WS_SEG = WS_T + 256 * 256 * 4;
constexpr size_t WS_P = WS_SEG + (size_t)256 * SEGQ * 16;
constexpr size_t WS_C = WS_P + 51200 * 8;
constexpr size_t WS_H = WS_C + 51200 * 2;
constexpr size_t WS_END = WS_H + 51200 * 128;
constexpr size_t WS_F = NFRAG * 1024;
static_assert(WS_END < 0xffffff, "ws");

DI float lo16(unsigned x) { return __uint_as_float(x << 16); }
DI float hi16(unsigned x) { return __uint_as_float(x & 0xffff0000u); }
DI unsigned pk2(float a, float b) { f32x2 v = {a, b}; return __builtin_bit_cast(unsigned, __builtin_convertvector(v, bf16x2)); }
DI float rb(float a) { return hi16(pk2(a, a)); }
DI f32x16 mfma(u32x4 a, u32x4 b, f32x16 c) {
  return __builtin_amdgcn_mfma_f32_32x32x16_bf16(__builtin_bit_cast(bf16x8, a), __builtin_bit_cast(bf16x8, b), c, 0, 0, 0);
}
DI f32x16 zero16() { f32x16 z;
#pragma unroll
  for (int r = 0; r < 16; ++r) z[r] = 0.f;
  return z; }
DI int wave_incl_scan(int v) {
  v += __builtin_amdgcn_update_dpp(0, v, 0x111, 0xf, 0xf, false);
  v += __builtin_amdgcn_update_dpp(0, v, 0x112, 0xf, 0xf, false);
  v += __builtin_amdgcn_update_dpp(0, v, 0x114, 0xf, 0xf, false);
  v += __builtin_amdgcn_update_dpp(0, v, 0x118, 0xf, 0xf, false);
  v += __builtin_amdgcn_update_dpp(0, v, 0x142, 0xa, 0xf, false);
  v += __builtin_amdgcn_update_dpp(0, v, 0x143, 0xc, 0xf, false);
  return v;
}
DI int chan_of(int t2, int i32) { return 32 * ((i32 >> 2) & 1) + 16 * t2 + 4 * (i32 >> 3) + (i32 & 3); }
DI u32x4 pack8(const f32x16& x, int s) {
  return u32x4{pk2(x[8 * s], x[8 * s + 1]), pk2(x[8 * s + 2], x[8 * s + 3]), pk2(x[8 * s + 4], x[8 * s + 5]), pk2(x[8 * s + 6], x[8 * s + 7])};
}
DI void setprio(int p) {
  if (p >= 3) __builtin_amdgcn_s_setprio(3);
  else if (p == 2) __builtin_amdgcn_s_setprio(2);
  else if (p == 1) __builtin_amdgcn_s_setprio(1);
  else __builtin_amdgcn_s_setprio(0);
}
DI u32x2 trread(unsigned a) { return __builtin_bit_cast(u32x2, __builtin_amdgcn_ds_read_tr16_b64_v4i16(LDSP(s16x4, a))); }
DI void st16(void* base, unsigned off, u32x4 v) {
  const __amdgpu_buffer_rsrc_t r = __builtin_amdgcn_make_buffer_rsrc(base, 0, 0x7fffffff, 0x00020000);
  __builtin_amdgcn_raw_buffer_store_b128(v, r, off, 0, WT);
}
DI unsigned qmod(float x) { return min((unsigned)(__builtin_amdgcn_fractf(x * 0.0625f) * 65536.0f), 65535u); }
DI unsigned opq(unsigned c) { asm volatile("" : "+v"(c)); return c; }
DI unsigned swap23(unsigned p) { return (p & ~12u) | ((p & 4u) << 1) | ((p & 8u) >> 1); }
DI void wg_barrier() { asm volatile("s_waitcnt lgkmcnt(0)\n\ts_barrier" ::: "memory"); }

DI void dma16(u32x4 rsrc, unsigned voff, unsigned ldsaddr) {
  asm volatile("s_mov_b32 m0, %2\n\ts_nop 3\n\tbuffer_load_dwordx4 %0, %1, 0 offen lds" ::"v"(voff), "s"(rsrc), "s"(ldsaddr) : "memory");
}
DI void dma16x4(u32x4 rsrc, const unsigned (&voff)[4], unsigned soff, unsigned m0) {
  asm volatile("s_mov_b32 m0, %6\n\ts_nop 3\n\tbuffer_load_dwordx4 %0, %4, %5 offen lds\n\t"
               "s_add_u32 m0, m0, 0x400\n\ts_nop 0\n\tbuffer_load_dwordx4 %1, %4, %5 offen lds\n\t"
               "s_add_u32 m0, m0, 0x400\n\ts_nop 0\n\tbuffer_load_dwordx4 %2, %4, %5 offen lds\n\t"
               "s_add_u32 m0, m0, 0x400\n\ts_nop 0\n\tbuffer_load_dwordx4 %3, %4, %5 offen lds"
               ::"v"(voff[0]), "v"(voff[1]), "v"(voff[2]), "v"(voff[3]), "s"(rsrc), "s"(soff), "s"(m0) : "memory", "scc");
}
constexpr int K1_CNT = 0, K1_STAGE = 8 * 256 * 4, K1_TOT = K1_STAGE + SEGQ * 16, K1_W1 = 0, K1_HT = 8192, K1_END = K1_HT + 8 * 4096;
#if defined(__HIP_DEVICE_COMPILE__)
#pragma clang attribute pop
#endif
extern "C" __global__ __launch_bounds__(512, 4) void k_bin(const int* __restrict__ src, const int* __restrict__ dst, const u16* __restrict__ sv,
                                                           const u16* __restrict__ pos, const u16* __restrict__ w1, char* __restrict__ ws,
                                                           unsigned shapeN, int E, unsigned magicBN, unsigned shapeE, const u16* __restrict__ w2,
                                                           const u16* __restrict__ wr, const u16* __restrict__ freqs) {
  asm volatile("" ::"s"(src), "s"(dst), "s"(sv), "s"(pos), "s"(w1), "s"(ws), "s"(shapeN), "s"(E), "s"(magicBN), "s"(shapeE));
  const int N = shapeN & 0xffff, BN = (shapeN >> 16) & 0xff, NPX = (shapeN >> 24) * BN, chunk = shapeE & 0xffff, W = shapeE >> 16, WP = (W + 7) & ~7;
  constexpr int HW = 200;
  unsigned* T = (unsigned*)(ws + WS_T);
  u32x4* seg = (u32x4*)(ws + WS_SEG);
  u32x4* wf = (u32x4*)ws;
  u32x2* ptab = (u32x2*)(ws + WS_P);
  u16* ctab = (u16*)(ws + WS_C);
  char* htab = ws + WS_H;
  __shared__ __attribute__((aligned(16))) char lds_raw[K1_END + 256];
  const unsigned L0 = (unsigned)(uintptr_t)(__attribute__((address_space(3))) char*)lds_raw;
  const int tid = TID, lane = tid & 63;
  const int wave = __builtin_amdgcn_readfirstlane(tid >> 6);
  int bid = BID;
  if (bid < WP + HW) { if (bid < WP) { if (bid >= W) return; bid += HW; } else bid -= WP; } else bid += W - WP;
  if (wave == 0) {
    uint64_t pc; unsigned len;
    asm volatile("s_getpc_b64 %0\npainn_self_lo:\n\ts_mov_b32 %1, painn_code_hi-painn_self_lo" : "=s"(pc), "=s"(len));
    const u32x4 rc = {(unsigned)pc, (unsigned)(pc >> 32) & 0xffffu, len, 0x00020000u};
    asm volatile("s_mov_b32 m0, %2\n\ts_nop 3\n\tbuffer_load_dword %0, %1, 0 offen lds" ::"v"(lane * 128), "s"(rc), "s"(L0 + K1_END) : "memory");
  }
  if (bid >= W + HW) {
    {
      uint64_t pc; unsigned len;
      asm volatile("s_getpc_b64 %0\npainn_code_lo:\n\ts_mov_b32 %1, painn_code_hi-painn_code_lo" : "=s"(pc), "=s"(len));
      const __amdgpu_buffer_rsrc_t rc = __builtin_amdgcn_make_buffer_rsrc((void*)pc, 0, len, 0x00020000);
      const unsigned x = __builtin_amdgcn_raw_buffer_load_b32(rc, tid * 64, 0, 0);
      asm volatile("" ::"v"(x));
    }
    if (wave == 1) {
      uint64_t pc; unsigned d;
      asm volatile("s_getpc_b64 %0\npainn_kd_ref:\n\ts_mov_b32 %1, k_agg.kd@rel32@lo+4" : "=s"(pc), "=s"(d));
      const unsigned x = *(const volatile unsigned*)(pc + (int64_t)(int)d + (lane & 15) * 4);
      asm volatile("" ::"v"(x));
    }
    const int idx = (bid - W - HW) * 512 + tid;
    if (idx >= NFRAG * 64) {
      if (idx < NFRAG * 64 + 16) ((unsigned*)wf)[WS_F / 4 + idx - NFRAG * 64] = (unsigned)freqs[idx - NFRAG * 64] << 16;
      return;
    }
    const int f = idx >> 6, i32 = lane & 31, h = lane >> 5;
    const u16* p; int s2;
    if (f < 24) { const int ct = f >> 2; p = w2 + (16 * (f & 3) + 4 * h) * 192 + 64 * (ct >> 1) + chan_of(ct & 1, i32); s2 = 8 * 192; }
    else { const int ct = f - 24; p = wr + 8 * h * 192 + 64 * (ct >> 1) + chan_of(ct & 1, i32); s2 = 4 * 192; }
    unsigned v[8];
#pragma unroll
    for (int j = 0; j < 8; ++j) v[j] = p[(j & 3) * 192 + (j >> 2) * s2];
    wf[idx] = u32x4{v[0] | v[1] << 16, v[2] | v[3] << 16, v[4] | v[5] << 16, v[6] | v[7] << 16};
    return;
  }
  if (bid < HW) {
    const int l31 = lane & 31, h = lane >> 5;
    const int nl = (wave * (HW >> 3) + (bid >> 3)) * 32, nend = min(N, ((bid & 7) + 1) * NPX);
    const int n0 = nl < NPX ? (bid & 7) * NPX + nl : N;
    const int nn = min(n0 + l31, N - 1);
    const unsigned HT = L0 + K1_HT + wave * 4096;
    {
      const uint64_t sp = (uint64_t)sv, wp = (uint64_t)w1;
      const u32x4 rs_sv = {(unsigned)sp, (unsigned)(sp >> 32), (unsigned)N * 512u, 0x00020000u};
      const u32x4 rs_w1 = {(unsigned)wp, (unsigned)(wp >> 32), 8192u, 0x00020000u};
      const unsigned o = (unsigned)(n0 + (lane >> 3)) * 512u + 16u * ((lane & 7) ^ (lane >> 3));
      const unsigned v4[4] = {o, o + 4096u, o + 8192u, o + 12288u};
      dma16x4(rs_sv, v4, 0, HT);
      dma16(rs_w1, tid * 16u, L0 + K1_W1 + wave * 1024);
    }
    unsigned px = pos[nn * 3], py = pos[nn * 3 + 1], pz = pos[nn * 3 + 2];
    asm volatile("s_waitcnt vmcnt(0)\n\ts_barrier" : "+v"(px), "+v"(py), "+v"(pz)::"memory");
    if (n0 >= N) return;
    {
      const uint64_t sp = (uint64_t)sv;
      const u32x4 rs_sv = {(unsigned)sp, (unsigned)(sp >> 32), (unsigned)N * 512u, 0x00020000u};
      asm volatile("s_mov_b32 m0, %2\n\ts_nop 3\n\tbuffer_load_dword %0, %1, 0 offen lds"
                   ::"v"(h == 0 ? (unsigned)(n0 + l31) * 512u + 128u : 0x7fffffffu), "s"(rs_sv), "s"(L0 + K1_END) : "memory");
    }
    u32x4 sf[4];
#pragma unroll
    for (int ks = 0; ks < 4; ++ks) sf[ks] = *LDSP(u32x4, HT + l31 * 128 + 16 * ((4 * h + ks) ^ (l31 & 7)));
    const unsigned ta = L0 + K1_W1 + (32 * h + ((lane >> 2) & 3)) * 128 + (16 * ((lane >> 4) & 1) + 4 * (lane & 3)) * 2;
    f32x16 d1[2];
#pragma unroll
    for (int ht = 0; ht < 2; ++ht) {
      d1[ht] = zero16();
#pragma unroll
      for (int ks = 0; ks < 4; ++ks) {
        const u32x2 a0 = trread(ta + ks * 1024 + ht * 64), a1 = trread(ta + ks * 1024 + 512 + ht * 64);
        d1[ht] = mfma(u32x4{a0[0], a0[1], a1[0], a1[1]}, sf[ks], d1[ht]);
      }
    }
#pragma unroll
    for (int ht = 0; ht < 2; ++ht) {
      f32x16 e = d1[ht] * -1.4426950408889634f;
#pragma unroll
      for (int r = 0; r < 16; ++r) e[r] = __builtin_amdgcn_exp2f(e[r]);
      e += 1.0f;
#pragma unroll
      for (int r = 0; r < 16; ++r) e[r] = __builtin_amdgcn_rcpf(e[r]);
      e *= d1[ht];
#pragma unroll
      for (int s = 0; s < 2; ++s) *LDSP(u32x4, HT + l31 * 128 + 16 * ((4 * h + 2 * ht + s) ^ (l31 & 7))) = pack8(e, s);
    }
#pragma unroll
    for (int i = 0; i < 4; ++i) {
      const int n = 8 * i + (lane >> 3);
      const u32x4 v = *LDSP(u32x4, HT + i * 1024 + lane * 16);
      if (n0 + n < nend) st16(htab, (n0 + n) * 128 + 16 * ((lane & 7) ^ (n & 7)), v);
    }
    if (h == 0 && n0 + l31 < nend) {
      ptab[n0 + l31] = u32x2{px | py << 16, pz << 16};
      ctab[n0 + l31] = (u16)(qmod(lo16(px)) >> 11 | (qmod(lo16(py)) >> 11) << 5 | (qmod(lo16(pz)) & 0xfc00u));
    }
    return;
  }
  const int cw = bid - HW;
  const int eend = min(E, (cw + 1) * chunk);
  const __amdgpu_buffer_rsrc_t rb_s = __builtin_amdgcn_make_buffer_rsrc((void*)src, 0, eend * 4, 0x00020000);
  const __amdgpu_buffer_rsrc_t rb_d = __builtin_amdgcn_make_buffer_rsrc((void*)dst, 0, eend * 4, 0x00020000);
  const int e0 = cw * chunk + tid * 4;
  u32x4 s4[2], d4[2];
#pragma unroll
  for (int u = 0; u < 2; ++u) d4[u] = __builtin_amdgcn_raw_buffer_load_b128(rb_d, (e0 + u * 2048) * 4, 0, 0);
#pragma unroll
  for (int u = 0; u < 2; ++u) s4[u] = __builtin_amdgcn_raw_buffer_load_b128(rb_s, (e0 + u * 2048) * 4, 0, 0);
  *LDSP(u32x4, L0 + K1_CNT + wave * 1024 + lane * 16) = u32x4{0, 0, 0, 0};
#pragma unroll
  for (int i = 0; i < 3; ++i) if (i * 512 + tid < SEGQ) *LDSP(u32x4, L0 + K1_STAGE + (i * 512 + tid) * 16) = u32x4{0, 0, 0, 0};
  unsigned ca[8], hi8[8], rank[8];
#pragma unroll
  for (int k = 0; k < 8; ++k) {
    const unsigned d = d4[k >> 2][k & 3];
    const unsigned b = (unsigned)(((uint64_t)d * magicBN) >> 32);
    ca[k] = L0 + K1_CNT + wave * 1024 + b * 4;
    rank[k] = SENT;
    if (e0 + (k >> 2) * 2048 + (k & 3) < eend) rank[k] = __hip_atomic_fetch_add(LDSP(unsigned, ca[k]), 1u, __ATOMIC_RELAXED, __HIP_MEMORY_SCOPE_WORKGROUP);
    const unsigned dl = d - b * (unsigned)BN;
    hi8[k] = dl + ((dl * 41u) >> 10) * 7u + 1u;
  }
  wg_barrier();
  if (wave == 0) {
    u32x4 c[8], tot = {0, 0, 0, 0};
#pragma unroll
    for (int w = 0; w < 8; ++w) { c[w] = *LDSP(u32x4, L0 + K1_CNT + w * 1024 + lane * 16); tot += c[w]; }
    const u32x4 q = (tot + 3u) >> 2;
    const unsigned ls = q[0] + q[1] + q[2] + q[3];
    const unsigned incl = wave_incl_scan(ls);
    u32x4 st; st[0] = incl - ls; st[1] = st[0] + q[0]; st[2] = st[1] + q[1]; st[3] = st[2] + q[2];
    u32x4 run = st * 4u;
#pragma unroll
    for (int w = 0; w < 8; ++w) { *LDSP(u32x4, L0 + K1_CNT + w * 1024 + lane * 16) = run; run += c[w]; }
    if (lane == 63) *LDSP(unsigned, L0 + K1_TOT) = incl;
    st16(T, cw * 1024 + 16 * lane, st | (q << 16));
  }
  wg_barrier();
  unsigned base[8];
#pragma unroll
  for (int k = 0; k < 8; ++k) base[k] = *LDSP(unsigned, ca[k]);
#pragma unroll
  for (int k = 0; k < 8; ++k)
    if (rank[k] != SENT) *LDSP(unsigned, L0 + K1_STAGE + (base[k] + rank[k]) * 4) = s4[k >> 2][k & 3] << 3 | hi8[k] << 24;
  const int total = *LDSP(int, L0 + K1_TOT);
  wg_barrier();
#pragma unroll
  for (int i = 0; i < 3; ++i) {
    const int qd = i * 512 + tid;
    if (qd < total) st16(seg, (cw * SEGQ + qd) * 16, *LDSP(u32x4, L0 + K1_STAGE + qd * 16));
  }
}

#if defined(__HIP_DEVICE_COMPILE__)
#pragma clang attribute push(__attribute__((target("no-packed-fp32-ops"))), apply_to = function)
#endif
DI u32x4 eqmask4(u32x4 a, unsigned b, unsigned ones) {
  u32x4 x;
#pragma unroll
  for (int i = 0; i < 4; ++i) x[i] = a[i] ^ b;
  asm("v_pk_min_u16 %0, %0, %4\n\tv_pk_min_u16 %1, %1, %4\n\tv_pk_min_u16 %2, %2, %4\n\tv_pk_min_u16 %3, %3, %4\n\t"
      "v_pk_sub_u16 %0, %0, %4\n\tv_pk_sub_u16 %1, %1, %4\n\tv_pk_sub_u16 %2, %2, %4\n\tv_pk_sub_u16 %3, %3, %4"
      : "+v"(x[0]), "+v"(x[1]), "+v"(x[2]), "+v"(x[3]) : "s"(ones));
  return x;
}

typedef __attribute__((ext_vector_type(2))) short s16x2;
extern "C" __global__ __launch_bounds__(512, 2) void k_agg(const u16* __restrict__ sv, const char* __restrict__ wf, u16* __restrict__ out, int N,
                                                           int W, int BN, int B, int bpx) {
  asm volatile("" ::"s"(sv), "s"(wf), "s"(out), "s"(N), "s"(W), "s"(BN), "s"(B), "s"(bpx));
  const int wave = __builtin_amdgcn_readfirstlane(TID >> 6);
  const int b = ((int)BID & 7) * bpx + ((int)BID >> 3);
  if (b >= B) return;
  constexpr unsigned L0 = 0;
  const int node0 = b * BN;
  const int nb = min(BN, N - node0);
  const uint64_t svp = (uint64_t)sv, wfp = (uint64_t)wf;
  const u32x4 rs_sv = {(unsigned)svp, (unsigned)(svp >> 32), (unsigned)N * 512u, 0x00020000u};
  const u32x4 rs_ws = {(unsigned)wfp, (unsigned)(wfp >> 32), (unsigned)WS_END, 0x00020000u};
#define FRESH_LANE int lane; asm volatile("v_mbcnt_lo_u32_b32 %0, -1, 0\n\tv_mbcnt_hi_u32_b32 %0, -1, %0" : "=v"(lane)); const int l31 = lane & 31, h = lane >> 5; (void)l31; (void)h
  const unsigned TB = L0 + L_TB + wave * TBS;

  f32x16 acc[4][2];
  u32x4 hN[4];
  int batch = 0, r1 = 0, r2 = 0;
  bool first = true;
  const int grp = wave;
  while (true) {
    u32x4 ol[13];
    {
      FRESH_LANE;
      const unsigned lo = 16u * lane;
      const unsigned v4[4] = {lo, lo + 1024u, lo + 2048u, lo + 3072u};
      const unsigned oo = first ? (unsigned)(node0 + wave * GN + h) * 512u + 16u * (l31 ^ h) : 0x7fff0000u;
#define OWNA(i) ((oo ^ (32u * ((i) & 7))) + 1024u * (i))
#define OWN4(j) asm volatile("s_nop 4\n\tbuffer_load_dwordx4 %0, %4, %8, 0 offen\n\tbuffer_load_dwordx4 %1, %5, %8, 0 offen\n\t" \
                             "buffer_load_dwordx4 %2, %6, %8, 0 offen\n\tbuffer_load_dwordx4 %3, %7, %8, 0 offen" \
                             : "=&v"(ol[4 * j]), "=&v"(ol[4 * j + 1]), "=&v"(ol[4 * j + 2]), "=&v"(ol[4 * j + 3]) \
                             : "v"(OWNA(4 * j)), "v"(OWNA(4 * j + 1)), "v"(OWNA(4 * j + 2)), "v"(OWNA(4 * j + 3)), "s"(rs_sv) : "memory")
#define OWN1() asm volatile("s_nop 4\n\tbuffer_load_dwordx4 %0, %1, %2, 0 offen" : "=&v"(ol[12]) : "v"(h ? 0x7fff0000u : OWNA(12)), "s"(rs_sv) : "memory")
      if (wave >= 4) {
        const int rw = wave - 4;
        const unsigned MAP = L_LIST + wave * LISTS + 1024;
        const int cpw = (W + 3) >> 2;
        const int cell = rw * cpw + lane;
        unsigned t;
        u32x2 dp;
        asm volatile("s_nop 4\n\tbuffer_load_dword %0, %2, %4, 0 offen\n\tbuffer_load_dwordx2 %1, %3, %4, 0 offen\n\ts_barrier\n\ts_waitcnt vmcnt(0)"
                     : "=&v"(t), "=&v"(dp) : "v"((lane < cpw && cell < W) ? (int)WS_T + (cell * 256 + b) * 4 : 0x7fffffff),
                       "v"((int)WS_P + (node0 + (2 * rw + h) * GN + l31 - 1) * 8), "s"(rs_ws) : "memory");
        const int q = t >> 16;
        const int incl = wave_incl_scan(q), excl = incl - q;
        const int total = __builtin_amdgcn_readlane(incl, 63);
        const int win0 = batch * BQ;
        const int gq = (int)(WS_SEG / 16) + cell * SEGQ + (int)(t & 0xffffu) - excl;
        const int hi = min(incl, win0 + BQ);
#pragma nounroll
        for (int i = max(excl, win0); i < hi; ++i) *LDSP(int, MAP + (i - win0) * 4) = (gq + i) * 16;
        unsigned mq[4];
#pragma unroll
        for (int s = 0; s < 4; ++s) mq[s] = *LDSP(int, MAP + (64 * s + lane) * 4);
#pragma unroll
        for (int s = 0; s < 4; ++s) if (win0 + 64 * s + lane >= total) mq[s] = 0x7fffffffu;
        dma16x4(rs_ws, mq, 0, L_W + rw * 4096);
        if (lane == 0) *LDSP(int, L_NB + rw * 4) = (total + BQ - 1) / BQ;
        const unsigned qx = (qmod(lo16(dp[0])) - 1024u) & 0xffffu, qy = qmod(hi16(dp[0])) - 1024u, qz = (qmod(__uint_as_float(dp[1])) - 512u) & 0xffffu;
        *LDSP(u32x4, L_DPOS + rw * 1024 + lo) = (unsigned)(l31 - 1) < (unsigned)GN ? u32x4{dp[0], dp[1], qx | qy << 16, qz} : u32x4{0x7fc07fc0u, 0x7fc00000u, 0u, 0x7fff0000u};
      } else {
        __builtin_amdgcn_s_barrier();
#pragma unroll
        for (int i = 0; i < 6; ++i) dma16x4(rs_ws, v4, WS_C + (wave * 6 + i) * 4096, L_TB + (wave * 6 + i) * 4096);
        dma16(rs_ws, WS_C + (96 + wave) * 1024u + lo, L_TB + (96 + wave) * 1024);
      }
      if (lane < 8) *LDSP(unsigned, L_CNT + lane * 32 + wave * 4) = lane >> 3;
      { const unsigned z = opq(0u); *LDSP(u32x4, L_RAW + wave * LISTS + lo) = u32x4{z, z, z, z}; *LDSP(u32x4, L_LIST + wave * LISTS + lo) = u32x4{z, z, z, z}; }
      OWN4(0); OWN1();
      asm volatile("s_waitcnt vmcnt(5)" ::: "memory");
      wg_barrier();
      u32x4 r4[2];
#pragma unroll
      for (int s = 0; s < 2; ++s) r4[s] = *LDSP(u32x4, L_W + (2 * s + h) * 4096 + (l31 >> 2) * 512 + wave * 64 + (lane & 3) * 16);
      OWN4(1);
      const unsigned one = opq(1u);
#pragma unroll
      for (int s = 0; s < 2; ++s) {
        unsigned cd[4];
        u32x2 dq[4];
#pragma unroll
        for (int c = 0; c < 4; ++c) {
          cd[c] = *LDSP(u16, L_TB + ((r4[s][c] >> 2) & 0x3fffffu));
          dq[c] = *LDSP(u32x2, L_DPOS + 8 + ((r4[s][c] >> 20) & 0xff0u));
        }
        unsigned rank[4];
#pragma unroll
        for (int c = 0; c < 4; ++c) {
          const unsigned cxy = ((cd[c] & 0x3ffu) * 0x400800u) & 0xf800f800u, cz = cd[c] & 0xfc00u;
          const unsigned qxy = dq[c][0], qz = dq[c][1];
          const s16x2 dxy = __builtin_bit_cast(s16x2, qxy) - __builtin_bit_cast(s16x2, cxy);
          const s16x2 dz = __builtin_bit_cast(s16x2, qz) - __builtin_bit_cast(s16x2, cz);
          const unsigned d2 = __builtin_amdgcn_sdot2(dxy, dxy, __builtin_amdgcn_sdot2(dz, dz, 0, false), false);
          rank[c] = 0x7fffffffu;
          if (d2 < 488400000u)
            rank[c] = __hip_atomic_fetch_add(LDSP(unsigned, L_CNT + (((r4[s][c] >> 24) & 0xe0u) | wave * 4)), one, __ATOMIC_RELAXED, __HIP_MEMORY_SCOPE_WORKGROUP);
        }
        if (s == 0) OWN4(2);
#pragma unroll
        for (int c = 0; c < 4; ++c) {
          const unsigned p = rank[c] - SUBC * r1;
          if (p < (unsigned)SUBC) *LDSP(unsigned, L_RAW + (r4[s][c] >> 29) * LISTS + (p * 32 + wave * 4)) = r4[s][c];
        }
      }
    }
    wg_barrier();
    bool more1, moreb;
    FRESH_LANE;
    const unsigned LIST = L0 + L_LIST + grp * LISTS;
    int nact = 0, nstep;
    bool lighter;
    unsigned exy[4], ezd[4], ep[4];
    float ef[4];
    u32x4 own[16];
    auto weights = [&](unsigned xy, unsigned zd, float f, unsigned p) {
      const float inv = __builtin_amdgcn_rcpf(hi16(zd));
      const unsigned hxy = pk2(lo16(xy) * inv, hi16(xy) * inv);
      const float hz = rb(lo16(zd) * inv);
      const unsigned fxy = pk2(f * lo16(hxy), f * hi16(hxy));
      const unsigned fzf = pk2(f * hz, f);
      if (p != SENT) {
        const unsigned a2 = LIST + p * 2;
        *LDSP(u16, a2 + O_F) = (u16)(fzf >> 16);
        *LDSP(u16, a2 + O_FX) = (u16)fxy;
        *LDSP(u16, a2 + O_FX + 256) = (u16)(fxy >> 16);
        *LDSP(u16, a2 + O_FX + 512) = (u16)fzf;
      }
    };
    {
      unsigned rec[4], rex[4];
      u32x2 ps[4], pd[4];
#pragma unroll
      for (int i = 0; i < 4; ++i) rec[i] = *LDSP(unsigned, L_RAW + grp * LISTS + lane * 4 + i * 256);
#pragma unroll
      for (int i = 0; i < 4; ++i) rex[i] = *LDSP(unsigned, L_RAW + grp * LISTS + lane * 4 + 1024 + i * 256);
      {
        asm volatile("s_nop 4\n\tbuffer_load_dwordx2 %0, %4, %8, %9 offen\n\tbuffer_load_dwordx2 %1, %5, %8, %9 offen\n\t"
                     "buffer_load_dwordx2 %2, %6, %8, %9 offen\n\tbuffer_load_dwordx2 %3, %7, %8, %9 offen"
                     : "=&v"(ps[0]), "=&v"(ps[1]), "=&v"(ps[2]), "=&v"(ps[3])
                     : "v"(rec[0] & 0xffffffu), "v"(rec[1] & 0xffffffu), "v"(rec[2] & 0xffffffu), "v"(rec[3] & 0xffffffu), "s"(rs_ws), "s"((unsigned)WS_P) : "memory");
      }
#pragma unroll
      for (int i = 0; i < 4; ++i) pd[i] = *LDSP(u32x2, L0 + L_DPOS + ((rec[i] >> 20) & 0xff0u));
      nstep = __builtin_amdgcn_ballot_w64(rec[3] != 0) ? 4 : __builtin_amdgcn_ballot_w64(rec[2] != 0) ? 3 : 2;
      {
        const u32x4 c0 = *LDSP(u32x4, L_CNT + (lane & 7) * 32), c1 = *LDSP(u32x4, L_CNT + (lane & 7) * 32 + 16);
        const int nbv = *LDSP(int, L_NB + (lane & 3) * 4);
        const unsigned mx = max(max(max(c0[0], c0[1]), max(c0[2], c0[3])), max(max(c1[0], c1[1]), max(c1[2], c1[3])));
        more1 = __builtin_amdgcn_ballot_w64(mx > (unsigned)(SUBC * (r1 + 1))) != 0;
        moreb = __builtin_amdgcn_ballot_w64(nbv > batch + 1) != 0;
        const int key = (c0[0] + c0[1] + c0[2] + c0[3] + c1[0] + c1[1] + c1[2] + c1[3]) * 2 + (((lane & 7) >> 2) ^ 1);
        lighter = __builtin_amdgcn_readlane(key, grp) < __builtin_amdgcn_readlane(key, grp ^ 4);
      }
#pragma unroll
      for (int i = 0; i < 2; ++i) *LDSP(unsigned, LIST + (64 * i + lane) * 4) = opq(0xffffffu);
      if (lane < 32) *LDSP(unsigned, LIST + (128 + lane) * 4) = opq(0xffffffu);
      *LDSP(unsigned, LIST + O_D + lane * 4) = 0x3f803f80u;
      *LDSP(unsigned, LIST + O_DL + lane * 4) = 0xffffffffu;
      asm volatile("s_waitcnt vmcnt(4)" : "+v"(ol[0]), "+v"(ol[1]), "+v"(ol[2]), "+v"(ol[3]), "+v"(ol[4]),
                   "+v"(ol[5]), "+v"(ol[6]), "+v"(ol[7]), "+v"(ol[8]), "+v"(ol[9]), "+v"(ol[10]), "+v"(ol[11]), "+v"(ol[12])::"memory");
#pragma unroll
      for (int i = 0; i < 12; ++i) *LDSP(u32x4, TB + i * 1024 + lane * 16) = ol[i];
      if (h == 0) *LDSP(u32x4, TB + 12 * 1024 + lane * 16) = ol[12];
      {
        const unsigned ra = TB + l31 * 512 + 16 * ((4 * h) ^ (l31 & 15));
#pragma unroll
        for (int i = 0; i < 16; ++i) own[i] = *LDSP(u32x4, (ra ^ (16 * (8 * ((i >> 2) & 1) + (i & 3)))) + 256 * (i >> 3));
      }
      asm volatile("s_waitcnt vmcnt(0)" : "+v"(ps[0]), "+v"(ps[1]), "+v"(ps[2]), "+v"(ps[3])::"memory");
      auto exact = [&](unsigned rc, u32x2 a, u32x2 c, unsigned& oxy, unsigned& ozd, float& of, unsigned& op) {
        const unsigned xy = pk2(lo16(a[0]) - lo16(c[0]), hi16(a[0]) - hi16(c[0]));
        const float z = rb(__uint_as_float(a[1]) - __uint_as_float(c[1]));
        const float x = lo16(xy), y = hi16(xy);
        const unsigned sq = pk2(x * x, y * y);
        const float q2 = rb(rb(lo16(sq) + hi16(sq) + rb(z * z)) + 1e-8f);
        const unsigned zd = pk2(z, __builtin_amdgcn_sqrtf(q2));
        const float d = hi16(zd);
        const float f = 0.5f * rb(rb(__builtin_amdgcn_cosf(rb(rb(3.141592653589793f * d) * 0.2f) * 0.15915494309189535f)) + 1.0f);
        const bool ok = (d < 5.0f) & (f != 0.0f);
        const uint64_t bm = __builtin_amdgcn_ballot_w64(ok);
        const unsigned p = __builtin_amdgcn_mbcnt_hi((unsigned)(bm >> 32), __builtin_amdgcn_mbcnt_lo((unsigned)bm, nact - CAP * r2));
        nact += __builtin_popcountll(bm);
        oxy = xy; ozd = zd; of = f; op = SENT;
        if (ok & (p < (unsigned)CAP)) {
          op = p;
          *LDSP(unsigned, LIST + swap23(p) * 4) = rc & 0xffffffu;
          *LDSP(u16, LIST + O_D + swap23(p) * 2) = (u16)(zd >> 16);
          *LDSP(u16, LIST + O_DL + p * 2) = (u16)(rc >> 24);
        }
      };
#pragma unroll
      for (int i = 0; i < 4; ++i) {
        exy[i] = 0; ezd[i] = 0; ef[i] = 0.f; ep[i] = SENT;
        if (i < nstep) exact(rec[i], ps[i], pd[i], exy[i], ezd[i], ef[i], ep[i]);
      }
#pragma nounroll
      while (__builtin_amdgcn_ballot_w64(rex[0] != 0)) {
        u32x2 a;
        asm volatile("s_nop 4\n\tbuffer_load_dwordx2 %0, %1, %2, %3 offen\n\ts_waitcnt vmcnt(0)" : "=&v"(a) : "v"(rex[0] & 0xffffffu), "s"(rs_ws), "s"((unsigned)WS_P) : "memory");
        unsigned xy, zd, p; float f;
        exact(rex[0], a, *LDSP(u32x2, L_DPOS + ((rex[0] >> 20) & 0xff0u)), xy, zd, f, p);
        weights(xy, zd, f, p);
        rex[0] = rex[1]; rex[1] = rex[2]; rex[2] = rex[3]; rex[3] = 0;
      }
    }
    const int cnt = __builtin_amdgcn_readfirstlane(max(min(nact - CAP * r2, CAP), 0));
    if (lane == 0) *LDSP(int, L_NB + 32 + wave * 4) = nact > CAP * (r2 + 1);
    const int ntile = (cnt + 31) >> 5;
    auto issue_h2 = [&](unsigned off) {
      asm volatile("s_nop 4\n\tbuffer_load_dwordx4 %0, %4, %5, %6 offen\n\tbuffer_load_dwordx4 %1, %4, %5, %6 offen offset:16\n\t"
                   "buffer_load_dwordx4 %2, %4, %5, %6 offen offset:32\n\tbuffer_load_dwordx4 %3, %4, %5, %6 offen offset:48"
                   : "=&v"(hN[0]), "=&v"(hN[1]), "=&v"(hN[2]), "=&v"(hN[3]) : "v"(off), "s"(rs_ws), "s"((unsigned)WS_H) : "memory");
    };
    auto issue_h = [&](int t) { FRESH_LANE; issue_h2(*LDSP(unsigned, LIST + (32 * t + l31) * 4) * 16u + 64u * h); };
    auto wait_h = [&]() {
      asm volatile("s_waitcnt vmcnt(12)" : "+v"(hN[0]), "+v"(hN[1]), "+v"(hN[2]), "+v"(hN[3])::"memory");
    };
    unsigned vo[4];
    auto prep_v = [&](int t) {
      FRESH_LANE;
      const int drow = lane >> 3;
      const unsigned dchunk = 16u * (((lane & 7) ^ (drow & 2)) + 8);
#pragma unroll
      for (int i = 0; i < 4; ++i) vo[i] = *LDSP(unsigned, LIST + (32 * t + 8 * i + drow) * 4) * 64u + dchunk;
    };
    auto issue_v = [&](int k) {
      asm volatile("s_waitcnt lgkmcnt(0)" ::: "memory");
      dma16x4(rs_sv, vo, 128 * k, TB + 4096 * k);
    };
    u32x4 rbf;
    auto calc_rbf = [&](int t) {
      FRESH_LANE;
      const float d = lo16(*LDSP(u16, LIST + O_D + 64 * t + 2 * l31));
      const f32x4 f0 = *LDSP(f32x4, L_FR + 32 * h), f1 = *LDSP(f32x4, L_FR + 32 * h + 16);
      const float inv = __builtin_amdgcn_rcpf(d);
#pragma unroll
      for (int i = 0; i < 4; ++i) {
        const float fa = i < 2 ? f0[2 * i] : f1[2 * i - 4], fb = i < 2 ? f0[2 * i + 1] : f1[2 * i - 3];
        const unsigned x = pk2(d * fa, d * fb);
        float ra = lo16(x) * 0.15915494309189535f, rc = hi16(x) * 0.15915494309189535f;
        ra -= __builtin_rintf(ra); rc -= __builtin_rintf(rc);
        rbf[i] = pk2(__builtin_amdgcn_sinf(ra) * inv, __builtin_amdgcn_sinf(rc) * inv);
      }
      asm volatile("" : "+v"(rbf));
    };
    setprio(3);
    issue_h(0);
    prep_v(0);
    {
      const unsigned lo = 16u * lane;
      const unsigned v4[4] = {lo, lo + 1024u, lo + 2048u, lo + 3072u};
      const int wo = min(wave * 4096, NFRAG * 1024 + 64 - 4096);
      dma16x4(rs_ws, v4, wo, L_W + wo);
      wg_barrier();
    }
    const bool late = lighter;
    if (!late) {
      issue_v(0); issue_v(1); issue_v(2);
      asm volatile("s_waitcnt vmcnt(12)\n\ts_barrier" ::: "memory");
    }
    setprio(min(ntile - 1, 2));
    SB();
#pragma unroll
    for (int i = 0; i < 4; ++i) if (i < nstep) weights(exy[i], ezd[i], ef[i], ep[i]);
    if (first) {
#pragma unroll
      for (int i = 0; i < 16; ++i)
#pragma unroll
        for (int k = 0; k < 4; ++k) {
          acc[i >> 2][(i >> 1) & 1][8 * (i & 1) + 2 * k] = lo16(own[i][k]);
          acc[i >> 2][(i >> 1) & 1][8 * (i & 1) + 2 * k + 1] = hi16(own[i][k]);
        }
      first = false;
    }
    SB();
    if (late) {
      asm volatile("s_waitcnt vmcnt(0)\n\ts_barrier" ::: "memory");
      issue_v(0); issue_v(1); issue_v(2);
    }
    const bool more2 = __builtin_amdgcn_ballot_w64(*LDSP(int, L_NB + 32 + (lane & 7) * 4) != 0) != 0;
    calc_rbf(0);
    wait_h();
    auto tile = [&](int t, auto nsc) {
      constexpr int NS = decltype(nsc)::value;
      FRESH_LANE;
      const unsigned WL = L0 + L_W + lane * 16, la = LIST + 64 * t + 16 * h;
      const unsigned mypair = (unsigned)(grp * 32 + l31 + 1) * 0x10001u;
      u32x4 Wb[5];
      auto ldW = [&](int ct) {
        Wb[4] = *LDSP(u32x4, WL + (24 + ct) * 1024);
#pragma unroll
        for (int ks = 0; ks < 4; ++ks) Wb[ks] = *LDSP(u32x4, WL + (4 * ct + ks) * 1024);
      };
      ldW(4);
      u32x4 msk[2], S1[2], sr[3][2];
#pragma unroll
      for (int s = 0; s < NS; ++s) msk[s] = *LDSP(u32x4, la + O_DL + 32 * s);
#pragma unroll
      for (int s = 0; s < NS; ++s)
#pragma unroll
        for (int k = 0; k < 3; ++k) sr[k][s] = *LDSP(u32x4, la + O_FX + 256 * k + 32 * s);
      SB();
      f32x16 ph, w;
      auto gemm = [&]() {
        w = mfma(rbf, Wb[4], zero16());
        ph = mfma(hN[0], Wb[0], zero16());
#pragma unroll
        for (int ks = 1; ks < 4; ++ks) ph = mfma(hN[ks], Wb[ks], ph);
      };
#define PIN1(a) asm volatile("" : "+v"(a))
#define PIN2(a, b) do { PIN1(a); PIN1(b); } while (0)
#define PIN3(a, b, c) asm volatile("" : "+v"(a), "+v"(b), "+v"(c))
      auto pack = [&](u32x4 (&a)[2]) {
        const f32x16 m = ph * w;
#pragma unroll
        for (int s = 0; s < NS; ++s) { a[s] = pack8(m, s); PIN1(a[s]); }
      };
#define MIX(id, lead, n, v) do { __builtin_amdgcn_sched_group_barrier(0x8, lead, id); _Pragma("unroll") for (int i_ = 0; i_ < n; ++i_) { \
        __builtin_amdgcn_sched_group_barrier(0x402, v, id); __builtin_amdgcn_sched_group_barrier(0x8, 1, id); } } while (0)
      u32x4 aa[2], ab[2];
      gemm();
#pragma unroll
      for (int s = 0; s < NS; ++s) {
        msk[s] = eqmask4(msk[s], mypair, 0x00010001u);
#pragma unroll
        for (int k = 0; k < 3; ++k) sr[k][s] &= msk[s];
      }
      MIX(0, 1, 4, 4 * NS);
      PIN2(ph, w);
      SB();
      ldW(5);
      SB();
      pack(aa);
      SB();
      gemm();
      PIN2(ph, w);
      SB();
      ldW(0);
      PIN2(aa[0], aa[NS - 1]);
      SB();
#pragma unroll
      for (int s = 0; s < NS; ++s)
#pragma unroll
        for (int k = 0; k < 3; ++k) acc[1 + k][0] = mfma(aa[s], sr[k][s], acc[1 + k][0]);
      pack(ab);
      MIX(1, 2, 3 * NS - 2, 6);
      PIN3(acc[1][0], acc[2][0], acc[3][0]);
      SB();
      gemm();
      PIN2(ph, w);
      SB();
      ldW(1);
      PIN2(ab[0], ab[NS - 1]);
      SB();
#pragma unroll
      for (int s = 0; s < NS; ++s)
#pragma unroll
        for (int k = 0; k < 3; ++k) acc[1 + k][1] = mfma(ab[s], sr[k][s], acc[1 + k][1]);
      pack(aa);
      MIX(2, 2, 3 * NS - 2, 6);
      PIN3(acc[1][1], acc[2][1], acc[3][1]);
      SB();
      unsigned tra, se, dn;
      f32x4 f0, f1;
      u32x4 rbn;
      {
        FRESH_LANE;
        const unsigned la2 = LIST + 64 * t + 16 * h, mp2 = (unsigned)(grp * 32 + l31 + 1) * 0x10001u;
#pragma unroll
        for (int s = 0; s < NS; ++s) { msk[s] = *LDSP(u32x4, la2 + O_DL + 32 * s); S1[s] = *LDSP(u32x4, la2 + O_F + 32 * s); }
        dn = *LDSP(u16, LIST + O_D + 64 * (t + 1) + 2 * l31);
        f0 = *LDSP(f32x4, L_FR + 32 * h); f1 = *LDSP(f32x4, L_FR + 32 * h + 16);
        se = *LDSP(unsigned, LIST + (32 * (t + 1) + l31) * 4);
#pragma unroll
        for (int i = 0; i < 4; ++i) vo[i] = *LDSP(unsigned, LIST + (32 * (t + 1) + 8 * i + (lane >> 3)) * 4);
        SB();
        gemm();
#pragma unroll
        for (int s = 0; s < NS; ++s) { S1[s] &= eqmask4(msk[s], mp2, 0x00010001u); PIN1(S1[s]); }
        const unsigned dchunk = 16u * (((lane & 7) ^ ((lane >> 3) & 2)) + 8);
#pragma unroll
        for (int i = 0; i < 4; ++i) { vo[i] = vo[i] * 64u + dchunk; PIN1(vo[i]); }
        se = se * 16u + 64u * h; PIN1(se);
        const int q4 = (lane >> 2) & 3, pp = lane & 3;
        tra = (q4 & 1) ? TB + (4 * h + (q4 >> 1)) * 128 + 64 * (pp & 1) + (lane & 16) + 8 * (pp >> 1) : 0x100000u;
        PIN1(tra);
        MIX(3, 2, 3, 8);
        PIN2(ph, w);
        SB();
      }
      ldW(2);
      PIN2(aa[0], aa[NS - 1]);
      SB();
#pragma unroll
      for (int s = 0; s < NS; ++s) acc[0][0] = mfma(aa[s], S1[s], acc[0][0]);
      PIN3(acc[0][0], ph, w);
      SB();
      pack(ab);
      SB();
      const float dnf = lo16(dn), inv = __builtin_amdgcn_rcpf(dnf);
      auto rbf2 = [&](float fa, float fb) {
        const unsigned x = pk2(dnf * fa, dnf * fb);
        float ra = lo16(x) * 0.15915494309189535f, rc = hi16(x) * 0.15915494309189535f;
        ra -= __builtin_rintf(ra); rc -= __builtin_rintf(rc);
        return pk2(__builtin_amdgcn_sinf(ra) * inv, __builtin_amdgcn_sinf(rc) * inv);
      };
      gemm();
      { unsigned r0 = rbf2(f0[0], f0[1]), r1_ = rbf2(f0[2], f0[3]); PIN2(r0, r1_); rbn[0] = r0; rbn[1] = r1_; }
      MIX(4, 1, 4, 6);
      PIN2(ph, w);
      SB();
      ldW(3);
      PIN2(ab[0], ab[NS - 1]);
      SB();
#pragma unroll
      for (int s = 0; s < NS; ++s) acc[0][1] = mfma(ab[s], S1[s], acc[0][1]);
      PIN3(acc[0][1], ph, w);
      SB();
      f32x16 mv[2];
      mv[0] = ph * w;
      PIN1(mv[0]);
      SB();
      gemm();
      { unsigned r0 = rbf2(f1[0], f1[1]), r1_ = rbf2(f1[2], f1[3]); PIN2(r0, r1_); rbn[2] = r0; rbn[3] = r1_; }
      MIX(5, 1, 4, 6);
      PIN2(ph, w);
      SB();
      issue_h2(se);
      rbf = rbn;
      mv[1] = ph * w;
      PIN1(mv[1]);
      SB();
      __builtin_amdgcn_s_setprio(3);
      asm volatile("s_waitcnt vmcnt(4)" ::: "memory");
      u32x2 va[8], vb[8];
      auto ldv = [&](u32x2 (&v)[8], int t2, int k) {
#pragma unroll
        for (int i = 0; i < 4 * NS; ++i)
          v[i] = trread(tra + k * 4096 + (8 * (i >> 1) + 2 * (i & 1)) * 128 + 32 * (t2 ^ (i & 1)));
      };
      auto vv = [&](const u32x2 (&v)[8], const f32x16& m, f32x16& ac) {
        u32x4 a[2];
#pragma unroll
        for (int i = 0; i < 4 * NS; ++i) {
          const unsigned v0 = v[i][0], v1 = v[i][1];
          a[i >> 2][i & 3] = pk2(m[2 * i] * __uint_as_float(v0), m[2 * i + 1] * __uint_as_float(v1));
        }
#pragma unroll
        for (int s = 0; s < NS; ++s) ac = mfma(a[s], S1[s], ac);
      };
      ldv(va, 0, 0); ldv(vb, 1, 0);
      SB();
      vv(va, mv[0], acc[1][0]);
      SB();
      issue_v(0);
      ldv(va, 0, 1);
      SB();
      vv(vb, mv[1], acc[1][1]);
      SB();
      ldv(vb, 1, 1);
      SB();
      vv(va, mv[0], acc[2][0]);
      SB();
      issue_v(1);
      ldv(va, 0, 2);
      SB();
      vv(vb, mv[1], acc[2][1]);
      SB();
      ldv(vb, 1, 2);
      SB();
      vv(va, mv[0], acc[3][0]);
      SB();
      issue_v(2);
      vv(vb, mv[1], acc[3][1]);
      SB();
      wait_h();
    };
    const int nfull = (cnt + 15) >> 5;
    for (int t = 0; t < nfull; ++t) { tile(t, std::integral_constant<int, 2>{}); setprio(min(ntile - 2 - t, 2)); }
    if (nfull < ntile) tile(nfull, std::integral_constant<int, 1>{});
    if (more2) ++r2;
    else if (more1) { ++r1; r2 = 0; }
    else if (moreb) { ++batch; r1 = 0; r2 = 0; }
    else break;
    asm volatile("s_waitcnt vmcnt(0)" ::: "memory");
    wg_barrier();
  }
  {
    FRESH_LANE;
    u32x4 o[16];
#pragma unroll
    for (int i = 0; i < 16; ++i) o[i] = pack8(acc[i >> 2][(i >> 1) & 1], i & 1);
    asm volatile("s_waitcnt vmcnt(0)" ::: "memory");
    if (l31 < GN) {
      const unsigned wa = TB + l31 * 512 + 16 * ((4 * h) ^ (l31 & 7));
#pragma unroll
      for (int i = 0; i < 16; ++i) *LDSP(u32x4, (wa ^ (16 * (i & 3))) + 128 * (i >> 2)) = o[i];
    }
    const int gw = __builtin_amdgcn_readfirstlane(max(min(nb - grp * GN, GN), 0));
    const uint64_t op = (uint64_t)out + (size_t)(node0 + grp * GN) * 512;
    const __amdgpu_buffer_rsrc_t rb_o = __builtin_amdgcn_make_buffer_rsrc((void*)op, 0, gw * 512, 0x00020000);
    u32x4 v[(GN + 1) / 2];
#pragma unroll
    for (int i = 0; i < (GN + 1) / 2; ++i) v[i] = *LDSP(u32x4, TB + i * 1024 + lane * 16);
    const unsigned oa = h * 512 + 16 * (l31 ^ h);
#pragma unroll
    for (int i = 0; i < (GN + 1) / 2; ++i) __builtin_amdgcn_raw_buffer_store_b128(v[i], rb_o, oa ^ (32 * (i & 3)), i * 1024, WT);
  }
  asm volatile("painn_code_hi:");
}

extern "C" __global__ __launch_bounds__(192) void k_gen_phi(const u16* __restrict__ sv, const u16* __restrict__ w1, const u16* __restrict__ w2,
                                                            float* __restrict__ phi) {
  __shared__ float s[64], hd[64];
  const int t = TID; const size_t n = BID;
  if (t < 64) s[t] = lo16(sv[n * 256 + t]);
  wg_barrier();
  if (t < 64) {
    float a = 0.f;
    for (int k = 0; k < 64; ++k) a += s[k] * lo16(w1[k * 64 + t]);
    hd[t] = rb(a * __builtin_amdgcn_rcpf(1.0f + __builtin_amdgcn_exp2f(a * -1.4426950408889634f)));
  }
  wg_barrier();
  float a = 0.f;
  for (int k = 0; k < 64; ++k) a += hd[k] * lo16(w2[k * 192 + t]);
  phi[n * 192 + t] = a;
}
extern "C" __global__ __launch_bounds__(256) void k_gen_edge(const u16* __restrict__ sv, const u16* __restrict__ pos, const int* __restrict__ src,
                                                             const int* __restrict__ dst, const u16* __restrict__ freqs, const u16* __restrict__ wr,
                                                             const float* __restrict__ phi, float* __restrict__ acc, int E) {
  const size_t e = (size_t)BID * 4 + (TID >> 6);
  const int c = TID & 63;
  if (e >= (size_t)E) return;
  const size_t s = src[e], t = dst[e];
  const float x = rb(lo16(pos[s * 3]) - lo16(pos[t * 3])), y = rb(lo16(pos[s * 3 + 1]) - lo16(pos[t * 3 + 1])), z = rb(lo16(pos[s * 3 + 2]) - lo16(pos[t * 3 + 2]));
  const float d = rb(__builtin_amdgcn_sqrtf(rb(rb(rb(x * x) + rb(y * y) + rb(z * z)) + 1e-8f)));
  if (!(d < 5.0f)) return;
  const float inv = __builtin_amdgcn_rcpf(d);
  const float f = 0.5f * rb(rb(__builtin_amdgcn_cosf(rb(rb(3.141592653589793f * d) * 0.2f) * 0.15915494309189535f)) + 1.0f);
  float w0 = 0.f, w1 = 0.f, w2 = 0.f;
  for (int k = 0; k < 16; ++k) {
    float r = rb(d * lo16(freqs[k])) * 0.15915494309189535f;
    r -= __builtin_rintf(r);
    const float b = rb(__builtin_amdgcn_sinf(r) * inv);
    w0 += b * lo16(wr[k * 192 + c]); w1 += b * lo16(wr[k * 192 + 64 + c]); w2 += b * lo16(wr[k * 192 + 128 + c]);
  }
  const float ms = phi[s * 192 + c] * w0 * f, mv = phi[s * 192 + 64 + c] * w1 * f, mr = phi[s * 192 + 128 + c] * w2 * f;
  float* a = acc + t * 256 + c;
  __hip_atomic_fetch_add(a, ms, __ATOMIC_RELAXED, __HIP_MEMORY_SCOPE_AGENT);
  __hip_atomic_fetch_add(a + 64, mv * lo16(sv[s * 256 + 64 + c]) + mr * rb(x * inv), __ATOMIC_RELAXED, __HIP_MEMORY_SCOPE_AGENT);
  __hip_atomic_fetch_add(a + 128, mv * lo16(sv[s * 256 + 128 + c]) + mr * rb(y * inv), __ATOMIC_RELAXED, __HIP_MEMORY_SCOPE_AGENT);
  __hip_atomic_fetch_add(a + 192, mv * lo16(sv[s * 256 + 192 + c]) + mr * rb(z * inv), __ATOMIC_RELAXED, __HIP_MEMORY_SCOPE_AGENT);
}
extern "C" __global__ __launch_bounds__(256) void k_gen_out(const u16* __restrict__ sv, const float* __restrict__ acc, u16* __restrict__ out) {
  const size_t i = (size_t)BID * 256 + TID;
  out[i] = (u16)pk2(lo16(sv[i]) + acc[i], 0.f);
}

#if defined(__HIP_DEVICE_COMPILE__)
#pragma clang attribute pop
#endif
extern "C" void kernel_launch(void* const* d_in, const int* in_sizes, int n_in, void* d_out, int out_size, void* d_ws, size_t ws_size,
                              hipStream_t stream) {
  const u16* sv = (const u16*)d_in[0];
  const u16* pos = (const u16*)d_in[1];
  const int* src = (const int*)d_in[2];
  const int* dst = (const int*)d_in[3];
  const u16* fr = (const u16*)d_in[4];
  const u16* w1 = (const u16*)d_in[5];
  const u16* w2 = (const u16*)d_in[6];
  const u16* wr = (const u16*)d_in[7];
  const int N = in_sizes[1] / 3, E = in_sizes[2];
  char* ws = (char*)d_ws;
  if (N <= 0) return;
  if (N > 51200 || E > 256 * CH) {
    float* acc = (float*)ws;
    float* phi = acc + (size_t)N * 256;
    (void)hipMemsetAsync(acc, 0, (size_t)N * 1024, stream);
    k_gen_phi<<<N, 192, 0, stream>>>(sv, w1, w2, phi);
    if (E > 0) k_gen_edge<<<(E + 3) / 4, 256, 0, stream>>>(sv, pos, src, dst, fr, wr, phi, acc, E);
    k_gen_out<<<N, 256, 0, stream>>>(sv, acc, (u16*)d_out);
    return;
  }
  const int BN = (N + 255) / 256 < 2 ? 2 : (N + 255) / 256;
  const int B = (N + BN - 1) / BN, bpx = (B + 7) / 8;
  int chunk = (((E + 195) / 196) + 3) & ~3;
  chunk = chunk < 1024 ? 1024 : chunk > CH ? CH : chunk;
  const int W = (E + chunk - 1) / chunk;
  const int HW = 200, WP = (W + 7) & ~7;
  const unsigned magicBN = (unsigned)(0x100000000ull / (unsigned)BN) + 1u;
  (void)hipFuncSetAttribute((const void*)k_agg, hipFuncAttributeMaxDynamicSharedMemorySize, L_END);
  k_bin<<<WP + HW + 8, 512, 0, stream>>>(src, dst, sv, pos, w1, ws, (unsigned)N | (unsigned)BN << 16 | (unsigned)bpx << 24, E, magicBN,
                                         (unsigned)chunk | (unsigned)W << 16, w2, wr, fr);
  k_agg<<<8 * bpx, 512, L_END, stream>>>(sv, ws, (u16*)d_out, N, W, BN, B, bpx);
}
```

```cpp
#define CODEPF
#define SELFPF
#if defined(__HIP_DEVICE_COMPILE__)
#pragma clang attribute push(__attribute__((target("no-packed-fp32-ops"))), apply_to = function)
#endif
#include <hip/hip_runtime.h>
#include <stdint.h>
#include <type_traits>

#define STAG 2
#define HEAVYFIRST 1
#define NOST 1
#define NEARLY 1
#define NODEPRIO 0
#define K1ORD 1
#define BINPRIO 0
#define OWNE2 0
#define PMODE 6
#define P_START min(ntile - 1, 2)
#define P_NEXT min(ntile - 2 - t, 2)
#define P_PH2 __builtin_amdgcn_s_setprio(3)
#define TID __builtin_amdgcn_workitem_id_x()
#define BID __builtin_amdgcn_workgroup_id_x()
typedef unsigned short u16;
typedef __attribute__((ext_vector_type(8))) __bf16 bf16x8;
typedef __attribute__((ext_vector_type(2))) __bf16 bf16x2;
typedef __attribute__((ext_vector_type(4))) short s16x4;
typedef __attribute__((ext_vector_type(2))) float f32x2;
typedef __attribute__((ext_vector_type(4))) float f32x4;
typedef __attribute__((ext_vector_type(16))) float f32x16;
typedef __attribute__((ext_vector_type(4))) unsigned u32x4;
typedef __attribute__((ext_vector_type(3))) unsigned u32x3;
typedef __attribute__((ext_vector_type(2))) unsigned u32x2;
#define LDSP(T, off) ((__attribute__((address_space(3))) T*)(uintptr_t)(off))
#define DI __device__ __forceinline__
#define SB() __builtin_amdgcn_sched_barrier(0)

constexpr int CH = 4096;
constexpr int SEGQ = CH / 4 + 192;
constexpr int GN = 25;
constexpr int NFRAG = 30;
constexpr int SUBC = 32;
constexpr int CAP = 128;
constexpr int BQ = 256;
constexpr unsigned SENT = 0xFFFFFFFFu;
constexpr int L_DPOS = 0;
constexpr int L_RAW = 4096;
constexpr int L_CNT = L_RAW + 8192;
constexpr int L_NB = L_CNT + 256;
constexpr int L_LIST = L_NB + 64;
constexpr int O_D = 640, O_DL = O_D + 256, O_F = O_DL + 256, O_FX = O_F + 256;
constexpr int LISTS = O_FX + 768;
constexpr int L_W = L_LIST + 8 * LISTS;
constexpr int L_FR = L_W + NFRAG * 1024;
constexpr int L_TB = (L_FR + 64 + 511) / 512 * 512;
constexpr int TBS = GN * 512;
constexpr int L_END = L_TB + 8 * TBS;
static_assert(L_END <= 163840 && L_TB < 65536 && L_W % 16 == 0 && LISTS % 16 == 0 && SUBC == 32, "LDS");
constexpr size_t WS_T = 65536;
constexpr size_t WS_SEG = WS_T + 256 * 256 * 4;
constexpr size_t WS_P = WS_SEG + (size_t)256 * SEGQ * 16;
constexpr size_t WS_C = WS_P + 51200 * 8;
constexpr size_t WS_H = WS_C + 51200 * 2;
constexpr size_t WS_END = WS_H + 51200 * 128;
constexpr size_t WS_F = NFRAG * 1024;
static_assert(WS_END < 0xffffff, "ws");

#define STAMP(i)
#define STAMP1(i)
#define WAITV
DI float lo16(unsigned x) { return __uint_as_float(x << 16); }
DI float hi16(unsigned x) { return __uint_as_float(x & 0xffff0000u); }
DI unsigned pk2(float a, float b) { f32x2 v = {a, b}; return __builtin_bit_cast(unsigned, __builtin_convertvector(v, bf16x2)); }
DI float rb(float a) { return hi16(pk2(a, a)); }
DI f32x16 mfma(u32x4 a, u32x4 b, f32x16 c) {
  return __builtin_amdgcn_mfma_f32_32x32x16_bf16(__builtin_bit_cast(bf16x8, a), __builtin_bit_cast(bf16x8, b), c, 0, 0, 0);
}
DI f32x16 zero16() { f32x16 z;
#pragma unroll
  for (int r = 0; r < 16; ++r) z[r] = 0.f;
  return z; }
DI int wave_incl_scan(int v) {
  v += __builtin_amdgcn_update_dpp(0, v, 0x111, 0xf, 0xf, false);
  v += __builtin_amdgcn_update_dpp(0, v, 0x112, 0xf, 0xf, false);
  v += __builtin_amdgcn_update_dpp(0, v, 0x114, 0xf, 0xf, false);
  v += __builtin_amdgcn_update_dpp(0, v, 0x118, 0xf, 0xf, false);
  v += __builtin_amdgcn_update_dpp(0, v, 0x142, 0xa, 0xf, false);
  v += __builtin_amdgcn_update_dpp(0, v, 0x143, 0xc, 0xf, false);
  return v;
}
DI int chan_of(int t2, int i32) { return 32 * ((i32 >> 2) & 1) + 16 * t2 + 4 * (i32 >> 3) + (i32 & 3); }
DI u32x4 pack8(const f32x16& x, int s) {
  return u32x4{pk2(x[8 * s], x[8 * s + 1]), pk2(x[8 * s + 2], x[8 * s + 3]), pk2(x[8 * s + 4], x[8 * s + 5]), pk2(x[8 * s + 6], x[8 * s + 7])};
}
DI void setprio(int p) {
  if (p >= 3) __builtin_amdgcn_s_setprio(3);
  else if (p == 2) __builtin_amdgcn_s_setprio(2);
  else if (p == 1) __builtin_amdgcn_s_setprio(1);
  else __builtin_amdgcn_s_setprio(0);
}
DI u32x2 trread(unsigned a) { return __builtin_bit_cast(u32x2, __builtin_amdgcn_ds_read_tr16_b64_v4i16(LDSP(s16x4, a))); }
#define K1AUX 17
#define IDXAUX 0
#define K2AUX 17
DI void st16(void* base, unsigned off, u32x4 v) {
  const __amdgpu_buffer_rsrc_t r = __builtin_amdgcn_make_buffer_rsrc(base, 0, 0x7fffffff, 0x00020000);
  __builtin_amdgcn_raw_buffer_store_b128(v, r, off, 0, K1AUX);
}
DI unsigned qmod(float x) { return min((unsigned)(__builtin_amdgcn_fractf(x * 0.0625f) * 65536.0f), 65535u); }
DI unsigned opq(unsigned c) { asm volatile("" : "+v"(c)); return c; }
DI unsigned swap23(unsigned p) { return (p & ~12u) | ((p & 4u) << 1) | ((p & 8u) >> 1); }
DI void wg_barrier() { asm volatile("s_waitcnt lgkmcnt(0)\n\ts_barrier" ::: "memory"); }

DI void dma16(u32x4 rsrc, unsigned voff, unsigned ldsaddr) {
  asm volatile("s_mov_b32 m0, %2\n\ts_nop 3\n\tbuffer_load_dwordx4 %0, %1, 0 offen lds" ::"v"(voff), "s"(rsrc), "s"(ldsaddr) : "memory");
}
DI void dma16x4(u32x4 rsrc, const unsigned (&voff)[4], unsigned soff, unsigned m0) {
  asm volatile("s_mov_b32 m0, %6\n\ts_nop 3\n\tbuffer_load_dwordx4 %0, %4, %5 offen lds\n\t"
               "s_add_u32 m0, m0, 0x400\n\ts_nop 0\n\tbuffer_load_dwordx4 %1, %4, %5 offen lds\n\t"
               "s_add_u32 m0, m0, 0x400\n\ts_nop 0\n\tbuffer_load_dwordx4 %2, %4, %5 offen lds\n\t"
               "s_add_u32 m0, m0, 0x400\n\ts_nop 0\n\tbuffer_load_dwordx4 %3, %4, %5 offen lds"
               ::"v"(voff[0]), "v"(voff[1]), "v"(voff[2]), "v"(voff[3]), "s"(rsrc), "s"(soff), "s"(m0) : "memory", "scc");
}
constexpr int K1_CNT = 0, K1_STAGE = 8 * 256 * 4, K1_TOT = K1_STAGE + SEGQ * 16, K1_W1 = 0, K1_HT = 8192, K1_END = K1_HT + 8 * 4096;
#if defined(__HIP_DEVICE_COMPILE__)
#pragma clang attribute pop
#endif
extern "C" __global__ __launch_bounds__(512, 4) void k_bin(const int* __restrict__ src, const int* __restrict__ dst, unsigned* __restrict__ T,
                                                u32x4* __restrict__ seg, const u16* __restrict__ sv, const u16* __restrict__ pos,
                                                const u16* __restrict__ w1, const u16* __restrict__ w2, const u16* __restrict__ wr, const u16* __restrict__ freqs,
                                                u32x4* __restrict__ wf, u32x2* __restrict__ ptab, u16* __restrict__ ctab, char* __restrict__ htab, int N, int E,
                                                int W, int HW, unsigned magicBN, int BN, int WP, int NPX, int chunk) {
  __shared__ __attribute__((aligned(16))) char lds_raw[K1_END + 256];
  const unsigned L0 = (unsigned)(uintptr_t)(__attribute__((address_space(3))) char*)lds_raw;
  const int tid = TID, lane = tid & 63;
  const int wave = __builtin_amdgcn_readfirstlane(tid >> 6);
  int bid = BID;
  if (bid < WP + HW) { if (bid < WP) { if (bid >= W) return; bid += HW; } else bid -= WP; } else bid += W - WP;
  STAMP1(0);
  if (wave == 0) {
    uint64_t pc; unsigned len;
    asm volatile("s_getpc_b64 %0\npainn_self_lo:\n\ts_mov_b32 %1, painn_code_hi-painn_self_lo" : "=s"(pc), "=s"(len));
    const u32x4 rc = {(unsigned)pc, (unsigned)(pc >> 32) & 0xffffu, len, 0x00020000u};
    asm volatile("s_mov_b32 m0, %2\n\ts_nop 3\n\tbuffer_load_dword %0, %1, 0 offen lds" ::"v"(lane * 128), "s"(rc), "s"(L0 + K1_END) : "memory");
  }
  if (bid >= W + HW) {
    {
      uint64_t pc; unsigned len;
      asm volatile("s_getpc_b64 %0\npainn_code_lo:\n\ts_mov_b32 %1, painn_code_hi-painn_code_lo" : "=s"(pc), "=s"(len));
      const __amdgpu_buffer_rsrc_t rc = __builtin_amdgcn_make_buffer_rsrc((void*)pc, 0, len, 0x00020000);
      const unsigned x = __builtin_amdgcn_raw_buffer_load_b32(rc, tid * 64, 0, 0);
      asm volatile("" ::"v"(x));
    }
    const int idx = (bid - W - HW) * 512 + tid;
    if (idx >= NFRAG * 64) {
      if (idx < NFRAG * 64 + 16) ((unsigned*)wf)[WS_F / 4 + idx - NFRAG * 64] = (unsigned)freqs[idx - NFRAG * 64] << 16;
      return;
    }
    const int f = idx >> 6, i32 = lane & 31, h = lane >> 5;
    const u16* p; int s2;
    if (f < 24) { const int ct = f >> 2; p = w2 + (16 * (f & 3) + 4 * h) * 192 + 64 * (ct >> 1) + chan_of(ct & 1, i32); s2 = 8 * 192; }
    else { const int ct = f - 24; p = wr + 8 * h * 192 + 64 * (ct >> 1) + chan_of(ct & 1, i32); s2 = 4 * 192; }
    unsigned v[8];
#pragma unroll
    for (int j = 0; j < 8; ++j) v[j] = p[(j & 3) * 192 + (j >> 2) * s2];
    wf[idx] = u32x4{v[0] | v[1] << 16, v[2] | v[3] << 16, v[4] | v[5] << 16, v[6] | v[7] << 16};
    return;
  }
  if (bid < HW) {
    const int l31 = lane & 31, h = lane >> 5;
    const int nl = (wave * (HW >> 3) + (bid >> 3)) * 32, nend = min(N, ((bid & 7) + 1) * NPX);
    const int n0 = nl < NPX ? (bid & 7) * NPX + nl : N;
    const int nn = min(n0 + l31, N - 1);
    __builtin_amdgcn_s_setprio(NODEPRIO);
    const unsigned HT = L0 + K1_HT + wave * 4096;
    {
      const uint64_t sp = (uint64_t)sv, wp = (uint64_t)w1;
      const u32x4 rs_sv = {(unsigned)sp, (unsigned)(sp >> 32), (unsigned)N * 512u, 0x00020000u};
      const u32x4 rs_w1 = {(unsigned)wp, (unsigned)(wp >> 32), 8192u, 0x00020000u};
      const unsigned o = (unsigned)(n0 + (lane >> 3)) * 512u + 16u * ((lane & 7) ^ (lane >> 3));
      const unsigned v4[4] = {o, o + 4096u, o + 8192u, o + 12288u};
      dma16x4(rs_sv, v4, 0, HT);
      dma16(rs_w1, tid * 16u, L0 + K1_W1 + wave * 1024);
    }
    unsigned px = pos[nn * 3], py = pos[nn * 3 + 1], pz = pos[nn * 3 + 2];
    asm volatile("s_waitcnt vmcnt(0)\n\ts_barrier" : "+v"(px), "+v"(py), "+v"(pz)::"memory");
    STAMP1(1); STAMP1(2);
    if (n0 >= N) return;
    u32x4 sf[4];
#pragma unroll
    for (int ks = 0; ks < 4; ++ks) sf[ks] = *LDSP(u32x4, HT + l31 * 128 + 16 * ((4 * h + ks) ^ (l31 & 7)));
    const unsigned ta = L0 + K1_W1 + (32 * h + ((lane >> 2) & 3)) * 128 + (16 * ((lane >> 4) & 1) + 4 * (lane & 3)) * 2;
    f32x16 d1[2];
#pragma unroll
    for (int ht = 0; ht < 2; ++ht) {
      d1[ht] = zero16();
#pragma unroll
      for (int ks = 0; ks < 4; ++ks) {
        const u32x2 a0 = trread(ta + ks * 1024 + ht * 64), a1 = trread(ta + ks * 1024 + 512 + ht * 64);
        d1[ht] = mfma(u32x4{a0[0], a0[1], a1[0], a1[1]}, sf[ks], d1[ht]);
      }
    }
#pragma unroll
    for (int ht = 0; ht < 2; ++ht) {
      f32x16 e = d1[ht] * -1.4426950408889634f;
#pragma unroll
      for (int r = 0; r < 16; ++r) e[r] = __builtin_amdgcn_exp2f(e[r]);
      e += 1.0f;
#pragma unroll
      for (int r = 0; r < 16; ++r) e[r] = __builtin_amdgcn_rcpf(e[r]);
      e *= d1[ht];
#pragma unroll
      for (int s = 0; s < 2; ++s) *LDSP(u32x4, HT + l31 * 128 + 16 * ((4 * h + 2 * ht + s) ^ (l31 & 7))) = pack8(e, s);
    }
    STAMP1(3);
#pragma unroll
    for (int i = 0; i < 4; ++i) {
      const int n = 8 * i + (lane >> 3);
      const u32x4 v = *LDSP(u32x4, HT + i * 1024 + lane * 16);
      if (n0 + n < nend) st16(htab, (n0 + n) * 128 + 16 * ((lane & 7) ^ (n & 7)), v);
    }
    if (h == 0 && n0 + l31 < nend) {
      ptab[n0 + l31] = u32x2{px | py << 16, pz << 16};
      ctab[n0 + l31] = (u16)(qmod(lo16(px)) >> 11 | (qmod(lo16(py)) >> 11) << 5 | (qmod(lo16(pz)) & 0xfc00u));
    }
    STAMP1(4); WAITV; STAMP1(5);
    return;
  }
  __builtin_amdgcn_s_setprio(BINPRIO);
  const int cw = bid - HW;
  const int eend = min(E, (cw + 1) * chunk);
  const __amdgpu_buffer_rsrc_t rb_s = __builtin_amdgcn_make_buffer_rsrc((void*)src, 0, eend * 4, 0x00020000);
  const __amdgpu_buffer_rsrc_t rb_d = __builtin_amdgcn_make_buffer_rsrc((void*)dst, 0, eend * 4, 0x00020000);
  const int e0 = cw * chunk + tid * 4;
  u32x4 s4[2], d4[2];
#pragma unroll
  for (int u = 0; u < 2; ++u) d4[u] = __builtin_amdgcn_raw_buffer_load_b128(rb_d, (e0 + u * 2048) * 4, 0, IDXAUX);
#pragma unroll
  for (int u = 0; u < 2; ++u) s4[u] = __builtin_amdgcn_raw_buffer_load_b128(rb_s, (e0 + u * 2048) * 4, 0, IDXAUX);
  *LDSP(u32x4, L0 + K1_CNT + wave * 1024 + lane * 16) = u32x4{0, 0, 0, 0};
#pragma unroll
  for (int i = 0; i < 3; ++i) if (i * 512 + tid < SEGQ) *LDSP(u32x4, L0 + K1_STAGE + (i * 512 + tid) * 16) = u32x4{0, 0, 0, 0};
  WAITV; STAMP1(1);
  unsigned ca[8], hi8[8], rank[8];
#pragma unroll
  for (int k = 0; k < 8; ++k) {
    const unsigned d = d4[k >> 2][k & 3];
    const unsigned b = (unsigned)(((uint64_t)d * magicBN) >> 32);
    ca[k] = L0 + K1_CNT + wave * 1024 + b * 4;
    rank[k] = SENT;
    if (e0 + (k >> 2) * 2048 + (k & 3) < eend) rank[k] = __hip_atomic_fetch_add(LDSP(unsigned, ca[k]), 1u, __ATOMIC_RELAXED, __HIP_MEMORY_SCOPE_WORKGROUP);
    const unsigned dl = d - b * (unsigned)BN;
    hi8[k] = dl + ((dl * 41u) >> 10) * 7u + 1u;
  }
  wg_barrier();
  STAMP1(2);
  if (wave == 0) {
    u32x4 c[8], tot = {0, 0, 0, 0};
#pragma unroll
    for (int w = 0; w < 8; ++w) { c[w] = *LDSP(u32x4, L0 + K1_CNT + w * 1024 + lane * 16); tot += c[w]; }
    const u32x4 q = (tot + 3u) >> 2;
    const unsigned ls = q[0] + q[1] + q[2] + q[3];
    const unsigned incl = wave_incl_scan(ls);
    u32x4 st; st[0] = incl - ls; st[1] = st[0] + q[0]; st[2] = st[1] + q[1]; st[3] = st[2] + q[2];
    u32x4 run = st * 4u;
#pragma unroll
    for (int w = 0; w < 8; ++w) { *LDSP(u32x4, L0 + K1_CNT + w * 1024 + lane * 16) = run; run += c[w]; }
    if (lane == 63) *LDSP(unsigned, L0 + K1_TOT) = incl;
    st16(T, cw * 1024 + 16 * lane, st | (q << 16));
  }
  wg_barrier();
  STAMP1(3);
  unsigned base[8];
#pragma unroll
  for (int k = 0; k < 8; ++k) base[k] = *LDSP(unsigned, ca[k]);
#pragma unroll
  for (int k = 0; k < 8; ++k)
    if (rank[k] != SENT) *LDSP(unsigned, L0 + K1_STAGE + (base[k] + rank[k]) * 4) = s4[k >> 2][k & 3] << 3 | hi8[k] << 24;
  const int total = *LDSP(int, L0 + K1_TOT);
  wg_barrier();
#pragma unroll
  for (int i = 0; i < 3; ++i) {
    const int qd = i * 512 + tid;
    if (qd < total) st16(seg, (cw * SEGQ + qd) * 16, *LDSP(u32x4, L0 + K1_STAGE + qd * 16));
  }
  STAMP1(4); WAITV; STAMP1(5);
}

#if defined(__HIP_DEVICE_COMPILE__)
#pragma clang attribute push(__attribute__((target("no-packed-fp32-ops"))), apply_to = function)
#endif
DI u32x4 eqmask4(u32x4 a, unsigned b, unsigned ones) {
  u32x4 x;
#pragma unroll
  for (int i = 0; i < 4; ++i) x[i] = a[i] ^ b;
  asm("v_pk_min_u16 %0, %0, %4\n\tv_pk_min_u16 %1, %1, %4\n\tv_pk_min_u16 %2, %2, %4\n\tv_pk_min_u16 %3, %3, %4\n\t"
      "v_pk_sub_u16 %0, %0, %4\n\tv_pk_sub_u16 %1, %1, %4\n\tv_pk_sub_u16 %2, %2, %4\n\tv_pk_sub_u16 %3, %3, %4"
      : "+v"(x[0]), "+v"(x[1]), "+v"(x[2]), "+v"(x[3]) : "s"(ones));
  return x;
}

typedef __attribute__((ext_vector_type(2))) short s16x2;
extern "C" __global__ __launch_bounds__(512, 2) void k_agg(const u16* __restrict__ sv, const char* __restrict__ wf, u16* __restrict__ out, int N,
                                                           int W, int BN, int B, int bpx) {
  const int tid = TID, lane0 = tid & 63; (void)lane0;
  const int wave = __builtin_amdgcn_readfirstlane(tid >> 6);
  const int b = ((int)BID & 7) * bpx + ((int)BID >> 3);
  if (b >= B) return;
  unsigned long long* diag = (unsigned long long*)((char*)wf + (64 << 20)); (void)diag;
  STAMP(0);
  constexpr unsigned L0 = 0;
  const int node0 = b * BN;
  const int nb = min(BN, N - node0);
  const uint64_t svp = (uint64_t)sv, wfp = (uint64_t)wf;
  const u32x4 rs_sv = {(unsigned)svp, (unsigned)(svp >> 32), (unsigned)N * 512u, 0x00020000u};
  const u32x4 rs_ws = {(unsigned)wfp, (unsigned)(wfp >> 32), (unsigned)WS_END, 0x00020000u};
#define FRESH_LANE int lane; asm volatile("v_mbcnt_lo_u32_b32 %0, -1, 0\n\tv_mbcnt_hi_u32_b32 %0, -1, %0" : "=v"(lane)); const int l31 = lane & 31, h = lane >> 5; (void)l31; (void)h
  const unsigned TB = L0 + L_TB + wave * TBS;

  f32x16 acc[4][2];
  u32x4 hN[4];
  int batch = 0, r1 = 0;
  bool first = true;
  const int grp = wave;
  while (true) {
    u32x4 ol[13];
    {
      FRESH_LANE;
      const unsigned lo = 16u * lane;
      const unsigned v4[4] = {lo, lo + 1024u, lo + 2048u, lo + 3072u};
      const unsigned oo = first ? (unsigned)(node0 + wave * GN + h) * 512u + 16u * (l31 ^ h) : 0x7fff0000u;
#define OWNA(i) ((oo ^ (32u * ((i) & 7))) + 1024u * (i))
#define OWN4(j) asm volatile("s_nop 4\n\tbuffer_load_dwordx4 %0, %4, %8, 0 offen\n\tbuffer_load_dwordx4 %1, %5, %8, 0 offen\n\t" \
                             "buffer_load_dwordx4 %2, %6, %8, 0 offen\n\tbuffer_load_dwordx4 %3, %7, %8, 0 offen" \
                             : "=&v"(ol[4 * j]), "=&v"(ol[4 * j + 1]), "=&v"(ol[4 * j + 2]), "=&v"(ol[4 * j + 3]) \
                             : "v"(OWNA(4 * j)), "v"(OWNA(4 * j + 1)), "v"(OWNA(4 * j + 2)), "v"(OWNA(4 * j + 3)), "s"(rs_sv) : "memory")
#define OWN1() asm volatile("s_nop 4\n\tbuffer_load_dwordx4 %0, %1, %2, 0 offen" : "=&v"(ol[12]) : "v"(h ? 0x7fff0000u : OWNA(12)), "s"(rs_sv) : "memory")
      if (wave >= 4) {
        const int rw = wave - 4;
        const unsigned MAP = L_LIST + wave * LISTS;
        const int cpw = (W + 3) >> 2;
        const int cell = rw * cpw + lane;
        unsigned t;
        u32x2 dp;
        asm volatile("s_nop 4\n\tbuffer_load_dword %0, %2, %4, 0 offen\n\tbuffer_load_dwordx2 %1, %3, %4, 0 offen\n\ts_barrier\n\ts_waitcnt vmcnt(0)"
                     : "=&v"(t), "=&v"(dp) : "v"((lane < cpw && cell < W) ? (int)WS_T + (cell * 256 + b) * 4 : 0x7fffffff),
                       "v"((int)WS_P + (node0 + (2 * rw + h) * GN + l31 - 1) * 8), "s"(rs_ws) : "memory");
        const int q = t >> 16;
        const int incl = wave_incl_scan(q), excl = incl - q;
        const int total = __builtin_amdgcn_readlane(incl, 63);
        STAMP(1);
        const int win0 = batch * BQ;
        const int gq = (int)(WS_SEG / 16) + cell * SEGQ + (int)(t & 0xffffu) - excl;
        const int hi = min(incl, win0 + BQ);
#pragma nounroll
        for (int i = max(excl, win0); i < hi; ++i) *LDSP(int, MAP + (i - win0) * 4) = (gq + i) * 16;
        unsigned mq[4];
#pragma unroll
        for (int s = 0; s < 4; ++s) mq[s] = *LDSP(int, MAP + (64 * s + lane) * 4);
#pragma unroll
        for (int s = 0; s < 4; ++s) if (win0 + 64 * s + lane >= total) mq[s] = 0x7fffffffu;
        dma16x4(rs_ws, mq, 0, L_W + rw * 4096);
        if (lane == 0) *LDSP(int, L_NB + rw * 4) = (total + BQ - 1) / BQ;
        const unsigned qx = (qmod(lo16(dp[0])) - 1024u) & 0xffffu, qy = qmod(hi16(dp[0])) - 1024u, qz = (qmod(__uint_as_float(dp[1])) - 512u) & 0xffffu;
        *LDSP(u32x4, L_DPOS + rw * 1024 + lo) = (unsigned)(l31 - 1) < (unsigned)GN ? u32x4{dp[0], dp[1], qx | qy << 16, qz} : u32x4{0x7fc07fc0u, 0x7fc00000u, 0u, 0x7fff0000u};
      } else {
        __builtin_amdgcn_s_barrier();
#pragma unroll
        for (int i = 0; i < 6; ++i) dma16x4(rs_ws, v4, WS_C + (wave * 6 + i) * 4096, L_TB + (wave * 6 + i) * 4096);
        dma16(rs_ws, WS_C + (96 + wave) * 1024u + lo, L_TB + (96 + wave) * 1024);
      }
      if (lane < 8) *LDSP(unsigned, L_CNT + lane * 32 + wave * 4) = lane >> 3;
      { const unsigned z = opq(0u); *LDSP(u32x4, L_RAW + wave * 1024 + lo) = u32x4{z, z, z, z}; }
      OWN4(0); OWN1();
      if (OWNE2) OWN4(1);
      asm volatile("s_waitcnt vmcnt(%0)" ::"i"(OWNE2 ? 9 : 5) : "memory");
      STAMP(2);
      wg_barrier();
      u32x4 r4[2];
#pragma unroll
      for (int s = 0; s < 2; ++s) r4[s] = *LDSP(u32x4, L_W + (2 * s + h) * 4096 + ((wave + 2 * (2 * s + h)) & 7) * 512 + l31 * 16);
      if (OWNE2) OWN4(2); else OWN4(1);
      const unsigned one = opq(1u);
#pragma unroll
      for (int s = 0; s < 2; ++s) {
        unsigned cd[4];
        u32x2 dq[4];
#pragma unroll
        for (int c = 0; c < 4; ++c) {
          cd[c] = *LDSP(u16, L_TB + ((r4[s][c] >> 2) & 0x3fffffu));
          dq[c] = *LDSP(u32x2, L_DPOS + 8 + ((r4[s][c] >> 20) & 0xff0u));
        }
        unsigned rank[4];
#pragma unroll
        for (int c = 0; c < 4; ++c) {
          const unsigned cxy = ((cd[c] & 0x3ffu) * 0x400800u) & 0xf800f800u, cz = cd[c] & 0xfc00u;
          const unsigned qxy = dq[c][0], qz = dq[c][1];
          const s16x2 dxy = __builtin_bit_cast(s16x2, qxy) - __builtin_bit_cast(s16x2, cxy);
          const s16x2 dz = __builtin_bit_cast(s16x2, qz) - __builtin_bit_cast(s16x2, cz);
          const unsigned d2 = __builtin_amdgcn_sdot2(dxy, dxy, __builtin_amdgcn_sdot2(dz, dz, 0, false), false);
          rank[c] = 0x7fffffffu;
          if (d2 < 488400000u)
            rank[c] = __hip_atomic_fetch_add(LDSP(unsigned, L_CNT + (((r4[s][c] >> 24) & 0xe0u) | wave * 4)), one, __ATOMIC_RELAXED, __HIP_MEMORY_SCOPE_WORKGROUP);
        }
        if (s == 0 && !OWNE2) OWN4(2);
#pragma unroll
        for (int c = 0; c < 4; ++c) {
          const unsigned p = rank[c] - SUBC * r1;
          if (p < (unsigned)SUBC) *LDSP(unsigned, L_RAW + (((r4[s][c] >> 19) & 0x1c00u) | wave * 4) + p * 32) = r4[s][c];
        }
      }
    }
    STAMP(3);
    wg_barrier();
    STAMP(4);
    bool more1, moreb;
    for (int r2 = 0;; ++r2) {
    FRESH_LANE;
    const unsigned LIST = L0 + L_LIST + grp * LISTS;
    int nact = 0, nstep;
    bool lighter;
    unsigned exy[4], ezd[4], ep[4];
    float ef[4];
    u32x4 own[16];
    {
      unsigned rec[4];
      u32x2 ps[4], pd[4];
#pragma unroll
      for (int i = 0; i < 4; ++i) rec[i] = *LDSP(unsigned, L_RAW + grp * 1024 + lane * 4 + i * 256);
      {
        asm volatile("s_nop 4\n\tbuffer_load_dwordx2 %0, %4, %8, %9 offen\n\tbuffer_load_dwordx2 %1, %5, %8, %9 offen\n\t"
                     "buffer_load_dwordx2 %2, %6, %8, %9 offen\n\tbuffer_load_dwordx2 %3, %7, %8, %9 offen"
                     : "=&v"(ps[0]), "=&v"(ps[1]), "=&v"(ps[2]), "=&v"(ps[3])
                     : "v"(rec[0] & 0xffffffu), "v"(rec[1] & 0xffffffu), "v"(rec[2] & 0xffffffu), "v"(rec[3] & 0xffffffu), "s"(rs_ws), "s"((unsigned)WS_P) : "memory");
      }
#pragma unroll
      for (int i = 0; i < 4; ++i) pd[i] = *LDSP(u32x2, L0 + L_DPOS + ((rec[i] >> 20) & 0xff0u));
      nstep = __builtin_amdgcn_ballot_w64(rec[3] != 0) ? 4 : __builtin_amdgcn_ballot_w64(rec[2] != 0) ? 3 : 2;
      {
        const u32x4 c0 = *LDSP(u32x4, L_CNT + (lane & 7) * 32), c1 = *LDSP(u32x4, L_CNT + (lane & 7) * 32 + 16);
        const int nbv = *LDSP(int, L_NB + (lane & 3) * 4);
        const unsigned mx = max(max(max(c0[0], c0[1]), max(c0[2], c0[3])), max(max(c1[0], c1[1]), max(c1[2], c1[3])));
        more1 = __builtin_amdgcn_ballot_w64(mx > (unsigned)(SUBC * (r1 + 1))) != 0;
        moreb = __builtin_amdgcn_ballot_w64(nbv > batch + 1) != 0;
        const int key = (c0[0] + c0[1] + c0[2] + c0[3] + c1[0] + c1[1] + c1[2] + c1[3]) * 2 + (((lane & 7) >> 2) ^ 1);
        lighter = HEAVYFIRST ? __builtin_amdgcn_readlane(key, grp) < __builtin_amdgcn_readlane(key, grp ^ 4) : wave >= 4;
      }
#pragma unroll
      for (int i = 0; i < 2; ++i) *LDSP(unsigned, LIST + (64 * i + lane) * 4) = opq(0xffffffu);
      if (lane < 32) *LDSP(unsigned, LIST + (128 + lane) * 4) = opq(0xffffffu);
      *LDSP(unsigned, LIST + O_D + lane * 4) = 0x3f803f80u;
      *LDSP(unsigned, LIST + O_DL + lane * 4) = 0xffffffffu;
      STAMP(11);
      asm volatile("s_waitcnt vmcnt(4)" : "+v"(ol[0]), "+v"(ol[1]), "+v"(ol[2]), "+v"(ol[3]), "+v"(ol[4]),
                   "+v"(ol[5]), "+v"(ol[6]), "+v"(ol[7]), "+v"(ol[8]), "+v"(ol[9]), "+v"(ol[10]), "+v"(ol[11]), "+v"(ol[12])::"memory");
#pragma unroll
      for (int i = 0; i < 12; ++i) *LDSP(u32x4, TB + i * 1024 + lane * 16) = ol[i];
      if (h == 0) *LDSP(u32x4, TB + 12 * 1024 + lane * 16) = ol[12];
      {
        const unsigned ra = TB + l31 * 512 + 16 * ((4 * h) ^ (l31 & 15));
#pragma unroll
        for (int i = 0; i < 16; ++i) own[i] = *LDSP(u32x4, (ra ^ (16 * (8 * ((i >> 2) & 1) + (i & 3)))) + 256 * (i >> 3));
      }
      asm volatile("s_waitcnt vmcnt(0)" : "+v"(ps[0]), "+v"(ps[1]), "+v"(ps[2]), "+v"(ps[3])::"memory");
      STAMP(12);
#pragma unroll
      for (int i = 0; i < 4; ++i) {
        exy[i] = 0; ezd[i] = 0; ef[i] = 0.f; ep[i] = SENT;
        if (i >= nstep) continue;
        const unsigned xy = pk2(lo16(ps[i][0]) - lo16(pd[i][0]), hi16(ps[i][0]) - hi16(pd[i][0]));
        const float z = rb(__uint_as_float(ps[i][1]) - __uint_as_float(pd[i][1]));
        const float x = lo16(xy), y = hi16(xy);
        const unsigned sq = pk2(x * x, y * y);
        const float q2 = rb(rb(lo16(sq) + hi16(sq) + rb(z * z)) + 1e-8f);
        const unsigned zd = pk2(z, __builtin_amdgcn_sqrtf(q2));
        const float d = hi16(zd);
        const float f = 0.5f * rb(rb(__builtin_amdgcn_cosf(rb(rb(3.141592653589793f * d) * 0.2f) * 0.15915494309189535f)) + 1.0f);
        const bool ok = (d < 5.0f) & (f != 0.0f);
        const uint64_t bm = __builtin_amdgcn_ballot_w64(ok);
        const unsigned p = __builtin_amdgcn_mbcnt_hi((unsigned)(bm >> 32), __builtin_amdgcn_mbcnt_lo((unsigned)bm, nact - CAP * r2));
        nact += __builtin_popcountll(bm);
        exy[i] = xy; ezd[i] = zd; ef[i] = f;
        if (ok & (p < (unsigned)CAP)) {
          ep[i] = p;
          *LDSP(unsigned, LIST + swap23(p) * 4) = rec[i] & 0xffffffu;
          *LDSP(u16, LIST + O_D + swap23(p) * 2) = (u16)(zd >> 16);
          *LDSP(u16, LIST + O_DL + p * 2) = (u16)(rec[i] >> 24);
        }
      }
    }
    const int cnt = min(nact - CAP * r2, CAP);
    const int ntile = (cnt + 31) >> 5;
    auto issue_h2 = [&](unsigned off) {
      asm volatile("s_nop 4\n\tbuffer_load_dwordx4 %0, %4, %5, %6 offen\n\tbuffer_load_dwordx4 %1, %4, %5, %6 offen offset:16\n\t"
                   "buffer_load_dwordx4 %2, %4, %5, %6 offen offset:32\n\tbuffer_load_dwordx4 %3, %4, %5, %6 offen offset:48"
                   : "=&v"(hN[0]), "=&v"(hN[1]), "=&v"(hN[2]), "=&v"(hN[3]) : "v"(off), "s"(rs_ws), "s"((unsigned)WS_H) : "memory");
    };
    auto issue_h = [&](int t) { FRESH_LANE; issue_h2(*LDSP(unsigned, LIST + (32 * t + l31) * 4) * 16u + 64u * h); };
    auto wait_h = [&]() {
      asm volatile("s_waitcnt vmcnt(12)" : "+v"(hN[0]), "+v"(hN[1]), "+v"(hN[2]), "+v"(hN[3])::"memory");
    };
    unsigned vo[4];
    auto prep_v = [&](int t) {
      FRESH_LANE;
      const int drow = lane >> 3;
      const unsigned dchunk = 16u * (((lane & 7) ^ (drow & 2)) + 8);
#pragma unroll
      for (int i = 0; i < 4; ++i) vo[i] = *LDSP(unsigned, LIST + (32 * t + 8 * i + drow) * 4) * 64u + dchunk;
    };
    auto issue_v = [&](int k) {
      asm volatile("s_waitcnt lgkmcnt(0)" ::: "memory");
      dma16x4(rs_sv, vo, 128 * k, TB + 4096 * k);
    };
    u32x4 rbf;
    auto calc_rbf = [&](int t) {
      FRESH_LANE;
      const float d = lo16(*LDSP(u16, LIST + O_D + 64 * t + 2 * l31));
      const f32x4 f0 = *LDSP(f32x4, L_FR + 32 * h), f1 = *LDSP(f32x4, L_FR + 32 * h + 16);
      const float inv = __builtin_amdgcn_rcpf(d);
#pragma unroll
      for (int i = 0; i < 4; ++i) {
        const float fa = i < 2 ? f0[2 * i] : f1[2 * i - 4], fb = i < 2 ? f0[2 * i + 1] : f1[2 * i - 3];
        const unsigned x = pk2(d * fa, d * fb);
        float ra = lo16(x) * 0.15915494309189535f, rc = hi16(x) * 0.15915494309189535f;
        ra -= __builtin_rintf(ra); rc -= __builtin_rintf(rc);
        rbf[i] = pk2(__builtin_amdgcn_sinf(ra) * inv, __builtin_amdgcn_sinf(rc) * inv);
      }
      asm volatile("" : "+v"(rbf));
    };
    setprio(3);
    issue_h(0);
    prep_v(0);
    STAMP(5);
    if (r2 == 0) {
      const unsigned lo = 16u * lane;
      const unsigned v4[4] = {lo, lo + 1024u, lo + 2048u, lo + 3072u};
      const int wo = min(wave * 4096, NFRAG * 1024 + 64 - 4096);
      dma16x4(rs_ws, v4, wo, L_W + wo);
      __builtin_amdgcn_s_barrier();
    }
    const bool late = r2 == 0 && lighter;
    if (!late) {
      issue_v(0); issue_v(1); issue_v(2);
      if (r2 == 0) asm volatile("s_waitcnt vmcnt(12)\n\ts_barrier" ::: "memory");
    }
    setprio(P_START);
    SB();
#pragma unroll
    for (int i = 0; i < 4; ++i) {
      if (i >= nstep) break;
      const float inv = __builtin_amdgcn_rcpf(hi16(ezd[i])), f = ef[i];
      const unsigned hxy = pk2(lo16(exy[i]) * inv, hi16(exy[i]) * inv);
      const float hz = rb(lo16(ezd[i]) * inv);
      const unsigned fxy = pk2(f * lo16(hxy), f * hi16(hxy));
      const unsigned fzf = pk2(f * hz, f);
      if (ep[i] != SENT) {
        const unsigned a2 = LIST + ep[i] * 2;
        *LDSP(u16, a2 + O_F) = (u16)(fzf >> 16);
        *LDSP(u16, a2 + O_FX) = (u16)fxy;
        *LDSP(u16, a2 + O_FX + 256) = (u16)(fxy >> 16);
        *LDSP(u16, a2 + O_FX + 512) = (u16)fzf;
      }
    }
    if (first) {
#pragma unroll
      for (int i = 0; i < 16; ++i)
#pragma unroll
        for (int k = 0; k < 4; ++k) {
          acc[i >> 2][(i >> 1) & 1][8 * (i & 1) + 2 * k] = lo16(own[i][k]);
          acc[i >> 2][(i >> 1) & 1][8 * (i & 1) + 2 * k + 1] = hi16(own[i][k]);
        }
      first = false;
    }
    SB();
    if (late) {
      asm volatile("s_waitcnt vmcnt(0)\n\ts_barrier" ::: "memory");
      issue_v(0); issue_v(1); issue_v(2);
    }
    calc_rbf(0);
    wait_h();
    STAMP(6);
    auto tile = [&](int t, auto nsc) {
      constexpr int NS = decltype(nsc)::value;
      FRESH_LANE;
      const unsigned WL = L0 + L_W + lane * 16, la = LIST + 64 * t + 16 * h;
      const unsigned mypair = (unsigned)(grp * 32 + l31 + 1) * 0x10001u;
      u32x4 Wb[5];
      auto ldW = [&](int ct) {
        Wb[4] = *LDSP(u32x4, WL + (24 + ct) * 1024);
#pragma unroll
        for (int ks = 0; ks < 4; ++ks) Wb[ks] = *LDSP(u32x4, WL + (4 * ct + ks) * 1024);
      };
      ldW(4);
      u32x4 msk[2], S1[2], sr[3][2];
#pragma unroll
      for (int s = 0; s < NS; ++s) msk[s] = *LDSP(u32x4, la + O_DL + 32 * s);
#pragma unroll
      for (int s = 0; s < NS; ++s)
#pragma unroll
        for (int k = 0; k < 3; ++k) sr[k][s] = *LDSP(u32x4, la + O_FX + 256 * k + 32 * s);
      SB();
      f32x16 ph, w;
      auto gemm = [&]() {
        w = mfma(rbf, Wb[4], zero16());
        ph = mfma(hN[0], Wb[0], zero16());
#pragma unroll
        for (int ks = 1; ks < 4; ++ks) ph = mfma(hN[ks], Wb[ks], ph);
      };
#define PIN1(a) asm volatile("" : "+v"(a))
#define PIN2(a, b) do { PIN1(a); PIN1(b); } while (0)
#define PIN3(a, b, c) asm volatile("" : "+v"(a), "+v"(b), "+v"(c))
      auto pack = [&](u32x4 (&a)[2]) {
        const f32x16 m = ph * w;
#pragma unroll
        for (int s = 0; s < NS; ++s) { a[s] = pack8(m, s); PIN1(a[s]); }
      };
#define MIX(id, lead, n, v) do { __builtin_amdgcn_sched_group_barrier(0x8, lead, id); _Pragma("unroll") for (int i_ = 0; i_ < n; ++i_) { \
        __builtin_amdgcn_sched_group_barrier(0x402, v, id); __builtin_amdgcn_sched_group_barrier(0x8, 1, id); } } while (0)
      u32x4 aa[2], ab[2];
      gemm();
#pragma unroll
      for (int s = 0; s < NS; ++s) {
        msk[s] = eqmask4(msk[s], mypair, 0x00010001u);
#pragma unroll
        for (int k = 0; k < 3; ++k) sr[k][s] &= msk[s];
      }
      MIX(0, 1, 4, 4 * NS);
      PIN2(ph, w);
      SB();
      ldW(5);
      SB();
      pack(aa);
      SB();
      gemm();
      PIN2(ph, w);
      SB();
      ldW(0);
      PIN2(aa[0], aa[NS - 1]);
      SB();
#pragma unroll
      for (int s = 0; s < NS; ++s)
#pragma unroll
        for (int k = 0; k < 3; ++k) acc[1 + k][0] = mfma(aa[s], sr[k][s], acc[1 + k][0]);
      pack(ab);
      MIX(1, 2, 3 * NS - 2, 6);
      PIN3(acc[1][0], acc[2][0], acc[3][0]);
      SB();
      gemm();
      PIN2(ph, w);
      SB();
      ldW(1);
      PIN2(ab[0], ab[NS - 1]);
      SB();
#pragma unroll
      for (int s = 0; s < NS; ++s)
#pragma unroll
        for (int k = 0; k < 3; ++k) acc[1 + k][1] = mfma(ab[s], sr[k][s], acc[1 + k][1]);
      pack(aa);
      MIX(2, 2, 3 * NS - 2, 6);
      PIN3(acc[1][1], acc[2][1], acc[3][1]);
      SB();
      unsigned tra, se, dn;
      f32x4 f0, f1;
      u32x4 rbn;
      {
        FRESH_LANE;
        const unsigned la2 = LIST + 64 * t + 16 * h, mp2 = (unsigned)(grp * 32 + l31 + 1) * 0x10001u;
#pragma unroll
        for (int s = 0; s < NS; ++s) { msk[s] = *LDSP(u32x4, la2 + O_DL + 32 * s); S1[s] = *LDSP(u32x4, la2 + O_F + 32 * s); }
        dn = *LDSP(u16, LIST + O_D + 64 * (t + 1) + 2 * l31);
        f0 = *LDSP(f32x4, L_FR + 32 * h); f1 = *LDSP(f32x4, L_FR + 32 * h + 16);
        se = *LDSP(unsigned, LIST + (32 * (t + 1) + l31) * 4);
#pragma unroll
        for (int i = 0; i < 4; ++i) vo[i] = *LDSP(unsigned, LIST + (32 * (t + 1) + 8 * i + (lane >> 3)) * 4);
        SB();
        gemm();
#pragma unroll
        for (int s = 0; s < NS; ++s) { S1[s] &= eqmask4(msk[s], mp2, 0x00010001u); PIN1(S1[s]); }
        const unsigned dchunk = 16u * (((lane & 7) ^ ((lane >> 3) & 2)) + 8);
#pragma unroll
        for (int i = 0; i < 4; ++i) { vo[i] = vo[i] * 64u + dchunk; PIN1(vo[i]); }
        se = se * 16u + 64u * h; PIN1(se);
        const int q4 = (lane >> 2) & 3, pp = lane & 3;
        tra = (q4 & 1) ? TB + (4 * h + (q4 >> 1)) * 128 + 64 * (pp & 1) + (lane & 16) + 8 * (pp >> 1) : 0x100000u;
        PIN1(tra);
        MIX(3, 2, 3, 8);
        PIN2(ph, w);
        SB();
      }
      ldW(2);
      PIN2(aa[0], aa[NS - 1]);
      SB();
#pragma unroll
      for (int s = 0; s < NS; ++s) acc[0][0] = mfma(aa[s], S1[s], acc[0][0]);
      PIN3(acc[0][0], ph, w);
      SB();
      pack(ab);
      SB();
      const float dnf = lo16(dn), inv = __builtin_amdgcn_rcpf(dnf);
      auto rbf2 = [&](float fa, float fb) {
        const unsigned x = pk2(dnf * fa, dnf * fb);
        float ra = lo16(x) * 0.15915494309189535f, rc = hi16(x) * 0.15915494309189535f;
        ra -= __builtin_rintf(ra); rc -= __builtin_rintf(rc);
        return pk2(__builtin_amdgcn_sinf(ra) * inv, __builtin_amdgcn_sinf(rc) * inv);
      };
      gemm();
      { unsigned r0 = rbf2(f0[0], f0[1]), r1_ = rbf2(f0[2], f0[3]); PIN2(r0, r1_); rbn[0] = r0; rbn[1] = r1_; }
      MIX(4, 1, 4, 6);
      PIN2(ph, w);
      SB();
      ldW(3);
      PIN2(ab[0], ab[NS - 1]);
      SB();
#pragma unroll
      for (int s = 0; s < NS; ++s) acc[0][1] = mfma(ab[s], S1[s], acc[0][1]);
      PIN3(acc[0][1], ph, w);
      SB();
      f32x16 mv[2];
      mv[0] = ph * w;
      PIN1(mv[0]);
      SB();
      gemm();
      { unsigned r0 = rbf2(f1[0], f1[1]), r1_ = rbf2(f1[2], f1[3]); PIN2(r0, r1_); rbn[2] = r0; rbn[3] = r1_; }
      MIX(5, 1, 4, 6);
      PIN2(ph, w);
      SB();
      issue_h2(se);
      rbf = rbn;
      mv[1] = ph * w;
      PIN1(mv[1]);
      SB();
      P_PH2;
      asm volatile("s_waitcnt vmcnt(4)" ::: "memory");
      if (t < 2) STAMP(7 + 2 * t);
      u32x2 va[8], vb[8];
      auto ldv = [&](u32x2 (&v)[8], int t2, int k) {
#pragma unroll
        for (int i = 0; i < 4 * NS; ++i)
          v[i] = trread(tra + k * 4096 + (8 * (i >> 1) + 2 * (i & 1)) * 128 + 32 * (t2 ^ (i & 1)));
      };
      auto vv = [&](const u32x2 (&v)[8], const f32x16& m, f32x16& ac) {
        u32x4 a[2];
#pragma unroll
        for (int i = 0; i < 4 * NS; ++i) {
          const unsigned v0 = v[i][0], v1 = v[i][1];
          a[i >> 2][i & 3] = pk2(m[2 * i] * __uint_as_float(v0), m[2 * i + 1] * __uint_as_float(v1));
        }
#pragma unroll
        for (int s = 0; s < NS; ++s) ac = mfma(a[s], S1[s], ac);
      };
      ldv(va, 0, 0); ldv(vb, 1, 0);
      SB();
      vv(va, mv[0], acc[1][0]);
      SB();
      issue_v(0);
      ldv(va, 0, 1);
      SB();
      vv(vb, mv[1], acc[1][1]);
      SB();
      ldv(vb, 1, 1);
      SB();
      vv(va, mv[0], acc[2][0]);
      SB();
      issue_v(1);
      ldv(va, 0, 2);
      SB();
      vv(vb, mv[1], acc[2][1]);
      SB();
      ldv(vb, 1, 2);
      SB();
      vv(va, mv[0], acc[3][0]);
      SB();
      issue_v(2);
      if (t < 2) STAMP(8 + 2 * t);
      vv(vb, mv[1], acc[3][1]);
      SB();
      wait_h();
    };
    const int nfull = (cnt + 15) >> 5;
    for (int t = 0; t < nfull; ++t) { tile(t, std::integral_constant<int, 2>{}); setprio(P_NEXT); }
    if (nfull < ntile) tile(nfull, std::integral_constant<int, 1>{});
    if (nact <= CAP * (r2 + 1)) break;
    asm volatile("s_waitcnt vmcnt(0)" ::: "memory");
#pragma unroll
    for (int i = 0; i < 13; ++i) asm volatile("" : "=v"(ol[i]));
    }
    if (more1) ++r1;
    else if (moreb) { ++batch; r1 = 0; }
    else break;
    asm volatile("s_waitcnt vmcnt(0)" ::: "memory");
    wg_barrier();
  }
  STAMP(13);
  {
    FRESH_LANE;
    u32x4 o[16];
#pragma unroll
    for (int i = 0; i < 16; ++i) o[i] = pack8(acc[i >> 2][(i >> 1) & 1], i & 1);
    asm volatile("s_waitcnt vmcnt(0)" ::: "memory");
    if (l31 < GN) {
      const unsigned wa = TB + l31 * 512 + 16 * ((4 * h) ^ (l31 & 7));
#pragma unroll
      for (int i = 0; i < 16; ++i) *LDSP(u32x4, (wa ^ (16 * (i & 3))) + 128 * (i >> 2)) = o[i];
    }
    const int gw = __builtin_amdgcn_readfirstlane(max(min(nb - grp * GN, GN), 0));
    const uint64_t op = (uint64_t)out + (size_t)(node0 + grp * GN) * 512;
    const __amdgpu_buffer_rsrc_t rb_o = __builtin_amdgcn_make_buffer_rsrc((void*)op, 0, NOST * gw * 512, 0x00020000);
    u32x4 v[(GN + 1) / 2];
#pragma unroll
    for (int i = 0; i < (GN + 1) / 2; ++i) v[i] = *LDSP(u32x4, TB + i * 1024 + lane * 16);
    const unsigned oa = h * 512 + 16 * (l31 ^ h);
#pragma unroll
    for (int i = 0; i < (GN + 1) / 2; ++i) __builtin_amdgcn_raw_buffer_store_b128(v[i], rb_o, oa ^ (32 * (i & 3)), i * 1024, K2AUX);
  }
  asm volatile("painn_code_hi:");
  STAMP(14);
  STAMP(15);
}

#if defined(__HIP_DEVICE_COMPILE__)
#pragma clang attribute pop
#endif
extern "C" void kernel_launch(void* const* d_in, const int* in_sizes, int n_in, void* d_out, int out_size, void* d_ws, size_t ws_size,
                              hipStream_t stream) {
  const u16* sv = (const u16*)d_in[0];
  const u16* pos = (const u16*)d_in[1];
  const int* src = (const int*)d_in[2];
  const int* dst = (const int*)d_in[3];
  const u16* fr = (const u16*)d_in[4];
  const u16* w1 = (const u16*)d_in[5];
  const u16* w2 = (const u16*)d_in[6];
  const u16* wr = (const u16*)d_in[7];
  const int N = in_sizes[1] / 3, E = in_sizes[2];
  const int BN = (N + 255) / 256 < 2 ? 2 : (N + 255) / 256;
  const int B = (N + BN - 1) / BN, bpx = (B + 7) / 8;
#define HWN 200
#define WTGT 196
  int chunk = (((E + WTGT - 1) / WTGT) + 3) & ~3;
  if (chunk < 1024) chunk = 1024;
  const int W = (E + chunk - 1) / chunk;
  const int NPX = bpx * BN, HW = HWN, WP = (W + 7) & ~7;
  const unsigned magicBN = (unsigned)(0x100000000ull / (unsigned)BN) + 1u;
  char* ws = (char*)d_ws;
  (void)hipFuncSetAttribute((const void*)k_agg, hipFuncAttributeMaxDynamicSharedMemorySize, L_END);
  k_bin<<<WP + HW + 8, 512, 0, stream>>>(src, dst, (unsigned*)(ws + WS_T), (u32x4*)(ws + WS_SEG), sv, pos, w1, w2, wr, fr, (u32x4*)ws,
                                        (u32x2*)(ws + WS_P), (u16*)(ws + WS_C), ws + WS_H, N, E, W, HW, magicBN, BN, WP, NPX, chunk);
  k_agg<<<8 * bpx, 512, L_END, stream>>>(sv, ws, (u16*)d_out, N, W, BN, B, bpx);
}
```

```cpp
#if defined(__HIP_DEVICE_COMPILE__)
#pragma clang attribute push(__attribute__((target("no-packed-fp32-ops"))), apply_to = function)
#endif
#include <hip/hip_runtime.h>
#include <stdint.h>
#include <type_traits>

#define TID __builtin_amdgcn_workitem_id_x()
#define BID __builtin_amdgcn_workgroup_id_x()
typedef unsigned short u16;
typedef __attribute__((ext_vector_type(8))) __bf16 bf16x8;
typedef __attribute__((ext_vector_type(2))) __bf16 bf16x2;
typedef __attribute__((ext_vector_type(4))) short s16x4;
typedef __attribute__((ext_vector_type(2))) float f32x2;
typedef __attribute__((ext_vector_type(4))) float f32x4;
typedef __attribute__((ext_vector_type(16))) float f32x16;
typedef __attribute__((ext_vector_type(4))) unsigned u32x4;
typedef __attribute__((ext_vector_type(3))) unsigned u32x3;
typedef __attribute__((ext_vector_type(2))) unsigned u32x2;
#define LDSP(T, off) ((__attribute__((address_space(3))) T*)(uintptr_t)(off))
#define DI __device__ __forceinline__
#define SB() __builtin_amdgcn_sched_barrier(0)

constexpr int WT = 17;
constexpr int CH = 4096;
constexpr int SEGQ = CH / 4 + 192;
constexpr int GN = 25;
constexpr int NFRAG = 30;
constexpr int SUBC = 64;
constexpr int CAP = 128;
constexpr int BQ = 256;
constexpr unsigned SENT = 0xFFFFFFFFu;
constexpr int L_DPOS = 0;
constexpr int L_CNT = 4096;
constexpr int L_NB = L_CNT + 256;
constexpr int L_RAW = L_NB + 64;
constexpr int L_LIST = L_RAW + 1024;
constexpr int O_D = 640, O_DL = O_D + 256, O_F = O_DL + 256, O_FX = O_F + 256;
constexpr int LISTS = 1024 + O_FX + 768;
constexpr int L_W = L_RAW + 8 * LISTS;
constexpr int L_FR = L_W + NFRAG * 1024;
constexpr int L_TB = (L_FR + 64 + 511) / 512 * 512;
constexpr int TBS = GN * 512;
constexpr int L_END = L_TB + 8 * TBS;
static_assert(L_END <= 163840 && L_TB < 65536 && L_W % 16 == 0 && LISTS % 16 == 0 && SUBC == 64, "LDS");
constexpr size_t WS_T = 65536;
constexpr size_t WS_SEG = WS_T + 256 * 256 * 4;
constexpr size_t WS_P = WS_SEG + (size_t)256 * SEGQ * 16;
constexpr size_t WS_C = WS_P + 51200 * 8;
constexpr size_t WS_H = WS_C + 51200 * 2;
constexpr size_t WS_END = WS_H + 51200 * 128;
constexpr size_t WS_F = NFRAG * 1024;
static_assert(WS_END < 0xffffff, "ws");

DI float lo16(unsigned x) { return __uint_as_float(x << 16); }
DI float hi16(unsigned x) { return __uint_as_float(x & 0xffff0000u); }
DI unsigned pk2(float a, float b) { f32x2 v = {a, b}; return __builtin_bit_cast(unsigned, __builtin_convertvector(v, bf16x2)); }
DI float rb(float a) { return hi16(pk2(a, a)); }
DI f32x16 mfma(u32x4 a, u32x4 b, f32x16 c) {
  return __builtin_amdgcn_mfma_f32_32x32x16_bf16(__builtin_bit_cast(bf16x8, a), __builtin_bit_cast(bf16x8, b), c, 0, 0, 0);
}
DI f32x16 zero16() { f32x16 z;
#pragma unroll
  for (int r = 0; r < 16; ++r) z[r] = 0.f;
  return z; }
DI int wave_incl_scan(int v) {
  v += __builtin_amdgcn_update_dpp(0, v, 0x111, 0xf, 0xf, false);
  v += __builtin_amdgcn_update_dpp(0, v, 0x112, 0xf, 0xf, false);
  v += __builtin_amdgcn_update_dpp(0, v, 0x114, 0xf, 0xf, false);
  v += __builtin_amdgcn_update_dpp(0, v, 0x118, 0xf, 0xf, false);
  v += __builtin_amdgcn_update_dpp(0, v, 0x142, 0xa, 0xf, false);
  v += __builtin_amdgcn_update_dpp(0, v, 0x143, 0xc, 0xf, false);
  return v;
}
DI int chan_of(int t2, int i32) { return 32 * ((i32 >> 2) & 1) + 16 * t2 + 4 * (i32 >> 3) + (i32 & 3); }
DI u32x4 pack8(const f32x16& x, int s) {
  return u32x4{pk2(x[8 * s], x[8 * s + 1]), pk2(x[8 * s + 2], x[8 * s + 3]), pk2(x[8 * s + 4], x[8 * s + 5]), pk2(x[8 * s + 6], x[8 * s + 7])};
}
DI void setprio(int p) {
  if (p >= 3) __builtin_amdgcn_s_setprio(3);
  else if (p == 2) __builtin_amdgcn_s_setprio(2);
  else if (p == 1) __builtin_amdgcn_s_setprio(1);
  else __builtin_amdgcn_s_setprio(0);
}
DI u32x2 trread(unsigned a) { return __builtin_bit_cast(u32x2, __builtin_amdgcn_ds_read_tr16_b64_v4i16(LDSP(s16x4, a))); }
DI void st16(void* base, unsigned off, u32x4 v) {
  const __amdgpu_buffer_rsrc_t r = __builtin_amdgcn_make_buffer_rsrc(base, 0, 0x7fffffff, 0x00020000);
  __builtin_amdgcn_raw_buffer_store_b128(v, r, off, 0, WT);
}
DI unsigned qmod(float x) { return min((unsigned)(__builtin_amdgcn_fractf(x * 0.0625f) * 65536.0f), 65535u); }
DI unsigned opq(unsigned c) { asm volatile("" : "+v"(c)); return c; }
DI unsigned swap23(unsigned p) { return (p & ~12u) | ((p & 4u) << 1) | ((p & 8u) >> 1); }
DI void wg_barrier() { asm volatile("s_waitcnt lgkmcnt(0)\n\ts_barrier" ::: "memory"); }

DI void dma16(u32x4 rsrc, unsigned voff, unsigned ldsaddr) {
  asm volatile("s_mov_b32 m0, %2\n\ts_nop 3\n\tbuffer_load_dwordx4 %0, %1, 0 offen lds" ::"v"(voff), "s"(rsrc), "s"(ldsaddr) : "memory");
}
DI void dma16x4(u32x4 rsrc, const unsigned (&voff)[4], unsigned soff, unsigned m0) {
  asm volatile("s_mov_b32 m0, %6\n\ts_nop 3\n\tbuffer_load_dwordx4 %0, %4, %5 offen lds\n\t"
               "s_add_u32 m0, m0, 0x400\n\ts_nop 0\n\tbuffer_load_dwordx4 %1, %4, %5 offen lds\n\t"
               "s_add_u32 m0, m0, 0x400\n\ts_nop 0\n\tbuffer_load_dwordx4 %2, %4, %5 offen lds\n\t"
               "s_add_u32 m0, m0, 0x400\n\ts_nop 0\n\tbuffer_load_dwordx4 %3, %4, %5 offen lds"
               ::"v"(voff[0]), "v"(voff[1]), "v"(voff[2]), "v"(voff[3]), "s"(rsrc), "s"(soff), "s"(m0) : "memory", "scc");
}
constexpr int K1_CNT = 0, K1_STAGE = 8 * 256 * 4, K1_TOT = K1_STAGE + SEGQ * 16, K1_W1 = 0, K1_HT = 8192, K1_END = K1_HT + 8 * 4096;
#if defined(__HIP_DEVICE_COMPILE__)
#pragma clang attribute pop
#endif
extern "C" __global__ __launch_bounds__(512, 4) void k_bin(const int* __restrict__ src, const int* __restrict__ dst, const u16* __restrict__ sv,
                                                           const u16* __restrict__ pos, const u16* __restrict__ w1, char* __restrict__ ws,
                                                           unsigned shapeN, int E, unsigned magicBN, unsigned shapeE, const u16* __restrict__ w2,
                                                           const u16* __restrict__ wr, const u16* __restrict__ freqs) {
  asm volatile("" ::"s"(src), "s"(dst), "s"(sv), "s"(pos), "s"(w1), "s"(ws), "s"(shapeN), "s"(E), "s"(magicBN), "s"(shapeE));
  const int N = shapeN & 0xffff, BN = (shapeN >> 16) & 0xff, NPX = (shapeN >> 24) * BN, chunk = shapeE & 0xffff, W = shapeE >> 16, WP = (W + 7) & ~7;
  constexpr int HW = 200;
  unsigned* T = (unsigned*)(ws + WS_T);
  u32x4* seg = (u32x4*)(ws + WS_SEG);
  u32x4* wf = (u32x4*)ws;
  u32x2* ptab = (u32x2*)(ws + WS_P);
  u16* ctab = (u16*)(ws + WS_C);
  char* htab = ws + WS_H;
  __shared__ __attribute__((aligned(16))) char lds_raw[K1_END + 256];
  const unsigned L0 = (unsigned)(uintptr_t)(__attribute__((address_space(3))) char*)lds_raw;
  const int tid = TID, lane = tid & 63;
  const int wave = __builtin_amdgcn_readfirstlane(tid >> 6);
  int bid = BID;
  if (bid < WP + HW) { if (bid < WP) { if (bid >= W) return; bid += HW; } else bid -= WP; } else bid += W - WP;
  if (wave == 0) {
    uint64_t pc; unsigned len;
    asm volatile("s_getpc_b64 %0\npainn_self_lo:\n\ts_mov_b32 %1, painn_code_hi-painn_self_lo" : "=s"(pc), "=s"(len));
    const u32x4 rc = {(unsigned)pc, (unsigned)(pc >> 32) & 0xffffu, len, 0x00020000u};
    asm volatile("s_mov_b32 m0, %2\n\ts_nop 3\n\tbuffer_load_dword %0, %1, 0 offen lds" ::"v"(lane * 128), "s"(rc), "s"(L0 + K1_END) : "memory");
  }
  if (bid >= W + HW) {
    {
      uint64_t pc; unsigned len;
      asm volatile("s_getpc_b64 %0\npainn_code_lo:\n\ts_mov_b32 %1, painn_code_hi-painn_code_lo" : "=s"(pc), "=s"(len));
      const __amdgpu_buffer_rsrc_t rc = __builtin_amdgcn_make_buffer_rsrc((void*)pc, 0, len, 0x00020000);
      const unsigned x = __builtin_amdgcn_raw_buffer_load_b32(rc, tid * 64, 0, 0);
      asm volatile("" ::"v"(x));
    }
    const int idx = (bid - W - HW) * 512 + tid;
    if (idx >= NFRAG * 64) {
      if (idx < NFRAG * 64 + 16) ((unsigned*)wf)[WS_F / 4 + idx - NFRAG * 64] = (unsigned)freqs[idx - NFRAG * 64] << 16;
      return;
    }
    const int f = idx >> 6, i32 = lane & 31, h = lane >> 5;
    const u16* p; int s2;
    if (f < 24) { const int ct = f >> 2; p = w2 + (16 * (f & 3) + 4 * h) * 192 + 64 * (ct >> 1) + chan_of(ct & 1, i32); s2 = 8 * 192; }
    else { const int ct = f - 24; p = wr + 8 * h * 192 + 64 * (ct >> 1) + chan_of(ct & 1, i32); s2 = 4 * 192; }
    unsigned v[8];
#pragma unroll
    for (int j = 0; j < 8; ++j) v[j] = p[(j & 3) * 192 + (j >> 2) * s2];
    wf[idx] = u32x4{v[0] | v[1] << 16, v[2] | v[3] << 16, v[4] | v[5] << 16, v[6] | v[7] << 16};
    return;
  }
  if (bid < HW) {
    const int l31 = lane & 31, h = lane >> 5;
    const int nl = (wave * (HW >> 3) + (bid >> 3)) * 32, nend = min(N, ((bid & 7) + 1) * NPX);
    const int n0 = nl < NPX ? (bid & 7) * NPX + nl : N;
    const int nn = min(n0 + l31, N - 1);
    const unsigned HT = L0 + K1_HT + wave * 4096;
    {
      const uint64_t sp = (uint64_t)sv, wp = (uint64_t)w1;
      const u32x4 rs_sv = {(unsigned)sp, (unsigned)(sp >> 32), (unsigned)N * 512u, 0x00020000u};
      const u32x4 rs_w1 = {(unsigned)wp, (unsigned)(wp >> 32), 8192u, 0x00020000u};
      const unsigned o = (unsigned)(n0 + (lane >> 3)) * 512u + 16u * ((lane & 7) ^ (lane >> 3));
      const unsigned v4[4] = {o, o + 4096u, o + 8192u, o + 12288u};
      dma16x4(rs_sv, v4, 0, HT);
      dma16(rs_w1, tid * 16u, L0 + K1_W1 + wave * 1024);
    }
    unsigned px = pos[nn * 3], py = pos[nn * 3 + 1], pz = pos[nn * 3 + 2];
    asm volatile("s_waitcnt vmcnt(0)\n\ts_barrier" : "+v"(px), "+v"(py), "+v"(pz)::"memory");
    if (n0 >= N) return;
    u32x4 sf[4];
#pragma unroll
    for (int ks = 0; ks < 4; ++ks) sf[ks] = *LDSP(u32x4, HT + l31 * 128 + 16 * ((4 * h + ks) ^ (l31 & 7)));
    const unsigned ta = L0 + K1_W1 + (32 * h + ((lane >> 2) & 3)) * 128 + (16 * ((lane >> 4) & 1) + 4 * (lane & 3)) * 2;
    f32x16 d1[2];
#pragma unroll
    for (int ht = 0; ht < 2; ++ht) {
      d1[ht] = zero16();
#pragma unroll
      for (int ks = 0; ks < 4; ++ks) {
        const u32x2 a0 = trread(ta + ks * 1024 + ht * 64), a1 = trread(ta + ks * 1024 + 512 + ht * 64);
        d1[ht] = mfma(u32x4{a0[0], a0[1], a1[0], a1[1]}, sf[ks], d1[ht]);
      }
    }
#pragma unroll
    for (int ht = 0; ht < 2; ++ht) {
      f32x16 e = d1[ht] * -1.4426950408889634f;
#pragma unroll
      for (int r = 0; r < 16; ++r) e[r] = __builtin_amdgcn_exp2f(e[r]);
      e += 1.0f;
#pragma unroll
      for (int r = 0; r < 16; ++r) e[r] = __builtin_amdgcn_rcpf(e[r]);
      e *= d1[ht];
#pragma unroll
      for (int s = 0; s < 2; ++s) *LDSP(u32x4, HT + l31 * 128 + 16 * ((4 * h + 2 * ht + s) ^ (l31 & 7))) = pack8(e, s);
    }
#pragma unroll
    for (int i = 0; i < 4; ++i) {
      const int n = 8 * i + (lane >> 3);
      const u32x4 v = *LDSP(u32x4, HT + i * 1024 + lane * 16);
      if (n0 + n < nend) st16(htab, (n0 + n) * 128 + 16 * ((lane & 7) ^ (n & 7)), v);
    }
    if (h == 0 && n0 + l31 < nend) {
      ptab[n0 + l31] = u32x2{px | py << 16, pz << 16};
      ctab[n0 + l31] = (u16)(qmod(lo16(px)) >> 11 | (qmod(lo16(py)) >> 11) << 5 | (qmod(lo16(pz)) & 0xfc00u));
    }
    return;
  }
  const int cw = bid - HW;
  const int eend = min(E, (cw + 1) * chunk);
  const __amdgpu_buffer_rsrc_t rb_s = __builtin_amdgcn_make_buffer_rsrc((void*)src, 0, eend * 4, 0x00020000);
  const __amdgpu_buffer_rsrc_t rb_d = __builtin_amdgcn_make_buffer_rsrc((void*)dst, 0, eend * 4, 0x00020000);
  const int e0 = cw * chunk + tid * 4;
  u32x4 s4[2], d4[2];
#pragma unroll
  for (int u = 0; u < 2; ++u) d4[u] = __builtin_amdgcn_raw_buffer_load_b128(rb_d, (e0 + u * 2048) * 4, 0, 0);
#pragma unroll
  for (int u = 0; u < 2; ++u) s4[u] = __builtin_amdgcn_raw_buffer_load_b128(rb_s, (e0 + u * 2048) * 4, 0, 0);
  *LDSP(u32x4, L0 + K1_CNT + wave * 1024 + lane * 16) = u32x4{0, 0, 0, 0};
#pragma unroll
  for (int i = 0; i < 3; ++i) if (i * 512 + tid < SEGQ) *LDSP(u32x4, L0 + K1_STAGE + (i * 512 + tid) * 16) = u32x4{0, 0, 0, 0};
  unsigned ca[8], hi8[8], rank[8];
#pragma unroll
  for (int k = 0; k < 8; ++k) {
    const unsigned d = d4[k >> 2][k & 3];
    const unsigned b = (unsigned)(((uint64_t)d * magicBN) >> 32);
    ca[k] = L0 + K1_CNT + wave * 1024 + b * 4;
    rank[k] = SENT;
    if (e0 + (k >> 2) * 2048 + (k & 3) < eend) rank[k] = __hip_atomic_fetch_add(LDSP(unsigned, ca[k]), 1u, __ATOMIC_RELAXED, __HIP_MEMORY_SCOPE_WORKGROUP);
    const unsigned dl = d - b * (unsigned)BN;
    hi8[k] = dl + ((dl * 41u) >> 10) * 7u + 1u;
  }
  wg_barrier();
  if (wave == 0) {
    u32x4 c[8], tot = {0, 0, 0, 0};
#pragma unroll
    for (int w = 0; w < 8; ++w) { c[w] = *LDSP(u32x4, L0 + K1_CNT + w * 1024 + lane * 16); tot += c[w]; }
    const u32x4 q = (tot + 3u) >> 2;
    const unsigned ls = q[0] + q[1] + q[2] + q[3];
    const unsigned incl = wave_incl_scan(ls);
    u32x4 st; st[0] = incl - ls; st[1] = st[0] + q[0]; st[2] = st[1] + q[1]; st[3] = st[2] + q[2];
    u32x4 run = st * 4u;
#pragma unroll
    for (int w = 0; w < 8; ++w) { *LDSP(u32x4, L0 + K1_CNT + w * 1024 + lane * 16) = run; run += c[w]; }
    if (lane == 63) *LDSP(unsigned, L0 + K1_TOT) = incl;
    st16(T, cw * 1024 + 16 * lane, st | (q << 16));
  }
  wg_barrier();
  unsigned base[8];
#pragma unroll
  for (int k = 0; k < 8; ++k) base[k] = *LDSP(unsigned, ca[k]);
#pragma unroll
  for (int k = 0; k < 8; ++k)
    if (rank[k] != SENT) *LDSP(unsigned, L0 + K1_STAGE + (base[k] + rank[k]) * 4) = s4[k >> 2][k & 3] << 3 | hi8[k] << 24;
  const int total = *LDSP(int, L0 + K1_TOT);
  wg_barrier();
#pragma unroll
  for (int i = 0; i < 3; ++i) {
    const int qd = i * 512 + tid;
    if (qd < total) st16(seg, (cw * SEGQ + qd) * 16, *LDSP(u32x4, L0 + K1_STAGE + qd * 16));
  }
}

#if defined(__HIP_DEVICE_COMPILE__)
#pragma clang attribute push(__attribute__((target("no-packed-fp32-ops"))), apply_to = function)
#endif
DI u32x4 eqmask4(u32x4 a, unsigned b, unsigned ones) {
  u32x4 x;
#pragma unroll
  for (int i = 0; i < 4; ++i) x[i] = a[i] ^ b;
  asm("v_pk_min_u16 %0, %0, %4\n\tv_pk_min_u16 %1, %1, %4\n\tv_pk_min_u16 %2, %2, %4\n\tv_pk_min_u16 %3, %3, %4\n\t"
      "v_pk_sub_u16 %0, %0, %4\n\tv_pk_sub_u16 %1, %1, %4\n\tv_pk_sub_u16 %2, %2, %4\n\tv_pk_sub_u16 %3, %3, %4"
      : "+v"(x[0]), "+v"(x[1]), "+v"(x[2]), "+v"(x[3]) : "s"(ones));
  return x;
}

typedef __attribute__((ext_vector_type(2))) short s16x2;
extern "C" __global__ __launch_bounds__(512, 2) void k_agg(const u16* __restrict__ sv, const char* __restrict__ wf, u16* __restrict__ out, int N,
                                                           int W, int BN, int B, int bpx) {
  asm volatile("" ::"s"(sv), "s"(wf), "s"(out), "s"(N), "s"(W), "s"(BN), "s"(B), "s"(bpx));
  const int wave = __builtin_amdgcn_readfirstlane(TID >> 6);
  const int b = ((int)BID & 7) * bpx + ((int)BID >> 3);
  if (b >= B) return;
  constexpr unsigned L0 = 0;
  const int node0 = b * BN;
  const int nb = min(BN, N - node0);
  const uint64_t svp = (uint64_t)sv, wfp = (uint64_t)wf;
  const u32x4 rs_sv = {(unsigned)svp, (unsigned)(svp >> 32), (unsigned)N * 512u, 0x00020000u};
  const u32x4 rs_ws = {(unsigned)wfp, (unsigned)(wfp >> 32), (unsigned)WS_END, 0x00020000u};
#define FRESH_LANE int lane; asm volatile("v_mbcnt_lo_u32_b32 %0, -1, 0\n\tv_mbcnt_hi_u32_b32 %0, -1, %0" : "=v"(lane)); const int l31 = lane & 31, h = lane >> 5; (void)l31; (void)h
  const unsigned TB = L0 + L_TB + wave * TBS;

  f32x16 acc[4][2];
  u32x4 hN[4];
  int batch = 0, r1 = 0, r2 = 0;
  bool first = true;
  const int grp = wave;
  while (true) {
    u32x4 ol[13];
    {
      FRESH_LANE;
      const unsigned lo = 16u * lane;
      const unsigned v4[4] = {lo, lo + 1024u, lo + 2048u, lo + 3072u};
      const unsigned oo = first ? (unsigned)(node0 + wave * GN + h) * 512u + 16u * (l31 ^ h) : 0x7fff0000u;
#define OWNA(i) ((oo ^ (32u * ((i) & 7))) + 1024u * (i))
#define OWN4(j) asm volatile("s_nop 4\n\tbuffer_load_dwordx4 %0, %4, %8, 0 offen\n\tbuffer_load_dwordx4 %1, %5, %8, 0 offen\n\t" \
                             "buffer_load_dwordx4 %2, %6, %8, 0 offen\n\tbuffer_load_dwordx4 %3, %7, %8, 0 offen" \
                             : "=&v"(ol[4 * j]), "=&v"(ol[4 * j + 1]), "=&v"(ol[4 * j + 2]), "=&v"(ol[4 * j + 3]) \
                             : "v"(OWNA(4 * j)), "v"(OWNA(4 * j + 1)), "v"(OWNA(4 * j + 2)), "v"(OWNA(4 * j + 3)), "s"(rs_sv) : "memory")
#define OWN1() asm volatile("s_nop 4\n\tbuffer_load_dwordx4 %0, %1, %2, 0 offen" : "=&v"(ol[12]) : "v"(h ? 0x7fff0000u : OWNA(12)), "s"(rs_sv) : "memory")
      if (wave >= 4) {
        const int rw = wave - 4;
        const unsigned MAP = L_LIST + wave * LISTS + 1024;
        const int cpw = (W + 3) >> 2;
        const int cell = rw * cpw + lane;
        unsigned t;
        u32x2 dp;
        asm volatile("s_nop 4\n\tbuffer_load_dword %0, %2, %4, 0 offen\n\tbuffer_load_dwordx2 %1, %3, %4, 0 offen\n\ts_barrier\n\ts_waitcnt vmcnt(0)"
                     : "=&v"(t), "=&v"(dp) : "v"((lane < cpw && cell < W) ? (int)WS_T + (cell * 256 + b) * 4 : 0x7fffffff),
                       "v"((int)WS_P + (node0 + (2 * rw + h) * GN + l31 - 1) * 8), "s"(rs_ws) : "memory");
        const int q = t >> 16;
        const int incl = wave_incl_scan(q), excl = incl - q;
        const int total = __builtin_amdgcn_readlane(incl, 63);
        const int win0 = batch * BQ;
        const int gq = (int)(WS_SEG / 16) + cell * SEGQ + (int)(t & 0xffffu) - excl;
        const int hi = min(incl, win0 + BQ);
#pragma nounroll
        for (int i = max(excl, win0); i < hi; ++i) *LDSP(int, MAP + (i - win0) * 4) = (gq + i) * 16;
        unsigned mq[4];
#pragma unroll
        for (int s = 0; s < 4; ++s) mq[s] = *LDSP(int, MAP + (64 * s + lane) * 4);
#pragma unroll
        for (int s = 0; s < 4; ++s) if (win0 + 64 * s + lane >= total) mq[s] = 0x7fffffffu;
        dma16x4(rs_ws, mq, 0, L_W + rw * 4096);
        if (lane == 0) *LDSP(int, L_NB + rw * 4) = (total + BQ - 1) / BQ;
        const unsigned qx = (qmod(lo16(dp[0])) - 1024u) & 0xffffu, qy = qmod(hi16(dp[0])) - 1024u, qz = (qmod(__uint_as_float(dp[1])) - 512u) & 0xffffu;
        *LDSP(u32x4, L_DPOS + rw * 1024 + lo) = (unsigned)(l31 - 1) < (unsigned)GN ? u32x4{dp[0], dp[1], qx | qy << 16, qz} : u32x4{0x7fc07fc0u, 0x7fc00000u, 0u, 0x7fff0000u};
      } else {
        __builtin_amdgcn_s_barrier();
#pragma unroll
        for (int i = 0; i < 6; ++i) dma16x4(rs_ws, v4, WS_C + (wave * 6 + i) * 4096, L_TB + (wave * 6 + i) * 4096);
        dma16(rs_ws, WS_C + (96 + wave) * 1024u + lo, L_TB + (96 + wave) * 1024);
      }
      if (lane < 8) *LDSP(unsigned, L_CNT + lane * 32 + wave * 4) = lane >> 3;
      { const unsigned z = opq(0u); *LDSP(u32x4, L_RAW + wave * LISTS + lo) = u32x4{z, z, z, z}; *LDSP(u32x4, L_LIST + wave * LISTS + lo) = u32x4{z, z, z, z}; }
      OWN4(0); OWN1();
      asm volatile("s_waitcnt vmcnt(5)" ::: "memory");
      wg_barrier();
      u32x4 r4[2];
#pragma unroll
      for (int s = 0; s < 2; ++s) r4[s] = *LDSP(u32x4, L_W + (2 * s + h) * 4096 + (l31 >> 2) * 512 + wave * 64 + (lane & 3) * 16);
      OWN4(1);
      const unsigned one = opq(1u);
#pragma unroll
      for (int s = 0; s < 2; ++s) {
        unsigned cd[4];
        u32x2 dq[4];
#pragma unroll
        for (int c = 0; c < 4; ++c) {
          cd[c] = *LDSP(u16, L_TB + ((r4[s][c] >> 2) & 0x3fffffu));
          dq[c] = *LDSP(u32x2, L_DPOS + 8 + ((r4[s][c] >> 20) & 0xff0u));
        }
        unsigned rank[4];
#pragma unroll
        for (int c = 0; c < 4; ++c) {
          const unsigned cxy = ((cd[c] & 0x3ffu) * 0x400800u) & 0xf800f800u, cz = cd[c] & 0xfc00u;
          const unsigned qxy = dq[c][0], qz = dq[c][1];
          const s16x2 dxy = __builtin_bit_cast(s16x2, qxy) - __builtin_bit_cast(s16x2, cxy);
          const s16x2 dz = __builtin_bit_cast(s16x2, qz) - __builtin_bit_cast(s16x2, cz);
          const unsigned d2 = __builtin_amdgcn_sdot2(dxy, dxy, __builtin_amdgcn_sdot2(dz, dz, 0, false), false);
          rank[c] = 0x7fffffffu;
          if (d2 < 488400000u)
            rank[c] = __hip_atomic_fetch_add(LDSP(unsigned, L_CNT + (((r4[s][c] >> 24) & 0xe0u) | wave * 4)), one, __ATOMIC_RELAXED, __HIP_MEMORY_SCOPE_WORKGROUP);
        }
        if (s == 0) OWN4(2);
#pragma unroll
        for (int c = 0; c < 4; ++c) {
          const unsigned p = rank[c] - SUBC * r1;
          if (p < (unsigned)SUBC) *LDSP(unsigned, L_RAW + (r4[s][c] >> 29) * LISTS + (p * 32 + wave * 4)) = r4[s][c];
        }
      }
    }
    wg_barrier();
    bool more1, moreb;
    FRESH_LANE;
    const unsigned LIST = L0 + L_LIST + grp * LISTS;
    int nact = 0, nstep;
    bool lighter;
    unsigned exy[4], ezd[4], ep[4];
    float ef[4];
    u32x4 own[16];
    auto weights = [&](unsigned xy, unsigned zd, float f, unsigned p) {
      const float inv = __builtin_amdgcn_rcpf(hi16(zd));
      const unsigned hxy = pk2(lo16(xy) * inv, hi16(xy) * inv);
      const float hz = rb(lo16(zd) * inv);
      const unsigned fxy = pk2(f * lo16(hxy), f * hi16(hxy));
      const unsigned fzf = pk2(f * hz, f);
      if (p != SENT) {
        const unsigned a2 = LIST + p * 2;
        *LDSP(u16, a2 + O_F) = (u16)(fzf >> 16);
        *LDSP(u16, a2 + O_FX) = (u16)fxy;
        *LDSP(u16, a2 + O_FX + 256) = (u16)(fxy >> 16);
        *LDSP(u16, a2 + O_FX + 512) = (u16)fzf;
      }
    };
    {
      unsigned rec[4], rex[4];
      u32x2 ps[4], pd[4];
#pragma unroll
      for (int i = 0; i < 4; ++i) rec[i] = *LDSP(unsigned, L_RAW + grp * LISTS + lane * 4 + i * 256);
#pragma unroll
      for (int i = 0; i < 4; ++i) rex[i] = *LDSP(unsigned, L_RAW + grp * LISTS + lane * 4 + 1024 + i * 256);
      {
        asm volatile("s_nop 4\n\tbuffer_load_dwordx2 %0, %4, %8, %9 offen\n\tbuffer_load_dwordx2 %1, %5, %8, %9 offen\n\t"
                     "buffer_load_dwordx2 %2, %6, %8, %9 offen\n\tbuffer_load_dwordx2 %3, %7, %8, %9 offen"
                     : "=&v"(ps[0]), "=&v"(ps[1]), "=&v"(ps[2]), "=&v"(ps[3])
                     : "v"(rec[0] & 0xffffffu), "v"(rec[1] & 0xffffffu), "v"(rec[2] & 0xffffffu), "v"(rec[3] & 0xffffffu), "s"(rs_ws), "s"((unsigned)WS_P) : "memory");
      }
#pragma unroll
      for (int i = 0; i < 4; ++i) pd[i] = *LDSP(u32x2, L0 + L_DPOS + ((rec[i] >> 20) & 0xff0u));
      nstep = __builtin_amdgcn_ballot_w64(rec[3] != 0) ? 4 : __builtin_amdgcn_ballot_w64(rec[2] != 0) ? 3 : 2;
      {
        const u32x4 c0 = *LDSP(u32x4, L_CNT + (lane & 7) * 32), c1 = *LDSP(u32x4, L_CNT + (lane & 7) * 32 + 16);
        const int nbv = *LDSP(int, L_NB + (lane & 3) * 4);
        const unsigned mx = max(max(max(c0[0], c0[1]), max(c0[2], c0[3])), max(max(c1[0], c1[1]), max(c1[2], c1[3])));
        more1 = __builtin_amdgcn_ballot_w64(mx > (unsigned)(SUBC * (r1 + 1))) != 0;
        moreb = __builtin_amdgcn_ballot_w64(nbv > batch + 1) != 0;
        const int key = (c0[0] + c0[1] + c0[2] + c0[3] + c1[0] + c1[1] + c1[2] + c1[3]) * 2 + (((lane & 7) >> 2) ^ 1);
        lighter = __builtin_amdgcn_readlane(key, grp) < __builtin_amdgcn_readlane(key, grp ^ 4);
      }
#pragma unroll
      for (int i = 0; i < 2; ++i) *LDSP(unsigned, LIST + (64 * i + lane) * 4) = opq(0xffffffu);
      if (lane < 32) *LDSP(unsigned, LIST + (128 + lane) * 4) = opq(0xffffffu);
      *LDSP(unsigned, LIST + O_D + lane * 4) = 0x3f803f80u;
      *LDSP(unsigned, LIST + O_DL + lane * 4) = 0xffffffffu;
      asm volatile("s_waitcnt vmcnt(4)" : "+v"(ol[0]), "+v"(ol[1]), "+v"(ol[2]), "+v"(ol[3]), "+v"(ol[4]),
                   "+v"(ol[5]), "+v"(ol[6]), "+v"(ol[7]), "+v"(ol[8]), "+v"(ol[9]), "+v"(ol[10]), "+v"(ol[11]), "+v"(ol[12])::"memory");
#pragma unroll
      for (int i = 0; i < 12; ++i) *LDSP(u32x4, TB + i * 1024 + lane * 16) = ol[i];
      if (h == 0) *LDSP(u32x4, TB + 12 * 1024 + lane * 16) = ol[12];
      {
        const unsigned ra = TB + l31 * 512 + 16 * ((4 * h) ^ (l31 & 15));
#pragma unroll
        for (int i = 0; i < 16; ++i) own[i] = *LDSP(u32x4, (ra ^ (16 * (8 * ((i >> 2) & 1) + (i & 3)))) + 256 * (i >> 3));
      }
      asm volatile("s_waitcnt vmcnt(0)" : "+v"(ps[0]), "+v"(ps[1]), "+v"(ps[2]), "+v"(ps[3])::"memory");
      auto exact = [&](unsigned rc, u32x2 a, u32x2 c, unsigned& oxy, unsigned& ozd, float& of, unsigned& op) {
        const unsigned xy = pk2(lo16(a[0]) - lo16(c[0]), hi16(a[0]) - hi16(c[0]));
        const float z = rb(__uint_as_float(a[1]) - __uint_as_float(c[1]));
        const float x = lo16(xy), y = hi16(xy);
        const unsigned sq = pk2(x * x, y * y);
        const float q2 = rb(rb(lo16(sq) + hi16(sq) + rb(z * z)) + 1e-8f);
        const unsigned zd = pk2(z, __builtin_amdgcn_sqrtf(q2));
        const float d = hi16(zd);
        const float f = 0.5f * rb(rb(__builtin_amdgcn_cosf(rb(rb(3.141592653589793f * d) * 0.2f) * 0.15915494309189535f)) + 1.0f);
        const bool ok = (d < 5.0f) & (f != 0.0f);
        const uint64_t bm = __builtin_amdgcn_ballot_w64(ok);
        const unsigned p = __builtin_amdgcn_mbcnt_hi((unsigned)(bm >> 32), __builtin_amdgcn_mbcnt_lo((unsigned)bm, nact - CAP * r2));
        nact += __builtin_popcountll(bm);
        oxy = xy; ozd = zd; of = f; op = SENT;
        if (ok & (p < (unsigned)CAP)) {
          op = p;
          *LDSP(unsigned, LIST + swap23(p) * 4) = rc & 0xffffffu;
          *LDSP(u16, LIST + O_D + swap23(p) * 2) = (u16)(zd >> 16);
          *LDSP(u16, LIST + O_DL + p * 2) = (u16)(rc >> 24);
        }
      };
#pragma unroll
      for (int i = 0; i < 4; ++i) {
        exy[i] = 0; ezd[i] = 0; ef[i] = 0.f; ep[i] = SENT;
        if (i < nstep) exact(rec[i], ps[i], pd[i], exy[i], ezd[i], ef[i], ep[i]);
      }
#pragma nounroll
      while (__builtin_amdgcn_ballot_w64(rex[0] != 0)) {
        u32x2 a;
        asm volatile("s_nop 4\n\tbuffer_load_dwordx2 %0, %1, %2, %3 offen\n\ts_waitcnt vmcnt(0)" : "=&v"(a) : "v"(rex[0] & 0xffffffu), "s"(rs_ws), "s"((unsigned)WS_P) : "memory");
        unsigned xy, zd, p; float f;
        exact(rex[0], a, *LDSP(u32x2, L_DPOS + ((rex[0] >> 20) & 0xff0u)), xy, zd, f, p);
        weights(xy, zd, f, p);
        rex[0] = rex[1]; rex[1] = rex[2]; rex[2] = rex[3]; rex[3] = 0;
      }
    }
    const int cnt = __builtin_amdgcn_readfirstlane(max(min(nact - CAP * r2, CAP), 0));
    if (lane == 0) *LDSP(int, L_NB + 32 + wave * 4) = nact > CAP * (r2 + 1);
    const int ntile = (cnt + 31) >> 5;
    auto issue_h2 = [&](unsigned off) {
      asm volatile("s_nop 4\n\tbuffer_load_dwordx4 %0, %4, %5, %6 offen\n\tbuffer_load_dwordx4 %1, %4, %5, %6 offen offset:16\n\t"
                   "buffer_load_dwordx4 %2, %4, %5, %6 offen offset:32\n\tbuffer_load_dwordx4 %3, %4, %5, %6 offen offset:48"
                   : "=&v"(hN[0]), "=&v"(hN[1]), "=&v"(hN[2]), "=&v"(hN[3]) : "v"(off), "s"(rs_ws), "s"((unsigned)WS_H) : "memory");
    };
    auto issue_h = [&](int t) { FRESH_LANE; issue_h2(*LDSP(unsigned, LIST + (32 * t + l31) * 4) * 16u + 64u * h); };
    auto wait_h = [&]() {
      asm volatile("s_waitcnt vmcnt(12)" : "+v"(hN[0]), "+v"(hN[1]), "+v"(hN[2]), "+v"(hN[3])::"memory");
    };
    unsigned vo[4];
    auto prep_v = [&](int t) {
      FRESH_LANE;
      const int drow = lane >> 3;
      const unsigned dchunk = 16u * (((lane & 7) ^ (drow & 2)) + 8);
#pragma unroll
      for (int i = 0; i < 4; ++i) vo[i] = *LDSP(unsigned, LIST + (32 * t + 8 * i + drow) * 4) * 64u + dchunk;
    };
    auto issue_v = [&](int k) {
      asm volatile("s_waitcnt lgkmcnt(0)" ::: "memory");
      dma16x4(rs_sv, vo, 128 * k, TB + 4096 * k);
    };
    u32x4 rbf;
    auto calc_rbf = [&](int t) {
      FRESH_LANE;
      const float d = lo16(*LDSP(u16, LIST + O_D + 64 * t + 2 * l31));
      const f32x4 f0 = *LDSP(f32x4, L_FR + 32 * h), f1 = *LDSP(f32x4, L_FR + 32 * h + 16);
      const float inv = __builtin_amdgcn_rcpf(d);
#pragma unroll
      for (int i = 0; i < 4; ++i) {
        const float fa = i < 2 ? f0[2 * i] : f1[2 * i - 4], fb = i < 2 ? f0[2 * i + 1] : f1[2 * i - 3];
        const unsigned x = pk2(d * fa, d * fb);
        float ra = lo16(x) * 0.15915494309189535f, rc = hi16(x) * 0.15915494309189535f;
        ra -= __builtin_rintf(ra); rc -= __builtin_rintf(rc);
        rbf[i] = pk2(__builtin_amdgcn_sinf(ra) * inv, __builtin_amdgcn_sinf(rc) * inv);
      }
      asm volatile("" : "+v"(rbf));
    };
    setprio(3);
    issue_h(0);
    prep_v(0);
    {
      const unsigned lo = 16u * lane;
      const unsigned v4[4] = {lo, lo + 1024u, lo + 2048u, lo + 3072u};
      const int wo = min(wave * 4096, NFRAG * 1024 + 64 - 4096);
      dma16x4(rs_ws, v4, wo, L_W + wo);
      wg_barrier();
    }
    const bool late = lighter;
    if (!late) {
      issue_v(0); issue_v(1); issue_v(2);
      asm volatile("s_waitcnt vmcnt(12)\n\ts_barrier" ::: "memory");
    }
    setprio(min(ntile - 1, 2));
    SB();
#pragma unroll
    for (int i = 0; i < 4; ++i) if (i < nstep) weights(exy[i], ezd[i], ef[i], ep[i]);
    if (first) {
#pragma unroll
      for (int i = 0; i < 16; ++i)
#pragma unroll
        for (int k = 0; k < 4; ++k) {
          acc[i >> 2][(i >> 1) & 1][8 * (i & 1) + 2 * k] = lo16(own[i][k]);
          acc[i >> 2][(i >> 1) & 1][8 * (i & 1) + 2 * k + 1] = hi16(own[i][k]);
        }
      first = false;
    }
    SB();
    if (late) {
      asm volatile("s_waitcnt vmcnt(0)\n\ts_barrier" ::: "memory");
      issue_v(0); issue_v(1); issue_v(2);
    }
    const bool more2 = __builtin_amdgcn_ballot_w64(*LDSP(int, L_NB + 32 + (lane & 7) * 4) != 0) != 0;
    calc_rbf(0);
    wait_h();
    auto tile = [&](int t, auto nsc) {
      constexpr int NS = decltype(nsc)::value;
      FRESH_LANE;
      const unsigned WL = L0 + L_W + lane * 16, la = LIST + 64 * t + 16 * h;
      const unsigned mypair = (unsigned)(grp * 32 + l31 + 1) * 0x10001u;
      u32x4 Wb[5];
      auto ldW = [&](int ct) {
        Wb[4] = *LDSP(u32x4, WL + (24 + ct) * 1024);
#pragma unroll
        for (int ks = 0; ks < 4; ++ks) Wb[ks] = *LDSP(u32x4, WL + (4 * ct + ks) * 1024);
      };
      ldW(4);
      u32x4 msk[2], S1[2], sr[3][2];
#pragma unroll
      for (int s = 0; s < NS; ++s) msk[s] = *LDSP(u32x4, la + O_DL + 32 * s);
#pragma unroll
      for (int s = 0; s < NS; ++s)
#pragma unroll
        for (int k = 0; k < 3; ++k) sr[k][s] = *LDSP(u32x4, la + O_FX + 256 * k + 32 * s);
      SB();
      f32x16 ph, w;
      auto gemm = [&]() {
        w = mfma(rbf, Wb[4], zero16());
        ph = mfma(hN[0], Wb[0], zero16());
#pragma unroll
        for (int ks = 1; ks < 4; ++ks) ph = mfma(hN[ks], Wb[ks], ph);
      };
#define PIN1(a) asm volatile("" : "+v"(a))
#define PIN2(a, b) do { PIN1(a); PIN1(b); } while (0)
#define PIN3(a, b, c) asm volatile("" : "+v"(a), "+v"(b), "+v"(c))
      auto pack = [&](u32x4 (&a)[2]) {
        const f32x16 m = ph * w;
#pragma unroll
        for (int s = 0; s < NS; ++s) { a[s] = pack8(m, s); PIN1(a[s]); }
      };
#define MIX(id, lead, n, v) do { __builtin_amdgcn_sched_group_barrier(0x8, lead, id); _Pragma("unroll") for (int i_ = 0; i_ < n; ++i_) { \
        __builtin_amdgcn_sched_group_barrier(0x402, v, id); __builtin_amdgcn_sched_group_barrier(0x8, 1, id); } } while (0)
      u32x4 aa[2], ab[2];
      gemm();
#pragma unroll
      for (int s = 0; s < NS; ++s) {
        msk[s] = eqmask4(msk[s], mypair, 0x00010001u);
#pragma unroll
        for (int k = 0; k < 3; ++k) sr[k][s] &= msk[s];
      }
      MIX(0, 1, 4, 4 * NS);
      PIN2(ph, w);
      SB();
      ldW(5);
      SB();
      pack(aa);
      SB();
      gemm();
      PIN2(ph, w);
      SB();
      ldW(0);
      PIN2(aa[0], aa[NS - 1]);
      SB();
#pragma unroll
      for (int s = 0; s < NS; ++s)
#pragma unroll
        for (int k = 0; k < 3; ++k) acc[1 + k][0] = mfma(aa[s], sr[k][s], acc[1 + k][0]);
      pack(ab);
      MIX(1, 2, 3 * NS - 2, 6);
      PIN3(acc[1][0], acc[2][0], acc[3][0]);
      SB();
      gemm();
      PIN2(ph, w);
      SB();
      ldW(1);
      PIN2(ab[0], ab[NS - 1]);
      SB();
#pragma unroll
      for (int s = 0; s < NS; ++s)
#pragma unroll
        for (int k = 0; k < 3; ++k) acc[1 + k][1] = mfma(ab[s], sr[k][s], acc[1 + k][1]);
      pack(aa);
      MIX(2, 2, 3 * NS - 2, 6);
      PIN3(acc[1][1], acc[2][1], acc[3][1]);
      SB();
      unsigned tra, se, dn;
      f32x4 f0, f1;
      u32x4 rbn;
      {
        FRESH_LANE;
        const unsigned la2 = LIST + 64 * t + 16 * h, mp2 = (unsigned)(grp * 32 + l31 + 1) * 0x10001u;
#pragma unroll
        for (int s = 0; s < NS; ++s) { msk[s] = *LDSP(u32x4, la2 + O_DL + 32 * s); S1[s] = *LDSP(u32x4, la2 + O_F + 32 * s); }
        dn = *LDSP(u16, LIST + O_D + 64 * (t + 1) + 2 * l31);
        f0 = *LDSP(f32x4, L_FR + 32 * h); f1 = *LDSP(f32x4, L_FR + 32 * h + 16);
        se = *LDSP(unsigned, LIST + (32 * (t + 1) + l31) * 4);
#pragma unroll
        for (int i = 0; i < 4; ++i) vo[i] = *LDSP(unsigned, LIST + (32 * (t + 1) + 8 * i + (lane >> 3)) * 4);
        SB();
        gemm();
#pragma unroll
        for (int s = 0; s < NS; ++s) { S1[s] &= eqmask4(msk[s], mp2, 0x00010001u); PIN1(S1[s]); }
        const unsigned dchunk = 16u * (((lane & 7) ^ ((lane >> 3) & 2)) + 8);
#pragma unroll
        for (int i = 0; i < 4; ++i) { vo[i] = vo[i] * 64u + dchunk; PIN1(vo[i]); }
        se = se * 16u + 64u * h; PIN1(se);
        const int q4 = (lane >> 2) & 3, pp = lane & 3;
        tra = (q4 & 1) ? TB + (4 * h + (q4 >> 1)) * 128 + 64 * (pp & 1) + (lane & 16) + 8 * (pp >> 1) : 0x100000u;
        PIN1(tra);
        MIX(3, 2, 3, 8);
        PIN2(ph, w);
        SB();
      }
      ldW(2);
      PIN2(aa[0], aa[NS - 1]);
      SB();
#pragma unroll
      for (int s = 0; s < NS; ++s) acc[0][0] = mfma(aa[s], S1[s], acc[0][0]);
      PIN3(acc[0][0], ph, w);
      SB();
      pack(ab);
      SB();
      const float dnf = lo16(dn), inv = __builtin_amdgcn_rcpf(dnf);
      auto rbf2 = [&](float fa, float fb) {
        const unsigned x = pk2(dnf * fa, dnf * fb);
        float ra = lo16(x) * 0.15915494309189535f, rc = hi16(x) * 0.15915494309189535f;
        ra -= __builtin_rintf(ra); rc -= __builtin_rintf(rc);
        return pk2(__builtin_amdgcn_sinf(ra) * inv, __builtin_amdgcn_sinf(rc) * inv);
      };
      gemm();
      { unsigned r0 = rbf2(f0[0], f0[1]), r1_ = rbf2(f0[2], f0[3]); PIN2(r0, r1_); rbn[0] = r0; rbn[1] = r1_; }
      MIX(4, 1, 4, 6);
      PIN2(ph, w);
      SB();
      ldW(3);
      PIN2(ab[0], ab[NS - 1]);
      SB();
#pragma unroll
      for (int s = 0; s < NS; ++s) acc[0][1] = mfma(ab[s], S1[s], acc[0][1]);
      PIN3(acc[0][1], ph, w);
      SB();
      f32x16 mv[2];
      mv[0] = ph * w;
      PIN1(mv[0]);
      SB();
      gemm();
      { unsigned r0 = rbf2(f1[0], f1[1]), r1_ = rbf2(f1[2], f1[3]); PIN2(r0, r1_); rbn[2] = r0; rbn[3] = r1_; }
      MIX(5, 1, 4, 6);
      PIN2(ph, w);
      SB();
      issue_h2(se);
      rbf = rbn;
      mv[1] = ph * w;
      PIN1(mv[1]);
      SB();
      __builtin_amdgcn_s_setprio(3);
      asm volatile("s_waitcnt vmcnt(4)" ::: "memory");
      u32x2 va[8], vb[8];
      auto ldv = [&](u32x2 (&v)[8], int t2, int k) {
#pragma unroll
        for (int i = 0; i < 4 * NS; ++i)
          v[i] = trread(tra + k * 4096 + (8 * (i >> 1) + 2 * (i & 1)) * 128 + 32 * (t2 ^ (i & 1)));
      };
      auto vv = [&](const u32x2 (&v)[8], const f32x16& m, f32x16& ac) {
        u32x4 a[2];
#pragma unroll
        for (int i = 0; i < 4 * NS; ++i) {
          const unsigned v0 = v[i][0], v1 = v[i][1];
          a[i >> 2][i & 3] = pk2(m[2 * i] * __uint_as_float(v0), m[2 * i + 1] * __uint_as_float(v1));
        }
#pragma unroll
        for (int s = 0; s < NS; ++s) ac = mfma(a[s], S1[s], ac);
      };
      ldv(va, 0, 0); ldv(vb, 1, 0);
      SB();
      vv(va, mv[0], acc[1][0]);
      SB();
      issue_v(0);
      ldv(va, 0, 1);
      SB();
      vv(vb, mv[1], acc[1][1]);
      SB();
      ldv(vb, 1, 1);
      SB();
      vv(va, mv[0], acc[2][0]);
      SB();
      issue_v(1);
      ldv(va, 0, 2);
      SB();
      vv(vb, mv[1], acc[2][1]);
      SB();
      ldv(vb, 1, 2);
      SB();
      vv(va, mv[0], acc[3][0]);
      SB();
      issue_v(2);
      vv(vb, mv[1], acc[3][1]);
      SB();
      wait_h();
    };
    const int nfull = (cnt + 15) >> 5;
    for (int t = 0; t < nfull; ++t) { tile(t, std::integral_constant<int, 2>{}); setprio(min(ntile - 2 - t, 2)); }
    if (nfull < ntile) tile(nfull, std::integral_constant<int, 1>{});
    if (more2) ++r2;
    else if (more1) { ++r1; r2 = 0; }
    else if (moreb) { ++batch; r1 = 0; r2 = 0; }
    else break;
    asm volatile("s_waitcnt vmcnt(0)" ::: "memory");
    wg_barrier();
  }
  {
    FRESH_LANE;
    u32x4 o[16];
#pragma unroll
    for (int i = 0; i < 16; ++i) o[i] = pack8(acc[i >> 2][(i >> 1) & 1], i & 1);
    asm volatile("s_waitcnt vmcnt(0)" ::: "memory");
    if (l31 < GN) {
      const unsigned wa = TB + l31 * 512 + 16 * ((4 * h) ^ (l31 & 7));
#pragma unroll
      for (int i = 0; i < 16; ++i) *LDSP(u32x4, (wa ^ (16 * (i & 3))) + 128 * (i >> 2)) = o[i];
    }
    const int gw = __builtin_amdgcn_readfirstlane(max(min(nb - grp * GN, GN), 0));
    const uint64_t op = (uint64_t)out + (size_t)(node0 + grp * GN) * 512;
    const __amdgpu_buffer_rsrc_t rb_o = __builtin_amdgcn_make_buffer_rsrc((void*)op, 0, gw * 512, 0x00020000);
    u32x4 v[(GN + 1) / 2];
#pragma unroll
    for (int i = 0; i < (GN + 1) / 2; ++i) v[i] = *LDSP(u32x4, TB + i * 1024 + lane * 16);
    const unsigned oa = h * 512 + 16 * (l31 ^ h);
#pragma unroll
    for (int i = 0; i < (GN + 1) / 2; ++i) __builtin_amdgcn_raw_buffer_store_b128(v[i], rb_o, oa ^ (32 * (i & 3)), i * 1024, WT);
  }
  asm volatile("painn_code_hi:");
}

extern "C" __global__ __launch_bounds__(192) void k_gen_phi(const u16* __restrict__ sv, const u16* __restrict__ w1, const u16* __restrict__ w2,
                                                            float* __restrict__ phi) {
  __shared__ float s[64], hd[64];
  const int t = TID; const size_t n = BID;
  if (t < 64) s[t] = lo16(sv[n * 256 + t]);
  wg_barrier();
  if (t < 64) {
    float a = 0.f;
    for (int k = 0; k < 64; ++k) a += s[k] * lo16(w1[k * 64 + t]);
    hd[t] = rb(a * __builtin_amdgcn_rcpf(1.0f + __builtin_amdgcn_exp2f(a * -1.4426950408889634f)));
  }
  wg_barrier();
  float a = 0.f;
  for (int k = 0; k < 64; ++k) a += hd[k] * lo16(w2[k * 192 + t]);
  phi[n * 192 + t] = a;
}
extern "C" __global__ __launch_bounds__(256) void k_gen_edge(const u16* __restrict__ sv, const u16* __restrict__ pos, const int* __restrict__ src,
                                                             const int* __restrict__ dst, const u16* __restrict__ freqs, const u16* __restrict__ wr,
                                                             const float* __restrict__ phi, float* __restrict__ acc, int E) {
  const size_t e = (size_t)BID * 4 + (TID >> 6);
  const int c = TID & 63;
  if (e >= (size_t)E) return;
  const size_t s = src[e], t = dst[e];
  const float x = rb(lo16(pos[s * 3]) - lo16(pos[t * 3])), y = rb(lo16(pos[s * 3 + 1]) - lo16(pos[t * 3 + 1])), z = rb(lo16(pos[s * 3 + 2]) - lo16(pos[t * 3 + 2]));
  const float d = rb(__builtin_amdgcn_sqrtf(rb(rb(rb(x * x) + rb(y * y) + rb(z * z)) + 1e-8f)));
  if (!(d < 5.0f)) return;
  const float inv = __builtin_amdgcn_rcpf(d);
  const float f = 0.5f * rb(rb(__builtin_amdgcn_cosf(rb(rb(3.141592653589793f * d) * 0.2f) * 0.15915494309189535f)) + 1.0f);
  float w0 = 0.f, w1 = 0.f, w2 = 0.f;
  for (int k = 0; k < 16; ++k) {
    float r = rb(d * lo16(freqs[k])) * 0.15915494309189535f;
    r -= __builtin_rintf(r);
    const float b = rb(__builtin_amdgcn_sinf(r) * inv);
    w0 += b * lo16(wr[k * 192 + c]); w1 += b * lo16(wr[k * 192 + 64 + c]); w2 += b * lo16(wr[k * 192 + 128 + c]);
  }
  const float ms = phi[s * 192 + c] * w0 * f, mv = phi[s * 192 + 64 + c] * w1 * f, mr = phi[s * 192 + 128 + c] * w2 * f;
  float* a = acc + t * 256 + c;
  __hip_atomic_fetch_add(a, ms, __ATOMIC_RELAXED, __HIP_MEMORY_SCOPE_AGENT);
  __hip_atomic_fetch_add(a + 64, mv * lo16(sv[s * 256 + 64 + c]) + mr * rb(x * inv), __ATOMIC_RELAXED, __HIP_MEMORY_SCOPE_AGENT);
  __hip_atomic_fetch_add(a + 128, mv * lo16(sv[s * 256 + 128 + c]) + mr * rb(y * inv), __ATOMIC_RELAXED, __HIP_MEMORY_SCOPE_AGENT);
  __hip_atomic_fetch_add(a + 192, mv * lo16(sv[s * 256 + 192 + c]) + mr * rb(z * inv), __ATOMIC_RELAXED, __HIP_MEMORY_SCOPE_AGENT);
}
extern "C" __global__ __launch_bounds__(256) void k_gen_out(const u16* __restrict__ sv, const float* __restrict__ acc, u16* __restrict__ out) {
  const size_t i = (size_t)BID * 256 + TID;
  out[i] = (u16)pk2(lo16(sv[i]) + acc[i], 0.f);
}

#if defined(__HIP_DEVICE_COMPILE__)
#pragma clang attribute pop
#endif
extern "C" void kernel_launch(void* const* d_in, const int* in_sizes, int n_in, void* d_out, int out_size, void* d_ws, size_t ws_size,
                              hipStream_t stream) {
  const u16* sv = (const u16*)d_in[0];
  const u16* pos = (const u16*)d_in[1];
  const int* src = (const int*)d_in[2];
  const int* dst = (const int*)d_in[3];
  const u16* fr = (const u16*)d_in[4];
  const u16* w1 = (const u16*)d_in[5];
  const u16* w2 = (const u16*)d_in[6];
  const u16* wr = (const u16*)d_in[7];
  const int N = in_sizes[1] / 3, E = in_sizes[2];
  char* ws = (char*)d_ws;
  if (N <= 0) return;
  if (N > 51200 || E > 256 * CH) {
    float* acc = (float*)ws;
    float* phi = acc + (size_t)N * 256;
    (void)hipMemsetAsync(acc, 0, (size_t)N * 1024, stream);
    k_gen_phi<<<N, 192, 0, stream>>>(sv, w1, w2, phi);
    if (E > 0) k_gen_edge<<<(E + 3) / 4, 256, 0, stream>>>(sv, pos, src, dst, fr, wr, phi, acc, E);
    k_gen_out<<<N, 256, 0, stream>>>(sv, acc, (u16*)d_out);
    return;
  }
  const int BN = (N + 255) / 256 < 2 ? 2 : (N + 255) / 256;
  const int B = (N + BN - 1) / BN, bpx = (B + 7) / 8;
  int chunk = (((E + 195) / 196) + 3) & ~3;
  chunk = chunk < 1024 ? 1024 : chunk > CH ? CH : chunk;
  const int W = (E + chunk - 1) / chunk;
  const int HW = 200, WP = (W + 7) & ~7;
  const unsigned magicBN = (unsigned)(0x100000000ull / (unsigned)BN) + 1u;
  (void)hipFuncSetAttribute((const void*)k_agg, hipFuncAttributeMaxDynamicSharedMemorySize, L_END);
  k_bin<<<WP + HW + 8, 512, 0, stream>>>(src, dst, sv, pos, w1, ws, (unsigned)N | (unsigned)BN << 16 | (unsigned)bpx << 24, E, magicBN,
                                         (unsigned)chunk | (unsigned)W << 16, w2, wr, fr);
  k_agg<<<8 * bpx, 512, L_END, stream>>>(sv, ws, (u16*)d_out, N, W, BN, B, bpx);
}
```

```cpp
#if defined(__HIP_DEVICE_COMPILE__)
#pragma clang attribute push(__attribute__((target("no-packed-fp32-ops"))), apply_to = function)
#endif
#include <hip/hip_runtime.h>
#include <stdint.h>
#include <type_traits>

#define TID __builtin_amdgcn_workitem_id_x()
#define BID __builtin_amdgcn_workgroup_id_x()
typedef unsigned short u16;
typedef __attribute__((ext_vector_type(8))) __bf16 bf16x8;
typedef __attribute__((ext_vector_type(2))) __bf16 bf16x2;
typedef __attribute__((ext_vector_type(4))) short s16x4;
typedef __attribute__((ext_vector_type(2))) float f32x2;
typedef __attribute__((ext_vector_type(4))) float f32x4;
typedef __attribute__((ext_vector_type(16))) float f32x16;
typedef __attribute__((ext_vector_type(4))) unsigned u32x4;
typedef __attribute__((ext_vector_type(2))) unsigned u32x2;
#define LDSP(T, off) ((__attribute__((address_space(3))) T*)(uintptr_t)(off))
#define DI __device__ __forceinline__
#define SB() __builtin_amdgcn_sched_barrier(0)

constexpr int WT = 17;
constexpr int CH = 4096;
constexpr int SEGQ = CH / 4 + 192;
constexpr int GN = 25;
constexpr int NFRAG = 30;
constexpr int SUBC = 64;
constexpr int CAP = 128;
constexpr int BQ = 256;
constexpr unsigned SENT = 0xFFFFFFFFu;
constexpr int L_DPOS = 0;
constexpr int L_CNT = 4096;
constexpr int L_NB = L_CNT + 256;
constexpr int L_RAW = L_NB + 64;
constexpr int L_LIST = L_RAW + 1024;
constexpr int O_D = 640, O_DL = O_D + 256, O_F = O_DL + 256, O_FX = O_F + 256;
constexpr int LISTS = 1024 + O_FX + 768;
constexpr int L_W = L_RAW + 8 * LISTS;
constexpr int L_FR = L_W + NFRAG * 1024;
constexpr int L_TB = (L_FR + 64 + 511) / 512 * 512;
constexpr int TBS = GN * 512;
constexpr int L_END = L_TB + 8 * TBS;
static_assert(L_END <= 163840 && L_TB < 65536 && L_W % 16 == 0 && LISTS % 16 == 0 && SUBC == 64, "LDS");
constexpr size_t WS_T = 65536;
constexpr size_t WS_SEG = WS_T + 256 * 256 * 4;
constexpr size_t WS_P = WS_SEG + (size_t)256 * SEGQ * 16;
constexpr size_t WS_C = WS_P + 51200 * 8;
constexpr size_t WS_H = WS_C + 51200 * 2;
constexpr size_t WS_END = WS_H + 51200 * 128;
constexpr size_t WS_F = NFRAG * 1024;
static_assert(WS_END < 0xffffff, "ws");

DI float lo16(unsigned x) { return __uint_as_float(x << 16); }
DI float hi16(unsigned x) { return __uint_as_float(x & 0xffff0000u); }
DI unsigned pk2(float a, float b) { f32x2 v = {a, b}; return __builtin_bit_cast(unsigned, __builtin_convertvector(v, bf16x2)); }
DI float rb(float a) { return hi16(pk2(a, a)); }
DI f32x16 mfma(u32x4 a, u32x4 b, f32x16 c) {
  return __builtin_amdgcn_mfma_f32_32x32x16_bf16(__builtin_bit_cast(bf16x8, a), __builtin_bit_cast(bf16x8, b), c, 0, 0, 0);
}
DI f32x16 zero16() { f32x16 z;
#pragma unroll
  for (int r = 0; r < 16; ++r) z[r] = 0.f;
  return z; }
DI int wave_incl_scan(int v) {
  v += __builtin_amdgcn_update_dpp(0, v, 0x111, 0xf, 0xf, false);
  v += __builtin_amdgcn_update_dpp(0, v, 0x112, 0xf, 0xf, false);
  v += __builtin_amdgcn_update_dpp(0, v, 0x114, 0xf, 0xf, false);
  v += __builtin_amdgcn_update_dpp(0, v, 0x118, 0xf, 0xf, false);
  v += __builtin_amdgcn_update_dpp(0, v, 0x142, 0xa, 0xf, false);
  v += __builtin_amdgcn_update_dpp(0, v, 0x143, 0xc, 0xf, false);
  return v;
}
DI int chan_of(int t2, int i32) { return 32 * ((i32 >> 2) & 1) + 16 * t2 + 4 * (i32 >> 3) + (i32 & 3); }
DI u32x4 pack8(const f32x16& x, int s) {
  return u32x4{pk2(x[8 * s], x[8 * s + 1]), pk2(x[8 * s + 2], x[8 * s + 3]), pk2(x[8 * s + 4], x[8 * s + 5]), pk2(x[8 * s + 6], x[8 * s + 7])};
}
DI void setprio(int p) {
  if (p >= 3) __builtin_amdgcn_s_setprio(3);
  else if (p == 2) __builtin_amdgcn_s_setprio(2);
  else if (p == 1) __builtin_amdgcn_s_setprio(1);
  else __builtin_amdgcn_s_setprio(0);
}
DI u32x2 trread(unsigned a) { return __builtin_bit_cast(u32x2, __builtin_amdgcn_ds_read_tr16_b64_v4i16(LDSP(s16x4, a))); }
DI void st16(void* base, unsigned off, u32x4 v) {
  const __amdgpu_buffer_rsrc_t r = __builtin_amdgcn_make_buffer_rsrc(base, 0, 0x7fffffff, 0x00020000);
  __builtin_amdgcn_raw_buffer_store_b128(v, r, off, 0, WT);
}
DI unsigned qmod(float x) { return min((unsigned)(__builtin_amdgcn_fractf(x * 0.0625f) * 65536.0f), 65535u); }
DI unsigned opq(unsigned c) { asm volatile("" : "+v"(c)); return c; }
DI unsigned swap23(unsigned p) { return (p & ~12u) | ((p & 4u) << 1) | ((p & 8u) >> 1); }
DI void wg_barrier() { asm volatile("s_waitcnt lgkmcnt(0)\n\ts_barrier" ::: "memory"); }

DI void dma16(u32x4 rsrc, unsigned voff, unsigned ldsaddr) {
  asm volatile("s_mov_b32 m0, %2\n\ts_nop 3\n\tbuffer_load_dwordx4 %0, %1, 0 offen lds" ::"v"(voff), "s"(rsrc), "s"(ldsaddr) : "memory");
}
DI void dma16x4(u32x4 rsrc, const unsigned (&voff)[4], unsigned soff, unsigned m0) {
  asm volatile("s_mov_b32 m0, %6\n\ts_nop 3\n\tbuffer_load_dwordx4 %0, %4, %5 offen lds\n\t"
               "s_add_u32 m0, m0, 0x400\n\ts_nop 0\n\tbuffer_load_dwordx4 %1, %4, %5 offen lds\n\t"
               "s_add_u32 m0, m0, 0x400\n\ts_nop 0\n\tbuffer_load_dwordx4 %2, %4, %5 offen lds\n\t"
               "s_add_u32 m0, m0, 0x400\n\ts_nop 0\n\tbuffer_load_dwordx4 %3, %4, %5 offen lds"
               ::"v"(voff[0]), "v"(voff[1]), "v"(voff[2]), "v"(voff[3]), "s"(rsrc), "s"(soff), "s"(m0) : "memory", "scc");
}
constexpr int K1_CNT = 0, K1_STAGE = 8 * 256 * 4, K1_TOT = K1_STAGE + SEGQ * 16, K1_W1 = 0, K1_HT = 8192, K1_END = K1_HT + 8 * 4096;
#if defined(__HIP_DEVICE_COMPILE__)
#pragma clang attribute pop
#endif
extern "C" __global__ __launch_bounds__(512, 4) void k_bin(const int* __restrict__ src, const int* __restrict__ dst, const u16* __restrict__ sv,
                                                           const u16* __restrict__ pos, const u16* __restrict__ w1, char* __restrict__ ws,
                                                           unsigned shapeN, int E, unsigned magicBN, unsigned shapeE, const u16* __restrict__ w2,
                                                           const u16* __restrict__ wr, const u16* __restrict__ freqs) {
  asm volatile("" ::"s"(src), "s"(dst), "s"(sv), "s"(pos), "s"(w1), "s"(ws), "s"(shapeN), "s"(E), "s"(magicBN), "s"(shapeE));
  const int N = shapeN & 0xffff, BN = (shapeN >> 16) & 0xff, NPX = (shapeN >> 24) * BN, chunk = shapeE & 0xffff, W = shapeE >> 16, WP = (W + 7) & ~7;
  constexpr int HW = 200;
  unsigned* T = (unsigned*)(ws + WS_T);
  u32x4* seg = (u32x4*)(ws + WS_SEG);
  u32x4* wf = (u32x4*)ws;
  u32x2* ptab = (u32x2*)(ws + WS_P);
  u16* ctab = (u16*)(ws + WS_C);
  char* htab = ws + WS_H;
  __shared__ __attribute__((aligned(16))) char lds_raw[K1_END + 256];
  const unsigned L0 = (unsigned)(uintptr_t)(__attribute__((address_space(3))) char*)lds_raw;
  const int tid = TID, lane = tid & 63;
  const int wave = __builtin_amdgcn_readfirstlane(tid >> 6);
  int bid = BID;
  if (bid < WP + HW) { if (bid < WP) { if (bid >= W) return; bid += HW; } else bid -= WP; } else bid += W - WP;
  if (wave == 0) {
    uint64_t pc; unsigned len;
    asm volatile("s_getpc_b64 %0\npainn_self_lo:\n\ts_mov_b32 %1, painn_code_hi-painn_self_lo" : "=s"(pc), "=s"(len));
    const u32x4 rc = {(unsigned)pc, (unsigned)(pc >> 32) & 0xffffu, len, 0x00020000u};
    asm volatile("s_mov_b32 m0, %2\n\ts_nop 3\n\tbuffer_load_dword %0, %1, 0 offen lds" ::"v"(lane * 128), "s"(rc), "s"(L0 + K1_END) : "memory");
  }
  if (bid >= W + HW) {
    {
      uint64_t pc; unsigned len;
      asm volatile("s_getpc_b64 %0\npainn_code_lo:\n\ts_mov_b32 %1, painn_code_hi-painn_code_lo" : "=s"(pc), "=s"(len));
      const __amdgpu_buffer_rsrc_t rc = __builtin_amdgcn_make_buffer_rsrc((void*)pc, 0, len, 0x00020000);
      const unsigned x = __builtin_amdgcn_raw_buffer_load_b32(rc, tid * 64, 0, 0);
      asm volatile("" ::"v"(x));
    }
    const int idx = (bid - W - HW) * 512 + tid;
    if (idx >= NFRAG * 64) {
      if (idx < NFRAG * 64 + 16) ((unsigned*)wf)[WS_F / 4 + idx - NFRAG * 64] = (unsigned)freqs[idx - NFRAG * 64] << 16;
      return;
    }
    const int f = idx >> 6, i32 = lane & 31, h = lane >> 5;
    const u16* p; int s2;
    if (f < 24) { const int ct = f >> 2; p = w2 + (16 * (f & 3) + 4 * h) * 192 + 64 * (ct >> 1) + chan_of(ct & 1, i32); s2 = 8 * 192; }
    else { const int ct = f - 24; p = wr + 8 * h * 192 + 64 * (ct >> 1) + chan_of(ct & 1, i32); s2 = 4 * 192; }
    unsigned v[8];
#pragma unroll
    for (int j = 0; j < 8; ++j) v[j] = p[(j & 3) * 192 + (j >> 2) * s2];
    wf[idx] = u32x4{v[0] | v[1] << 16, v[2] | v[3] << 16, v[4] | v[5] << 16, v[6] | v[7] << 16};
    return;
  }
  if (bid < HW) {
    const int l31 = lane & 31, h = lane >> 5;
    const int nl = (wave * (HW >> 3) + (bid >> 3)) * 32, nend = min(N, ((bid & 7) + 1) * NPX);
    const int n0 = nl < NPX ? (bid & 7) * NPX + nl : N;
    const int nn = min(n0 + l31, N - 1);
    const unsigned HT = L0 + K1_HT + wave * 4096;
    {
      const uint64_t sp = (uint64_t)sv, wp = (uint64_t)w1;
      const u32x4 rs_sv = {(unsigned)sp, (unsigned)(sp >> 32), (unsigned)N * 512u, 0x00020000u};
      const u32x4 rs_w1 = {(unsigned)wp, (unsigned)(wp >> 32), 8192u, 0x00020000u};
      const unsigned o = (unsigned)(n0 + (lane >> 3)) * 512u + 16u * ((lane & 7) ^ (lane >> 3));
      const unsigned v4[4] = {o, o + 4096u, o + 8192u, o + 12288u};
      dma16x4(rs_sv, v4, 0, HT);
      dma16(rs_w1, tid * 16u, L0 + K1_W1 + wave * 1024);
    }
    unsigned px = pos[nn * 3], py = pos[nn * 3 + 1], pz = pos[nn * 3 + 2];
    asm volatile("s_waitcnt vmcnt(0)\n\ts_barrier" : "+v"(px), "+v"(py), "+v"(pz)::"memory");
    if (n0 >= N) return;
    u32x4 sf[4];
#pragma unroll
    for (int ks = 0; ks < 4; ++ks) sf[ks] = *LDSP(u32x4, HT + l31 * 128 + 16 * ((4 * h + ks) ^ (l31 & 7)));
    const unsigned ta = L0 + K1_W1 + (32 * h + ((lane >> 2) & 3)) * 128 + (16 * ((lane >> 4) & 1) + 4 * (lane & 3)) * 2;
    f32x16 d1[2];
#pragma unroll
    for (int ht = 0; ht < 2; ++ht) {
      d1[ht] = zero16();
#pragma unroll
      for (int ks = 0; ks < 4; ++ks) {
        const u32x2 a0 = trread(ta + ks * 1024 + ht * 64), a1 = trread(ta + ks * 1024 + 512 + ht * 64);
        d1[ht] = mfma(u32x4{a0[0], a0[1], a1[0], a1[1]}, sf[ks], d1[ht]);
      }
    }
#pragma unroll
    for (int ht = 0; ht < 2; ++ht) {
      f32x16 e = d1[ht] * -1.4426950408889634f;
#pragma unroll
      for (int r = 0; r < 16; ++r) e[r] = __builtin_amdgcn_exp2f(e[r]);
      e += 1.0f;
#pragma unroll
      for (int r = 0; r < 16; ++r) e[r] = __builtin_amdgcn_rcpf(e[r]);
      e *= d1[ht];
#pragma unroll
      for (int s = 0; s < 2; ++s) *LDSP(u32x4, HT + l31 * 128 + 16 * ((4 * h + 2 * ht + s) ^ (l31 & 7))) = pack8(e, s);
    }
#pragma unroll
    for (int i = 0; i < 4; ++i) {
      const int n = 8 * i + (lane >> 3);
      const u32x4 v = *LDSP(u32x4, HT + i * 1024 + lane * 16);
      if (n0 + n < nend) st16(htab, (n0 + n) * 128 + 16 * ((lane & 7) ^ (n & 7)), v);
    }
    if (h == 0 && n0 + l31 < nend) {
      ptab[n0 + l31] = u32x2{px | py << 16, pz << 16};
      ctab[n0 + l31] = (u16)(qmod(lo16(px)) >> 11 | (qmod(lo16(py)) >> 11) << 5 | (qmod(lo16(pz)) & 0xfc00u));
    }
    return;
  }
  const int cw = bid - HW;
  const int eend = min(E, (cw + 1) * chunk);
  const __amdgpu_buffer_rsrc_t rb_s = __builtin_amdgcn_make_buffer_rsrc((void*)src, 0, eend * 4, 0x00020000);
  const __amdgpu_buffer_rsrc_t rb_d = __builtin_amdgcn_make_buffer_rsrc((void*)dst, 0, eend * 4, 0x00020000);
  const int e0 = cw * chunk + tid * 4;
  u32x4 s4[2], d4[2];
#pragma unroll
  for (int u = 0; u < 2; ++u) d4[u] = __builtin_amdgcn_raw_buffer_load_b128(rb_d, (e0 + u * 2048) * 4, 0, 0);
#pragma unroll
  for (int u = 0; u < 2; ++u) s4[u] = __builtin_amdgcn_raw_buffer_load_b128(rb_s, (e0 + u * 2048) * 4, 0, 0);
  *LDSP(u32x4, L0 + K1_CNT + wave * 1024 + lane * 16) = u32x4{0, 0, 0, 0};
#pragma unroll
  for (int i = 0; i < 3; ++i) if (i * 512 + tid < SEGQ) *LDSP(u32x4, L0 + K1_STAGE + (i * 512 + tid) * 16) = u32x4{0, 0, 0, 0};
  unsigned ca[8], hi8[8], rank[8];
#pragma unroll
  for (int k = 0; k < 8; ++k) {
    const unsigned d = d4[k >> 2][k & 3];
    const unsigned b = (unsigned)(((uint64_t)d * magicBN) >> 32);
    ca[k] = L0 + K1_CNT + wave * 1024 + b * 4;
    rank[k] = SENT;
    if (e0 + (k >> 2) * 2048 + (k & 3) < eend) rank[k] = __hip_atomic_fetch_add(LDSP(unsigned, ca[k]), 1u, __ATOMIC_RELAXED, __HIP_MEMORY_SCOPE_WORKGROUP);
    const unsigned dl = d - b * (unsigned)BN;
    hi8[k] = dl + ((dl * 41u) >> 10) * 7u + 1u;
  }
  wg_barrier();
  if (wave == 0) {
    u32x4 c[8], tot = {0, 0, 0, 0};
#pragma unroll
    for (int w = 0; w < 8; ++w) { c[w] = *LDSP(u32x4, L0 + K1_CNT + w * 1024 + lane * 16); tot += c[w]; }
    const u32x4 q = (tot + 3u) >> 2;
    const unsigned ls = q[0] + q[1] + q[2] + q[3];
    const unsigned incl = wave_incl_scan(ls);
    u32x4 st; st[0] = incl - ls; st[1] = st[0] + q[0]; st[2] = st[1] + q[1]; st[3] = st[2] + q[2];
    u32x4 run = st * 4u;
#pragma unroll
    for (int w = 0; w < 8; ++w) { *LDSP(u32x4, L0 + K1_CNT + w * 1024 + lane * 16) = run; run += c[w]; }
    if (lane == 63) *LDSP(unsigned, L0 + K1_TOT) = incl;
    st16(T, cw * 1024 + 16 * lane, st | (q << 16));
  }
  wg_barrier();
  unsigned base[8];
#pragma unroll
  for (int k = 0; k < 8; ++k) base[k] = *LDSP(unsigned, ca[k]);
#pragma unroll
  for (int k = 0; k < 8; ++k)
    if (rank[k] != SENT) *LDSP(unsigned, L0 + K1_STAGE + (base[k] + rank[k]) * 4) = s4[k >> 2][k & 3] << 3 | hi8[k] << 24;
  const int total = *LDSP(int, L0 + K1_TOT);
  wg_barrier();
#pragma unroll
  for (int i = 0; i < 3; ++i) {
    const int qd = i * 512 + tid;
    if (qd < total) st16(seg, (cw * SEGQ + qd) * 16, *LDSP(u32x4, L0 + K1_STAGE + qd * 16));
  }
}

#if defined(__HIP_DEVICE_COMPILE__)
#pragma clang attribute push(__attribute__((target("no-packed-fp32-ops"))), apply_to = function)
#endif
DI u32x4 eqmask4(u32x4 a, unsigned b, unsigned ones) {
  u32x4 x;
#pragma unroll
  for (int i = 0; i < 4; ++i) x[i] = a[i] ^ b;
  asm("v_pk_min_u16 %0, %0, %4\n\tv_pk_min_u16 %1, %1, %4\n\tv_pk_min_u16 %2, %2, %4\n\tv_pk_min_u16 %3, %3, %4\n\t"
      "v_pk_sub_u16 %0, %0, %4\n\tv_pk_sub_u16 %1, %1, %4\n\tv_pk_sub_u16 %2, %2, %4\n\tv_pk_sub_u16 %3, %3, %4"
      : "+v"(x[0]), "+v"(x[1]), "+v"(x[2]), "+v"(x[3]) : "s"(ones));
  return x;
}

typedef __attribute__((ext_vector_type(2))) short s16x2;
extern "C" __global__ __launch_bounds__(512, 2) void k_agg(const u16* __restrict__ sv, const char* __restrict__ wf, u16* __restrict__ out, int N,
                                                           int W, int BN, int B, int bpx) {
  asm volatile("" ::"s"(sv), "s"(wf), "s"(out), "s"(N), "s"(W), "s"(BN), "s"(B), "s"(bpx));
  const int wave = __builtin_amdgcn_readfirstlane(TID >> 6);
  const int b = ((int)BID & 7) * bpx + ((int)BID >> 3);
  if (b >= B) return;
  constexpr unsigned L0 = 0;
  const int node0 = b * BN;
  const int nb = min(BN, N - node0);
  const uint64_t svp = (uint64_t)sv, wfp = (uint64_t)wf;
  const u32x4 rs_sv = {(unsigned)svp, (unsigned)(svp >> 32), (unsigned)N * 512u, 0x00020000u};
  const u32x4 rs_ws = {(unsigned)wfp, (unsigned)(wfp >> 32), (unsigned)WS_END, 0x00020000u};
#define FRESH_LANE int lane; asm volatile("v_mbcnt_lo_u32_b32 %0, -1, 0\n\tv_mbcnt_hi_u32_b32 %0, -1, %0" : "=v"(lane)); const int l31 = lane & 31, h = lane >> 5; (void)l31; (void)h
  const unsigned TB = L0 + L_TB + wave * TBS;

  f32x16 acc[4][2];
  u32x4 hN[4];
  int batch = 0, r1 = 0, r2 = 0;
  bool first = true;
  const int grp = wave;
  while (true) {
    u32x4 ol[13];
    {
      FRESH_LANE;
      const unsigned lo = 16u * lane;
      const unsigned v4[4] = {lo, lo + 1024u, lo + 2048u, lo + 3072u};
      const unsigned oo = first ? (unsigned)(node0 + wave * GN + h) * 512u + 16u * (l31 ^ h) : 0x7fff0000u;
#define OWNA(i) ((oo ^ (32u * ((i) & 7))) + 1024u * (i))
#define OWN4(j) asm volatile("s_nop 4\n\tbuffer_load_dwordx4 %0, %4, %8, 0 offen\n\tbuffer_load_dwordx4 %1, %5, %8, 0 offen\n\t" \
                             "buffer_load_dwordx4 %2, %6, %8, 0 offen\n\tbuffer_load_dwordx4 %3, %7, %8, 0 offen" \
                             : "=&v"(ol[4 * j]), "=&v"(ol[4 * j + 1]), "=&v"(ol[4 * j + 2]), "=&v"(ol[4 * j + 3]) \
                             : "v"(OWNA(4 * j)), "v"(OWNA(4 * j + 1)), "v"(OWNA(4 * j + 2)), "v"(OWNA(4 * j + 3)), "s"(rs_sv) : "memory")
#define OWN1() asm volatile("s_nop 4\n\tbuffer_load_dwordx4 %0, %1, %2, 0 offen" : "=&v"(ol[12]) : "v"(h ? 0x7fff0000u : OWNA(12)), "s"(rs_sv) : "memory")
      if (wave >= 4) {
        const int rw = wave - 4;
        const unsigned MAP = L_LIST + wave * LISTS + 1024;
        const int cpw = (W + 3) >> 2;
        const int cell = rw * cpw + lane;
        unsigned t;
        u32x2 dp;
        asm volatile("s_nop 4\n\tbuffer_load_dword %0, %2, %4, 0 offen\n\tbuffer_load_dwordx2 %1, %3, %4, 0 offen\n\ts_barrier\n\ts_waitcnt vmcnt(0)"
                     : "=&v"(t), "=&v"(dp) : "v"((lane < cpw && cell < W) ? (int)WS_T + (cell * 256 + b) * 4 : 0x7fffffff),
                       "v"((int)WS_P + (node0 + (2 * rw + h) * GN + l31 - 1) * 8), "s"(rs_ws) : "memory");
        const int q = t >> 16;
        const int incl = wave_incl_scan(q), excl = incl - q;
        const int total = __builtin_amdgcn_readlane(incl, 63);
        const int win0 = batch * BQ;
        const int gq = (int)(WS_SEG / 16) + cell * SEGQ + (int)(t & 0xffffu) - excl;
        const int hi = min(incl, win0 + BQ);
#pragma nounroll
        for (int i = max(excl, win0); i < hi; ++i) *LDSP(int, MAP + (i - win0) * 4) = (gq + i) * 16;
        unsigned mq[4];
#pragma unroll
        for (int s = 0; s < 4; ++s) mq[s] = *LDSP(int, MAP + (64 * s + lane) * 4);
#pragma unroll
        for (int s = 0; s < 4; ++s) if (win0 + 64 * s + lane >= total) mq[s] = 0x7fffffffu;
        dma16x4(rs_ws, mq, 0, L_W + rw * 4096);
        if (lane == 0) *LDSP(int, L_NB + rw * 4) = (total + BQ - 1) / BQ;
        const unsigned qx = (qmod(lo16(dp[0])) - 1024u) & 0xffffu, qy = qmod(hi16(dp[0])) - 1024u, qz = (qmod(__uint_as_float(dp[1])) - 512u) & 0xffffu;
        *LDSP(u32x4, L_DPOS + rw * 1024 + lo) = (unsigned)(l31 - 1) < (unsigned)GN ? u32x4{dp[0], dp[1], qx | qy << 16, qz} : u32x4{0x7fc07fc0u, 0x7fc00000u, 0u, 0x7fff0000u};
      } else {
        __builtin_amdgcn_s_barrier();
#pragma unroll
        for (int i = 0; i < 6; ++i) dma16x4(rs_ws, v4, WS_C + (wave * 6 + i) * 4096, L_TB + (wave * 6 + i) * 4096);
        dma16(rs_ws, WS_C + (96 + wave) * 1024u + lo, L_TB + (96 + wave) * 1024);
      }
      if (lane < 8) *LDSP(unsigned, L_CNT + lane * 32 + wave * 4) = lane >> 3;
      { const unsigned z = opq(0u); *LDSP(u32x4, L_RAW + wave * LISTS + lo) = u32x4{z, z, z, z}; *LDSP(u32x4, L_LIST + wave * LISTS + lo) = u32x4{z, z, z, z}; }
      OWN4(0); OWN1();
      asm volatile("s_waitcnt vmcnt(5)" ::: "memory");
      wg_barrier();
      u32x4 r4[2];
#pragma unroll
      for (int s = 0; s < 2; ++s) r4[s] = *LDSP(u32x4, L_W + (2 * s + h) * 4096 + (l31 >> 2) * 512 + wave * 64 + (lane & 3) * 16);
      OWN4(1);
      const unsigned one = opq(1u);
#pragma unroll
      for (int s = 0; s < 2; ++s) {
        unsigned cd[4];
        u32x2 dq[4];
#pragma unroll
        for (int c = 0; c < 4; ++c) {
          cd[c] = *LDSP(u16, L_TB + ((r4[s][c] >> 2) & 0x3fffffu));
          dq[c] = *LDSP(u32x2, L_DPOS + 8 + ((r4[s][c] >> 20) & 0xff0u));
        }
        unsigned rank[4];
#pragma unroll
        for (int c = 0; c < 4; ++c) {
          const unsigned cxy = ((cd[c] & 0x3ffu) * 0x400800u) & 0xf800f800u, cz = cd[c] & 0xfc00u;
          const unsigned qxy = dq[c][0], qz = dq[c][1];
          const s16x2 dxy = __builtin_bit_cast(s16x2, qxy) - __builtin_bit_cast(s16x2, cxy);
          const s16x2 dz = __builtin_bit_cast(s16x2, qz) - __builtin_bit_cast(s16x2, cz);
          const unsigned d2 = __builtin_amdgcn_sdot2(dxy, dxy, __builtin_amdgcn_sdot2(dz, dz, 0, false), false);
          rank[c] = 0x7fffffffu;
          if (d2 < 488400000u)
            rank[c] = __hip_atomic_fetch_add(LDSP(unsigned, L_CNT + (((r4[s][c] >> 24) & 0xe0u) | wave * 4)), one, __ATOMIC_RELAXED, __HIP_MEMORY_SCOPE_WORKGROUP);
        }
        if (s == 0) OWN4(2);
#pragma unroll
        for (int c = 0; c < 4; ++c) {
          const unsigned p = rank[c] - SUBC * r1;
          if (p < (unsigned)SUBC) *LDSP(unsigned, L_RAW + (r4[s][c] >> 29) * LISTS + (p * 32 + wave * 4)) = r4[s][c];
        }
      }
    }
    wg_barrier();
    bool more1, moreb;
    FRESH_LANE;
    const unsigned LIST = L0 + L_LIST + grp * LISTS;
    int nact = 0, nstep;
    bool lighter;
    unsigned exy[4], ezd[4], ep[4];
    float ef[4];
    u32x4 own[16];
    auto weights = [&](unsigned xy, unsigned zd, float f, unsigned p) {
      const float inv = __builtin_amdgcn_rcpf(hi16(zd));
      const unsigned hxy = pk2(lo16(xy) * inv, hi16(xy) * inv);
      const float hz = rb(lo16(zd) * inv);
      const unsigned fxy = pk2(f * lo16(hxy), f * hi16(hxy));
      const unsigned fzf = pk2(f * hz, f);
      if (p != SENT) {
        const unsigned a2 = LIST + p * 2;
        *LDSP(u16, a2 + O_F) = (u16)(fzf >> 16);
        *LDSP(u16, a2 + O_FX) = (u16)fxy;
        *LDSP(u16, a2 + O_FX + 256) = (u16)(fxy >> 16);
        *LDSP(u16, a2 + O_FX + 512) = (u16)fzf;
      }
    };
    {
      unsigned rec[4], rex[4];
      u32x2 ps[4], pd[4];
#pragma unroll
      for (int i = 0; i < 4; ++i) rec[i] = *LDSP(unsigned, L_RAW + grp * LISTS + lane * 4 + i * 256);
#pragma unroll
      for (int i = 0; i < 4; ++i) rex[i] = *LDSP(unsigned, L_RAW + grp * LISTS + lane * 4 + 1024 + i * 256);
      {
        asm volatile("s_nop 4\n\tbuffer_load_dwordx2 %0, %4, %8, %9 offen\n\tbuffer_load_dwordx2 %1, %5, %8, %9 offen\n\t"
                     "buffer_load_dwordx2 %2, %6, %8, %9 offen\n\tbuffer_load_dwordx2 %3, %7, %8, %9 offen"
                     : "=&v"(ps[0]), "=&v"(ps[1]), "=&v"(ps[2]), "=&v"(ps[3])
                     : "v"(rec[0] & 0xffffffu), "v"(rec[1] & 0xffffffu), "v"(rec[2] & 0xffffffu), "v"(rec[3] & 0xffffffu), "s"(rs_ws), "s"((unsigned)WS_P) : "memory");
      }
#pragma unroll
      for (int i = 0; i < 4; ++i) pd[i] = *LDSP(u32x2, L0 + L_DPOS + ((rec[i] >> 20) & 0xff0u));
      nstep = __builtin_amdgcn_ballot_w64(rec[3] != 0) ? 4 : __builtin_amdgcn_ballot_w64(rec[2] != 0) ? 3 : 2;
      {
        const u32x4 c0 = *LDSP(u32x4, L_CNT + (lane & 7) * 32), c1 = *LDSP(u32x4, L_CNT + (lane & 7) * 32 + 16);
        const int nbv = *LDSP(int, L_NB + (lane & 3) * 4);
        const unsigned mx = max(max(max(c0[0], c0[1]), max(c0[2], c0[3])), max(max(c1[0], c1[1]), max(c1[2], c1[3])));
        more1 = __builtin_amdgcn_ballot_w64(mx > (unsigned)(SUBC * (r1 + 1))) != 0;
        moreb = __builtin_amdgcn_ballot_w64(nbv > batch + 1) != 0;
        const int key = (c0[0] + c0[1] + c0[2] + c0[3] + c1[0] + c1[1] + c1[2] + c1[3]) * 2 + (((lane & 7) >> 2) ^ 1);
        lighter = __builtin_amdgcn_readlane(key, grp) < __builtin_amdgcn_readlane(key, grp ^ 4);
      }
#pragma unroll
      for (int i = 0; i < 2; ++i) *LDSP(unsigned, LIST + (64 * i + lane) * 4) = opq(0xffffffu);
      if (lane < 32) *LDSP(unsigned, LIST + (128 + lane) * 4) = opq(0xffffffu);
      *LDSP(unsigned, LIST + O_D + lane * 4) = 0x3f803f80u;
      *LDSP(unsigned, LIST + O_DL + lane * 4) = 0xffffffffu;
      asm volatile("s_waitcnt vmcnt(4)" : "+v"(ol[0]), "+v"(ol[1]), "+v"(ol[2]), "+v"(ol[3]), "+v"(ol[4]),
                   "+v"(ol[5]), "+v"(ol[6]), "+v"(ol[7]), "+v"(ol[8]), "+v"(ol[9]), "+v"(ol[10]), "+v"(ol[11]), "+v"(ol[12])::"memory");
#pragma unroll
      for (int i = 0; i < 12; ++i) *LDSP(u32x4, TB + i * 1024 + lane * 16) = ol[i];
      if (h == 0) *LDSP(u32x4, TB + 12 * 1024 + lane * 16) = ol[12];
      {
        const unsigned ra = TB + l31 * 512 + 16 * ((4 * h) ^ (l31 & 15));
#pragma unroll
        for (int i = 0; i < 16; ++i) own[i] = *LDSP(u32x4, (ra ^ (16 * (8 * ((i >> 2) & 1) + (i & 3)))) + 256 * (i >> 3));
      }
      asm volatile("s_waitcnt vmcnt(0)" : "+v"(ps[0]), "+v"(ps[1]), "+v"(ps[2]), "+v"(ps[3])::"memory");
      auto exact = [&](unsigned rc, u32x2 a, u32x2 c, unsigned& oxy, unsigned& ozd, float& of, unsigned& op) {
        const unsigned xy = pk2(lo16(a[0]) - lo16(c[0]), hi16(a[0]) - hi16(c[0]));
        const float z = rb(__uint_as_float(a[1]) - __uint_as_float(c[1]));
        const float x = lo16(xy), y = hi16(xy);
        const unsigned sq = pk2(x * x, y * y);
        const float q2 = rb(rb(lo16(sq) + hi16(sq) + rb(z * z)) + 1e-8f);
        const unsigned zd = pk2(z, __builtin_amdgcn_sqrtf(q2));
        const float d = hi16(zd);
        const float f = 0.5f * rb(rb(__builtin_amdgcn_cosf(rb(rb(3.141592653589793f * d) * 0.2f) * 0.15915494309189535f)) + 1.0f);
        const bool ok = (d < 5.0f) & (f != 0.0f);
        const uint64_t bm = __builtin_amdgcn_ballot_w64(ok);
        const unsigned p = __builtin_amdgcn_mbcnt_hi((unsigned)(bm >> 32), __builtin_amdgcn_mbcnt_lo((unsigned)bm, nact - CAP * r2));
        nact += __builtin_popcountll(bm);
        oxy = xy; ozd = zd; of = f; op = SENT;
        if (ok & (p < (unsigned)CAP)) {
          op = p;
          *LDSP(unsigned, LIST + swap23(p) * 4) = rc & 0xffffffu;
          *LDSP(u16, LIST + O_D + swap23(p) * 2) = (u16)(zd >> 16);
          *LDSP(u16, LIST + O_DL + p * 2) = (u16)(rc >> 24);
        }
      };
#pragma unroll
      for (int i = 0; i < 4; ++i) {
        exy[i] = 0; ezd[i] = 0; ef[i] = 0.f; ep[i] = SENT;
        if (i < nstep) exact(rec[i], ps[i], pd[i], exy[i], ezd[i], ef[i], ep[i]);
      }
#pragma nounroll
      while (__builtin_amdgcn_ballot_w64(rex[0] != 0)) {
        u32x2 a;
        asm volatile("s_nop 4\n\tbuffer_load_dwordx2 %0, %1, %2, %3 offen\n\ts_waitcnt vmcnt(0)" : "=&v"(a) : "v"(rex[0] & 0xffffffu), "s"(rs_ws), "s"((unsigned)WS_P) : "memory");
        unsigned xy, zd, p; float f;
        exact(rex[0], a, *LDSP(u32x2, L_DPOS + ((rex[0] >> 20) & 0xff0u)), xy, zd, f, p);
        weights(xy, zd, f, p);
        rex[0] = rex[1]; rex[1] = rex[2]; rex[2] = rex[3]; rex[3] = 0;
      }
    }
    const int cnt = __builtin_amdgcn_readfirstlane(max(min(nact - CAP * r2, CAP), 0));
    if (lane == 0) *LDSP(int, L_NB + 32 + wave * 4) = nact > CAP * (r2 + 1);
    const int ntile = (cnt + 31) >> 5;
    auto issue_h2 = [&](unsigned off) {
      asm volatile("s_nop 4\n\tbuffer_load_dwordx4 %0, %4, %5, %6 offen\n\tbuffer_load_dwordx4 %1, %4, %5, %6 offen offset:16\n\t"
                   "buffer_load_dwordx4 %2, %4, %5, %6 offen offset:32\n\tbuffer_load_dwordx4 %3, %4, %5, %6 offen offset:48"
                   : "=&v"(hN[0]), "=&v"(hN[1]), "=&v"(hN[2]), "=&v"(hN[3]) : "v"(off), "s"(rs_ws), "s"((unsigned)WS_H) : "memory");
    };
    auto issue_hc = [&](unsigned off, int nxt) {
      asm volatile("s_cmp_eq_u32 %7, 0\n\ts_cbranch_scc1 2f\n\ts_nop 2\n\t"
                   "buffer_load_dwordx4 %0, %4, %5, %6 offen\n\tbuffer_load_dwordx4 %1, %4, %5, %6 offen offset:16\n\t"
                   "buffer_load_dwordx4 %2, %4, %5, %6 offen offset:32\n\tbuffer_load_dwordx4 %3, %4, %5, %6 offen offset:48\n2:"
                   : "=&v"(hN[0]), "=&v"(hN[1]), "=&v"(hN[2]), "=&v"(hN[3]) : "v"(off), "s"(rs_ws), "s"((unsigned)WS_H), "s"(nxt) : "memory", "scc");
    };
    auto issue_h = [&](int t) { FRESH_LANE; issue_h2(*LDSP(unsigned, LIST + (32 * t + l31) * 4) * 16u + 64u * h); };
    auto wait_h = [&]() {
      asm volatile("s_waitcnt vmcnt(12)" : "+v"(hN[0]), "+v"(hN[1]), "+v"(hN[2]), "+v"(hN[3])::"memory");
    };
    unsigned vo[4];
    auto prep_v = [&](int t) {
      FRESH_LANE;
      const int drow = lane >> 3;
      const unsigned dchunk = 16u * (((lane & 7) ^ (drow & 2)) + 8);
#pragma unroll
      for (int i = 0; i < 4; ++i) vo[i] = *LDSP(unsigned, LIST + (32 * t + 8 * i + drow) * 4) * 64u + dchunk;
    };
    auto issue_v = [&](int k) {
      asm volatile("s_waitcnt lgkmcnt(0)" ::: "memory");
      dma16x4(rs_sv, vo, 128 * k, TB + 4096 * k);
    };
    auto issue_vc = [&](int k, int nxt) {
      asm volatile("s_cmp_eq_u32 %7, 0\n\ts_cbranch_scc1 1f\n\ts_waitcnt lgkmcnt(0)\n\t"
                   "s_mov_b32 m0, %6\n\ts_nop 1\n\tbuffer_load_dwordx4 %0, %4, %5 offen lds\n\t"
                   "s_add_u32 m0, m0, 0x400\n\ts_nop 0\n\tbuffer_load_dwordx4 %1, %4, %5 offen lds\n\t"
                   "s_add_u32 m0, m0, 0x400\n\ts_nop 0\n\tbuffer_load_dwordx4 %2, %4, %5 offen lds\n\t"
                   "s_add_u32 m0, m0, 0x400\n\ts_nop 0\n\tbuffer_load_dwordx4 %3, %4, %5 offen lds\n1:"
                   ::"v"(vo[0]), "v"(vo[1]), "v"(vo[2]), "v"(vo[3]), "s"(rs_sv), "s"(128 * k), "s"(TB + 4096 * k), "s"(nxt) : "memory", "scc");
    };
    auto late_code = [&](int nxt) {
      FRESH_LANE;
      uint64_t pc; unsigned len;
      asm volatile("s_getpc_b64 %0\npainn_late_lo:\n\ts_mov_b32 %1, painn_code_hi-painn_late_lo" : "=s"(pc), "=s"(len));
      const u32x4 rc = {(unsigned)pc, (unsigned)(pc >> 32) & 0xffffu, (wave & 3) == 0 ? len : 0u, 0x00020000u};
      asm volatile("s_cmp_eq_u32 %3, 0\n\ts_cbranch_scc1 1f\n\ts_mov_b32 m0, %2\n\ts_nop 3\n\tbuffer_load_dword %0, %1, 0 offen lds\n1:"
                   ::"v"(lane * 128), "s"(rc), "s"(TB + 12288), "s"(nxt) : "memory", "scc");
    };
    u32x4 rbf;
    auto calc_rbf = [&](int t) {
      FRESH_LANE;
      const float d = lo16(*LDSP(u16, LIST + O_D + 64 * t + 2 * l31));
      const f32x4 f0 = *LDSP(f32x4, L_FR + 32 * h), f1 = *LDSP(f32x4, L_FR + 32 * h + 16);
      const float inv = __builtin_amdgcn_rcpf(d);
#pragma unroll
      for (int i = 0; i < 4; ++i) {
        const float fa = i < 2 ? f0[2 * i] : f1[2 * i - 4], fb = i < 2 ? f0[2 * i + 1] : f1[2 * i - 3];
        const unsigned x = pk2(d * fa, d * fb);
        float ra = lo16(x) * 0.15915494309189535f, rc = hi16(x) * 0.15915494309189535f;
        ra -= __builtin_rintf(ra); rc -= __builtin_rintf(rc);
        rbf[i] = pk2(__builtin_amdgcn_sinf(ra) * inv, __builtin_amdgcn_sinf(rc) * inv);
      }
      asm volatile("" : "+v"(rbf));
    };
    setprio(3);
    issue_h(0);
    prep_v(0);
    {
      const unsigned lo = 16u * lane;
      const unsigned v4[4] = {lo, lo + 1024u, lo + 2048u, lo + 3072u};
      const int wo = min(wave * 4096, NFRAG * 1024 + 64 - 4096);
      dma16x4(rs_ws, v4, wo, L_W + wo);
      wg_barrier();
    }
    const bool late = lighter;
    if (!late) {
      issue_v(0); issue_v(1); issue_v(2);
      asm volatile("s_waitcnt vmcnt(12)\n\ts_barrier" ::: "memory");
    }
    setprio(min(ntile - 1, 2));
    SB();
#pragma unroll
    for (int i = 0; i < 4; ++i) if (i < nstep) weights(exy[i], ezd[i], ef[i], ep[i]);
    if (first) {
#pragma unroll
      for (int i = 0; i < 16; ++i)
#pragma unroll
        for (int k = 0; k < 4; ++k) {
          acc[i >> 2][(i >> 1) & 1][8 * (i & 1) + 2 * k] = lo16(own[i][k]);
          acc[i >> 2][(i >> 1) & 1][8 * (i & 1) + 2 * k + 1] = hi16(own[i][k]);
        }
      first = false;
    }
    SB();
    if (late) {
      asm volatile("s_waitcnt vmcnt(0)\n\ts_barrier" ::: "memory");
      issue_v(0); issue_v(1); issue_v(2);
    }
    const bool more2 = __builtin_amdgcn_ballot_w64(*LDSP(int, L_NB + 32 + (lane & 7) * 4) != 0) != 0;
    calc_rbf(0);
    wait_h();
    auto tile = [&](int t, auto nsc) {
      constexpr int NS = decltype(nsc)::value;
      constexpr bool NX = NS == 2;
      const int nxt = ntile - 1 - t;
      FRESH_LANE;
      const unsigned WL = L0 + L_W + lane * 16, la = LIST + 64 * t + 16 * h;
      const unsigned mypair = (unsigned)(grp * 32 + l31 + 1) * 0x10001u;
      u32x4 Wb[5];
      auto ldW = [&](int ct) {
        Wb[4] = *LDSP(u32x4, WL + (24 + ct) * 1024);
#pragma unroll
        for (int ks = 0; ks < 4; ++ks) Wb[ks] = *LDSP(u32x4, WL + (4 * ct + ks) * 1024);
      };
      ldW(4);
      u32x4 msk[2], S1[2], sr[3][2];
#pragma unroll
      for (int s = 0; s < NS; ++s) msk[s] = *LDSP(u32x4, la + O_DL + 32 * s);
#pragma unroll
      for (int s = 0; s < NS; ++s)
#pragma unroll
        for (int k = 0; k < 3; ++k) sr[k][s] = *LDSP(u32x4, la + O_FX + 256 * k + 32 * s);
      SB();
      f32x16 ph, w;
      auto gemm = [&]() {
        w = mfma(rbf, Wb[4], zero16());
        ph = mfma(hN[0], Wb[0], zero16());
#pragma unroll
        for (int ks = 1; ks < 4; ++ks) ph = mfma(hN[ks], Wb[ks], ph);
      };
#define PIN1(a) asm volatile("" : "+v"(a))
#define PIN2(a, b) do { PIN1(a); PIN1(b); } while (0)
#define PIN3(a, b, c) asm volatile("" : "+v"(a), "+v"(b), "+v"(c))
      auto pack = [&](u32x4 (&a)[2]) {
        const f32x16 m = ph * w;
#pragma unroll
        for (int s = 0; s < NS; ++s) { a[s] = pack8(m, s); PIN1(a[s]); }
      };
#define MIX(id, lead, n, v) do { __builtin_amdgcn_sched_group_barrier(0x8, lead, id); _Pragma("unroll") for (int i_ = 0; i_ < n; ++i_) { \
        __builtin_amdgcn_sched_group_barrier(0x402, v, id); __builtin_amdgcn_sched_group_barrier(0x8, 1, id); } } while (0)
      u32x4 aa[2], ab[2];
      gemm();
#pragma unroll
      for (int s = 0; s < NS; ++s) {
        msk[s] = eqmask4(msk[s], mypair, 0x00010001u);
#pragma unroll
        for (int k = 0; k < 3; ++k) sr[k][s] &= msk[s];
      }
      MIX(0, 1, 4, 4 * NS);
      PIN2(ph, w);
      SB();
      ldW(5);
      SB();
      pack(aa);
      SB();
      gemm();
      PIN2(ph, w);
      SB();
      ldW(0);
      PIN2(aa[0], aa[NS - 1]);
      SB();
#pragma unroll
      for (int s = 0; s < NS; ++s)
#pragma unroll
        for (int k = 0; k < 3; ++k) acc[1 + k][0] = mfma(aa[s], sr[k][s], acc[1 + k][0]);
      pack(ab);
      MIX(1, 2, 3 * NS - 2, 6);
      PIN3(acc[1][0], acc[2][0], acc[3][0]);
      SB();
      gemm();
      PIN2(ph, w);
      SB();
      ldW(1);
      PIN2(ab[0], ab[NS - 1]);
      SB();
#pragma unroll
      for (int s = 0; s < NS; ++s)
#pragma unroll
        for (int k = 0; k < 3; ++k) acc[1 + k][1] = mfma(ab[s], sr[k][s], acc[1 + k][1]);
      pack(aa);
      MIX(2, 2, 3 * NS - 2, 6);
      PIN3(acc[1][1], acc[2][1], acc[3][1]);
      SB();
      unsigned tra, se, dn;
      f32x4 f0, f1;
      u32x4 rbn;
      {
        FRESH_LANE;
        const unsigned la2 = LIST + 64 * t + 16 * h, mp2 = (unsigned)(grp * 32 + l31 + 1) * 0x10001u;
#pragma unroll
        for (int s = 0; s < NS; ++s) { msk[s] = *LDSP(u32x4, la2 + O_DL + 32 * s); S1[s] = *LDSP(u32x4, la2 + O_F + 32 * s); }
        if (NX) {
          dn = *LDSP(u16, LIST + O_D + 64 * (t + 1) + 2 * l31);
          f0 = *LDSP(f32x4, L_FR + 32 * h); f1 = *LDSP(f32x4, L_FR + 32 * h + 16);
          se = *LDSP(unsigned, LIST + (32 * (t + 1) + l31) * 4);
#pragma unroll
          for (int i = 0; i < 4; ++i) vo[i] = *LDSP(unsigned, LIST + (32 * (t + 1) + 8 * i + (lane >> 3)) * 4);
        }
        SB();
        gemm();
#pragma unroll
        for (int s = 0; s < NS; ++s) { S1[s] &= eqmask4(msk[s], mp2, 0x00010001u); PIN1(S1[s]); }
        const unsigned dchunk = 16u * (((lane & 7) ^ ((lane >> 3) & 2)) + 8);
        if (NX) {
#pragma unroll
          for (int i = 0; i < 4; ++i) { vo[i] = vo[i] * 64u + dchunk; PIN1(vo[i]); }
          se = se * 16u + 64u * h; PIN1(se);
        }
        const int q4 = (lane >> 2) & 3, pp = lane & 3;
        tra = (q4 & 1) ? TB + (4 * h + (q4 >> 1)) * 128 + 64 * (pp & 1) + (lane & 16) + 8 * (pp >> 1) : 0x100000u;
        PIN1(tra);
        MIX(3, 2, 3, 8);
        PIN2(ph, w);
        SB();
      }
      ldW(2);
      PIN2(aa[0], aa[NS - 1]);
      SB();
#pragma unroll
      for (int s = 0; s < NS; ++s) acc[0][0] = mfma(aa[s], S1[s], acc[0][0]);
      PIN3(acc[0][0], ph, w);
      SB();
      pack(ab);
      SB();
      const float dnf = NX ? lo16(dn) : 1.0f, inv = __builtin_amdgcn_rcpf(dnf);
      auto rbf2 = [&](float fa, float fb) {
        const unsigned x = pk2(dnf * fa, dnf * fb);
        float ra = lo16(x) * 0.15915494309189535f, rc = hi16(x) * 0.15915494309189535f;
        ra -= __builtin_rintf(ra); rc -= __builtin_rintf(rc);
        return pk2(__builtin_amdgcn_sinf(ra) * inv, __builtin_amdgcn_sinf(rc) * inv);
      };
      gemm();
      if (NX) { unsigned r0 = rbf2(f0[0], f0[1]), r1_ = rbf2(f0[2], f0[3]); PIN2(r0, r1_); rbn[0] = r0; rbn[1] = r1_; }
      MIX(4, 1, 4, 6);
      PIN2(ph, w);
      SB();
      ldW(3);
      PIN2(ab[0], ab[NS - 1]);
      SB();
#pragma unroll
      for (int s = 0; s < NS; ++s) acc[0][1] = mfma(ab[s], S1[s], acc[0][1]);
      PIN3(acc[0][1], ph, w);
      SB();
      f32x16 mv[2];
      auto prod = [&](f32x16& m) {
#pragma unroll
        for (int r = 0; r < 8 * NS; ++r) { float x = ph[r] * w[r]; PIN1(x); m[r] = x; }
      };
      prod(mv[0]);
      SB();
      gemm();
      if (NX) { unsigned r0 = rbf2(f1[0], f1[1]), r1_ = rbf2(f1[2], f1[3]); PIN2(r0, r1_); rbn[2] = r0; rbn[3] = r1_; }
      MIX(5, 1, 4, 6);
      PIN2(ph, w);
      SB();
      if (NX) { issue_hc(se, nxt); rbf = rbn; }
      prod(mv[1]);
      SB();
      __builtin_amdgcn_s_setprio(3);
      if (NX) asm volatile("s_cmp_lg_u32 %0, 0\n\ts_cbranch_scc1 1f\n\ts_waitcnt vmcnt(0)\n1:\n\ts_waitcnt vmcnt(4)" ::"s"(nxt) : "memory", "scc");
      else asm volatile("s_waitcnt vmcnt(0)" ::: "memory");
      u32x2 va[8], vb[8];
      auto ldv = [&](u32x2 (&v)[8], int t2, int k) {
#pragma unroll
        for (int i = 0; i < 4 * NS; ++i)
          v[i] = trread(tra + k * 4096 + (8 * (i >> 1) + 2 * (i & 1)) * 128 + 32 * (t2 ^ (i & 1)));
      };
      auto vv = [&](const u32x2 (&v)[8], const f32x16& m, f32x16& ac) {
        u32x4 a[2];
#pragma unroll
        for (int i = 0; i < 4 * NS; ++i) {
          const unsigned v0 = v[i][0], v1 = v[i][1];
          a[i >> 2][i & 3] = pk2(m[2 * i] * __uint_as_float(v0), m[2 * i + 1] * __uint_as_float(v1));
        }
#pragma unroll
        for (int s = 0; s < NS; ++s) ac = mfma(a[s], S1[s], ac);
      };
      ldv(va, 0, 0); ldv(vb, 1, 0);
      SB();
      vv(va, mv[0], acc[1][0]);
      SB();
      if (NX) issue_vc(0, nxt);
      ldv(va, 0, 1);
      SB();
      vv(vb, mv[1], acc[1][1]);
      SB();
      ldv(vb, 1, 1);
      SB();
      vv(va, mv[0], acc[2][0]);
      SB();
      if (NX) issue_vc(1, nxt);
      ldv(va, 0, 2);
      SB();
      vv(vb, mv[1], acc[2][1]);
      SB();
      ldv(vb, 1, 2);
      SB();
      vv(va, mv[0], acc[3][0]);
      SB();
      if (NX) { issue_vc(2, nxt); late_code(nxt); }
      vv(vb, mv[1], acc[3][1]);
      SB();
      if (NX) asm volatile("s_waitcnt vmcnt(13)" : "+v"(hN[0]), "+v"(hN[1]), "+v"(hN[2]), "+v"(hN[3])::"memory");
    };
    const int nfull = (cnt + 15) >> 5;
    for (int t = 0; t < nfull; ++t) { tile(t, std::integral_constant<int, 2>{}); setprio(min(ntile - 2 - t, 2)); }
    if (nfull < ntile) tile(nfull, std::integral_constant<int, 1>{});
    if (more2) ++r2;
    else if (more1) { ++r1; r2 = 0; }
    else if (moreb) { ++batch; r1 = 0; r2 = 0; }
    else break;
    asm volatile("s_waitcnt vmcnt(0)" ::: "memory");
    wg_barrier();
  }
  {
    FRESH_LANE;
    u32x4 o[16];
#pragma unroll
    for (int i = 0; i < 16; ++i) o[i] = pack8(acc[i >> 2][(i >> 1) & 1], i & 1);
    asm volatile("s_waitcnt vmcnt(0)" ::: "memory");
    if (l31 < GN) {
      const unsigned wa = TB + l31 * 512 + 16 * ((4 * h) ^ (l31 & 7));
#pragma unroll
      for (int i = 0; i < 16; ++i) *LDSP(u32x4, (wa ^ (16 * (i & 3))) + 128 * (i >> 2)) = o[i];
    }
    const int gw = __builtin_amdgcn_readfirstlane(max(min(nb - grp * GN, GN), 0));
    const uint64_t op = (uint64_t)out + (size_t)(node0 + grp * GN) * 512;
    const __amdgpu_buffer_rsrc_t rb_o = __builtin_amdgcn_make_buffer_rsrc((void*)op, 0, gw * 512, 0x00020000);
    u32x4 v[(GN + 1) / 2];
#pragma unroll
    for (int i = 0; i < (GN + 1) / 2; ++i) v[i] = *LDSP(u32x4, TB + i * 1024 + lane * 16);
    const unsigned oa = h * 512 + 16 * (l31 ^ h);
#pragma unroll
    for (int i = 0; i < (GN + 1) / 2; ++i) __builtin_amdgcn_raw_buffer_store_b128(v[i], rb_o, oa ^ (32 * (i & 3)), i * 1024, WT);
  }
  asm volatile("painn_code_hi:");
}

extern "C" __global__ __launch_bounds__(192) void k_gen_phi(const u16* __restrict__ sv, const u16* __restrict__ w1, const u16* __restrict__ w2,
                                                            float* __restrict__ phi) {
  __shared__ float s[64], hd[64];
  const int t = TID; const size_t n = BID;
  if (t < 64) s[t] = lo16(sv[n * 256 + t]);
  wg_barrier();
  if (t < 64) {
    float a = 0.f;
    for (int k = 0; k < 64; ++k) a += s[k] * lo16(w1[k * 64 + t]);
    hd[t] = rb(a * __builtin_amdgcn_rcpf(1.0f + __builtin_amdgcn_exp2f(a * -1.4426950408889634f)));
  }
  wg_barrier();
  float a = 0.f;
  for (int k = 0; k < 64; ++k) a += hd[k] * lo16(w2[k * 192 + t]);
  phi[n * 192 + t] = a;
}
extern "C" __global__ __launch_bounds__(256) void k_gen_edge(const u16* __restrict__ sv, const u16* __restrict__ pos, const int* __restrict__ src,
                                                             const int* __restrict__ dst, const u16* __restrict__ freqs, const u16* __restrict__ wr,
                                                             const float* __restrict__ phi, float* __restrict__ acc, int E) {
  const size_t e = (size_t)BID * 4 + (TID >> 6);
  const int c = TID & 63;
  if (e >= (size_t)E) return;
  const size_t s = src[e], t = dst[e];
  const float x = rb(lo16(pos[s * 3]) - lo16(pos[t * 3])), y = rb(lo16(pos[s * 3 + 1]) - lo16(pos[t * 3 + 1])), z = rb(lo16(pos[s * 3 + 2]) - lo16(pos[t * 3 + 2]));
  const float d = rb(__builtin_amdgcn_sqrtf(rb(rb(rb(x * x) + rb(y * y) + rb(z * z)) + 1e-8f)));
  if (!(d < 5.0f)) return;
  const float inv = __builtin_amdgcn_rcpf(d);
  const float f = 0.5f * rb(rb(__builtin_amdgcn_cosf(rb(rb(3.141592653589793f * d) * 0.2f) * 0.15915494309189535f)) + 1.0f);
  float w0 = 0.f, w1 = 0.f, w2 = 0.f;
  for (int k = 0; k < 16; ++k) {
    float r = rb(d * lo16(freqs[k])) * 0.15915494309189535f;
    r -= __builtin_rintf(r);
    const float b = rb(__builtin_amdgcn_sinf(r) * inv);
    w0 += b * lo16(wr[k * 192 + c]); w1 += b * lo16(wr[k * 192 + 64 + c]); w2 += b * lo16(wr[k * 192 + 128 + c]);
  }
  const float ms = phi[s * 192 + c] * w0 * f, mv = phi[s * 192 + 64 + c] * w1 * f, mr = phi[s * 192 + 128 + c] * w2 * f;
  float* a = acc + t * 256 + c;
  __hip_atomic_fetch_add(a, ms, __ATOMIC_RELAXED, __HIP_MEMORY_SCOPE_AGENT);
  __hip_atomic_fetch_add(a + 64, mv * lo16(sv[s * 256 + 64 + c]) + mr * rb(x * inv), __ATOMIC_RELAXED, __HIP_MEMORY_SCOPE_AGENT);
  __hip_atomic_fetch_add(a + 128, mv * lo16(sv[s * 256 + 128 + c]) + mr * rb(y * inv), __ATOMIC_RELAXED, __HIP_MEMORY_SCOPE_AGENT);
  __hip_atomic_fetch_add(a + 192, mv * lo16(sv[s * 256 + 192 + c]) + mr * rb(z * inv), __ATOMIC_RELAXED, __HIP_MEMORY_SCOPE_AGENT);
}
extern "C" __global__ __launch_bounds__(256) void k_gen_out(const u16* __restrict__ sv, const float* __restrict__ acc, u16* __restrict__ out) {
  const size_t i = (size_t)BID * 256 + TID;
  out[i] = (u16)pk2(lo16(sv[i]) + acc[i], 0.f);
}

#if defined(__HIP_DEVICE_COMPILE__)
#pragma clang attribute pop
#endif
extern "C" void kernel_launch(void* const* d_in, const int* in_sizes, int n_in, void* d_out, int out_size, void* d_ws, size_t ws_size,
                              hipStream_t stream) {
  const u16* sv = (const u16*)d_in[0];
  const u16* pos = (const u16*)d_in[1];
  const int* src = (const int*)d_in[2];
  const int* dst = (const int*)d_in[3];
  const u16* fr = (const u16*)d_in[4];
  const u16* w1 = (const u16*)d_in[5];
  const u16* w2 = (const u16*)d_in[6];
  const u16* wr = (const u16*)d_in[7];
  const int N = in_sizes[1] / 3, E = in_sizes[2];
  char* ws = (char*)d_ws;
  if (N <= 0) return;
  if (N > 51200 || E > 256 * CH) {
    float* acc = (float*)ws;
    float* phi = acc + (size_t)N * 256;
    (void)hipMemsetAsync(acc, 0, (size_t)N * 1024, stream);
    k_gen_phi<<<N, 192, 0, stream>>>(sv, w1, w2, phi);
    if (E > 0) k_gen_edge<<<(E + 3) / 4, 256, 0, stream>>>(sv, pos, src, dst, fr, wr, phi, acc, E);
    k_gen_out<<<N, 256, 0, stream>>>(sv, acc, (u16*)d_out);
    return;
  }
  const int BN = (N + 255) / 256 < 2 ? 2 : (N + 255) / 256;
  const int B = (N + BN - 1) / BN, bpx = (B + 7) / 8;
  int chunk = (((E + 195) / 196) + 3) & ~3;
  chunk = chunk < 1024 ? 1024 : chunk > CH ? CH : chunk;
  const int W = (E + chunk - 1) / chunk;
  const int HW = 200, WP = (W + 7) & ~7;
  const unsigned magicBN = (unsigned)(0x100000000ull / (unsigned)BN) + 1u;
  (void)hipFuncSetAttribute((const void*)k_agg, hipFuncAttributeMaxDynamicSharedMemorySize, L_END);
  k_bin<<<WP + HW + 8, 512, 0, stream>>>(src, dst, sv, pos, w1, ws, (unsigned)N | (unsigned)BN << 16 | (unsigned)bpx << 24, E, magicBN,
                                         (unsigned)chunk | (unsigned)W << 16, w2, wr, fr);
  k_agg<<<8 * bpx, 512, L_END, stream>>>(sv, ws, (u16*)d_out, N, W, BN, B, bpx);
}
```
